```python
import torch

def _softmax(x, axis=-1):
    return torch.softmax(x, dim=axis)
def _gelu(x):
    return torch.nn.functional.gelu(x, approximate="tanh")
def _silu(x):
    return torch.nn.functional.silu(x)
def _sigmoid(x):
    return torch.sigmoid(x)
def _layernorm(x, axis=-1, eps=1e-5):
    m = x.mean(dim=axis, keepdim=True)
    v = x.var(dim=axis, keepdim=True, unbiased=False)
    return (x - m) / torch.sqrt(v + eps)
def _scatter_add(tgt, idx, src, axis=0):
    return tgt.clone().index_add_(axis, idx, src)
def _segment_sum(src, seg, n):
    out = torch.zeros((n,) + src.shape[1:], dtype=src.dtype)
    return out.index_add_(0, seg, src)
def _pad(x, pw):
    flat = tuple(v for pair in reversed(pw) for v in pair)
    return torch.nn.functional.pad(x, flat)

kernel_name = 'concat_heads__softmax_full_mha__mha_block__prompt'

def setup_inputs():

    _w = (lambda a: (lambda t: t.to(torch.bfloat16) if t.is_floating_point() else t)(torch.as_tensor(a)))
    def _A_setup():
        B, S, D = 1, 2048, 1024
        return {
            "x": _w((lambda *s: torch.randn(*s, dtype=torch.bfloat16) * 0.2)(B, S, D)),
            "wqkv": _w((lambda *s: torch.randn(*s, dtype=torch.bfloat16) * 0.2)(D, 3 * D)),
            "wo": _w((lambda *s: torch.randn(*s, dtype=torch.bfloat16) * 0.05)(D, D)),
        }
    _A_in = _A_setup()

    def _B_setup():
        B, S, D, H = 1, 2048, 1024, 32
        return {
            "x": _w((lambda *s: torch.randn(*s, dtype=torch.bfloat16) * 0.2)(B, S, D)),
            "wqkv": _w((lambda *s: torch.randn(*s, dtype=torch.bfloat16) * 0.2)(D, 3 * D)),
            "wo": _w((lambda *s: torch.randn(*s, dtype=torch.bfloat16) * 0.05)(D, D)),
        }
    _B_in = _B_setup()

    return {
        "A_x": _A_in["x"],
        "A_wqkv": _A_in["wqkv"],
        "A_wo": _A_in["wo"],
        "B_x": _B_in["x"],
        "B_wqkv": _B_in["wqkv"],
        "B_wo": _B_in["wo"],
    }

def reference(A_x, A_wqkv, A_wo, B_x, B_wqkv, B_wo):
    def _A_ref(x, wqkv, wo):
        B, S, D = x.shape
        H = 32; Dh = D // H
        qkv = (lambda x, *d: x.permute(*d))((x @ wqkv).reshape(B, S, 3, H, Dh), 2, 0, 3, 1, 4)
        q, k, v = qkv[0], qkv[1], qkv[2]
        scale = Dh ** -0.5
        scores = torch.einsum("bhqd,bhkd->bhqk", q, k) * scale
        p = _softmax(scores, axis=-1)
        o = torch.einsum("bhqk,bhkd->bhqd", p, v)
        return (lambda x, *d: x.permute(*d))(o, 0, 2, 1, 3).reshape(B, S, D) @ wo

    def _B_ref(x, wqkv, wo):
        B, S, D = x.shape
        H = 32
        Dh = D // H
        qkv = (lambda x, *d: x.permute(*d))((x @ wqkv).reshape(B, S, 3, H, Dh), 2, 0, 3, 1, 4)
        q, k, v = qkv[0], qkv[1], qkv[2]
        scores = torch.einsum("bhqd,bhkd->bhqk", q, k) * (Dh ** -0.5)
        p = _softmax(scores, axis=-1)
        o = torch.einsum("bhqk,bhkd->bhqd", p, v)
        return (lambda x, *d: x.permute(*d))(o, 0, 2, 1, 3).reshape(B, S, D) @ wo

    a = _A_ref(A_x, A_wqkv, A_wo)
    b = _B_ref(B_x, B_wqkv, B_wo)
    return torch.concatenate([a, b], axis=-1)
```

```cpp
#include <hip/hip_runtime.h>
#include <hip/hip_bf16.h>
typedef unsigned short u16;
__device__ __forceinline__ float bf2f(u16 v){ return __uint_as_float(((unsigned)v)<<16); }
__device__ __forceinline__ u16 f2bf(float f){ unsigned u=__float_as_uint(f); u += 0x7fffu + ((u>>16)&1u); return (u16)(u>>16); }
__global__ void gemm_naive(const u16* A, const u16* B, u16* C, int M, int N, int K, int ldc) {
  __shared__ float As[16][65], Bs[16][65];
  int tx = threadIdx.x & 15, ty = threadIdx.x >> 4;
  int bm = blockIdx.y*64, bn = blockIdx.x*64;
  float acc[4][4] = {};
  for (int k0=0;k0<K;k0+=16){
    for (int i=threadIdx.x;i<64*16;i+=256){ int r=i>>4,c=i&15; As[c][r]=bf2f(A[(size_t)(bm+r)*K+k0+c]); }
    for (int i=threadIdx.x;i<64*16;i+=256){ int r=i>>6,c=i&63; Bs[r][c]=bf2f(B[(size_t)(k0+r)*N+bn+c]); }
    __syncthreads();
    for (int k=0;k<16;++k){
      float a[4],b[4];
      for(int i=0;i<4;++i){a[i]=As[k][ty*4+i]; b[i]=Bs[k][tx*4+i];}
      for(int i=0;i<4;++i)for(int j=0;j<4;++j)acc[i][j]+=a[i]*b[j];
    }
    __syncthreads();
  }
  for(int i=0;i<4;++i)for(int j=0;j<4;++j) C[(size_t)(bm+ty*4+i)*ldc+bn+tx*4+j]=f2bf(acc[i][j]);
}
__global__ void attn_naive(const u16* qkv, u16* o) {
  int h = blockIdx.y; int q = blockIdx.x*64+threadIdx.x;
  float qv[32], acc[32];
  for(int d=0;d<32;++d){ qv[d]=bf2f(qkv[(size_t)q*3072+h*32+d])*0.17677669529663687f; acc[d]=0; }
  float m=-1e30f,l=0;
  __shared__ float Ks[32][32], Vs[32][32];
  for(int j0=0;j0<2048;j0+=32){
    __syncthreads();
    for(int i=threadIdx.x;i<1024;i+=64){int r=i>>5,c=i&31; Ks[r][c]=bf2f(qkv[(size_t)(j0+r)*3072+1024+h*32+c]); Vs[r][c]=bf2f(qkv[(size_t)(j0+r)*3072+2048+h*32+c]);}
    __syncthreads();
    for(int j=0;j<32;++j){
      float s=0; for(int d=0;d<32;++d) s+=qv[d]*Ks[j][d];
      float mn=fmaxf(m,s); float a=__expf(m-mn), p=__expf(s-mn); l=l*a+p;
      for(int d=0;d<32;++d) acc[d]=acc[d]*a+p*Vs[j][d];
      m=mn;
    }
  }
  for(int d=0;d<32;++d) o[(size_t)q*1024+h*32+d]=f2bf(acc[d]/l);
}
extern "C" void kernel_launch(void* const* d_in, const int* in_sizes, int n_in, void* d_out, int out_size, void* d_ws, size_t ws_size, hipStream_t stream) {
  u16* ws=(u16*)d_ws;
  for (int b=0;b<2;++b){
    const u16* x=(const u16*)d_in[3*b]; const u16* wqkv=(const u16*)d_in[3*b+1]; const u16* wo=(const u16*)d_in[3*b+2];
    u16* qkv=ws+(size_t)b*(2048*3072+2048*1024); u16* o=qkv+2048*3072;
    gemm_naive<<<dim3(3072/64,2048/64),256,0,stream>>>(x,wqkv,qkv,2048,3072,1024,3072);
    attn_naive<<<dim3(2048/64,32),64,0,stream>>>(qkv,o);
    gemm_naive<<<dim3(1024/64,2048/64),256,0,stream>>>(o,wo,(u16*)d_out+b*1024,2048,1024,1024,2048);
  }
}
```

```cpp
#include <hip/hip_runtime.h>
#include <stdint.h>

typedef __bf16 bf16x8 __attribute__((ext_vector_type(8)));
typedef __bf16 bf16x2 __attribute__((ext_vector_type(2)));
typedef short s16x4 __attribute__((ext_vector_type(4)));
typedef short s16x8 __attribute__((ext_vector_type(8)));
typedef float f32x16 __attribute__((ext_vector_type(16)));
typedef float f32x4 __attribute__((ext_vector_type(4)));
typedef float f32x2 __attribute__((ext_vector_type(2)));
typedef unsigned u32x4 __attribute__((ext_vector_type(4)));
typedef unsigned u32x2 __attribute__((ext_vector_type(2)));
#define LAS __attribute__((address_space(3)))
#define DI __device__ __forceinline__
#define MFMA32(a, b, c) __builtin_amdgcn_mfma_f32_32x32x16_bf16((a), (b), (c), 0, 0, 0)
#define MFMA16(a, b, c) __builtin_amdgcn_mfma_f32_16x16x32_bf16((a), (b), (c), 0, 0, 0)
#define SBAR() __builtin_amdgcn_sched_barrier(0)
#define PIN(x) asm volatile("" : "+v"(x))
typedef __amdgpu_buffer_rsrc_t srd_t;

constexpr float QSCALE = 0.17677669529663687f * 1.4426950408889634f;
constexpr size_t WS_KV = 0;
constexpr size_t WS_Q = 16u << 20;
constexpr size_t WS_O = 24u << 20;

DI unsigned rfl(unsigned x) { return __builtin_amdgcn_readfirstlane(x); }
DI srd_t make_srd(const void* p, unsigned bytes) { return __builtin_amdgcn_make_buffer_rsrc((void*)p, (short)0, (int)bytes, 0x00020000); }
DI void dma16(unsigned ldsdst, unsigned voff, srd_t srd, unsigned soff) {
    asm volatile("s_mov_b32 m0, %0\n\ts_nop 0\n\tbuffer_load_dwordx4 %1, %2, %3 offen lds" :: "s"(ldsdst), "v"(voff), "s"(srd), "s"(soff) : "memory");
}
DI unsigned cvtpk(float lo, float hi) { f32x2 v = {lo, hi}; bf16x2 b = __builtin_convertvector(v, bf16x2); return __builtin_bit_cast(unsigned, b); }
DI bf16x8 ldsfrag(const LAS char* p) { return *(const LAS bf16x8*)p; }
typedef short v4i16_t __attribute__((ext_vector_type(4)));
DI s16x4 ldstr(const LAS char* p) { return __builtin_amdgcn_ds_read_tr16_b64_v4i16((LAS v4i16_t*)p); }
DI bf16x8 cat8(s16x4 lo, s16x4 hi) { s16x8 r = __builtin_shufflevector(lo, hi, 0, 1, 2, 3, 4, 5, 6, 7); return __builtin_bit_cast(bf16x8, r); }
DI u32x4 pack8(const f32x16& x, int s, float mul) {
    u32x4 p; p[0] = cvtpk(x[8 * s] * mul, x[8 * s + 1] * mul); p[1] = cvtpk(x[8 * s + 2] * mul, x[8 * s + 3] * mul);
    p[2] = cvtpk(x[8 * s + 4] * mul, x[8 * s + 5] * mul); p[3] = cvtpk(x[8 * s + 6] * mul, x[8 * s + 7] * mul); return p; }
DI u32x4 pack8n(const f32x16& x, int s) {
    u32x4 p; p[0] = cvtpk(x[8 * s], x[8 * s + 1]); p[1] = cvtpk(x[8 * s + 2], x[8 * s + 3]);
    p[2] = cvtpk(x[8 * s + 4], x[8 * s + 5]); p[3] = cvtpk(x[8 * s + 6], x[8 * s + 7]); return p; }
#define WAITBAR(vm) asm volatile("s_waitcnt vmcnt(" #vm ") lgkmcnt(0)\n\ts_barrier" ::: "memory")

#define STAMP_BEGIN
#define STAMP_MID(i)
#define STAMP_END(ws, kid)
constexpr int P1_XS = 32768, P1_WS = 24576, P1_WOFF = 3 * P1_XS, P1_LDS = P1_WOFF + 2 * P1_WS;
__global__ __launch_bounds__(512, 2) void k_qkv(const uint16_t* xA, const uint16_t* wA, const uint16_t* xB, const uint16_t* wB, char* ws) {
    extern __shared__ __attribute__((aligned(16))) char lds_[];
    LAS char* lds = (LAS char*)lds_;
    STAMP_BEGIN
    const int tid = threadIdx.x, lane = tid & 63, wid = rfl(tid >> 6);
    const int bid = blockIdx.x, xcd = bid & 7, j = bid >> 3;
    const int b = xcd >> 2, mb = (xcd & 1) * 4 + (j & 3), hp = ((xcd >> 1) & 1) * 8 + (j >> 2);
    const int wm = wid & 3, wn = wid >> 2;
    const srd_t sx = make_srd(b ? xB : xA, 2048 * 1024 * 2), sw = make_srd(b ? wB : wA, 1024 * 3072 * 2);
    const unsigned lds0 = (unsigned)(uintptr_t)lds_;
    const unsigned vx0 = (lane >> 3) * 2048 + (((lane & 7) ^ (lane >> 4)) << 4);
    const unsigned vx1 = (lane >> 3) * 2048 + (((lane & 7) ^ (4 + (lane >> 4))) << 4);
    const unsigned vw = (lane >> 3) * 6144 + (((lane & 7) ^ (((lane >> 4) & 1) << 2)) << 4);
    const unsigned sx0 = (mb * 256 + wid * 32) * 2048, sw0 = wid * 8 * 6144 + hp * 128;
    auto dma_x = [&](int kt) {
        const unsigned dst = lds0 + (kt % 3) * P1_XS + wid * 4096;
#pragma unroll
        for (int i = 0; i < 4; ++i) dma16(dst + i * 1024, (i & 1) ? vx1 : vx0, sx, sx0 + i * 8 * 2048 + kt * 128);
    };
    auto dma_w = [&](int kt) {
        const unsigned dst = lds0 + P1_WOFF + (kt & 1) * P1_WS + wid * 1024;
#pragma unroll
        for (int i = 0; i < 3; ++i) dma16(dst + i * 8192, vw, sw, sw0 + i * 2048 + kt * 64 * 6144);
    };
    dma_w(0); dma_x(0); dma_w(1); dma_x(1); dma_x(2);
    const int r = lane & 31, hh = lane >> 5;
    unsigned xa[4];
#pragma unroll
    for (int s = 0; s < 4; ++s) xa[s] = (wm * 64 + r) * 128 + (((2 * s + hh) ^ ((r >> 1) & 7)) << 4);
    const int g = lane >> 4, q4 = (lane >> 2) & 3, p4 = lane & 3;
    const unsigned wa = P1_WOFF + (8 * (g >> 1) + q4) * 128 + ((wn ^ (q4 >> 1)) << 6) + (g & 1) * 32 + p4 * 8;
    f32x16 acc[2][3];
#pragma unroll
    for (int a = 0; a < 2; ++a)
#pragma unroll
        for (int c = 0; c < 3; ++c)
#pragma unroll
            for (int i = 0; i < 16; ++i) acc[a][c][i] = 0.f;
    bf16x8 xf[2][2], wf[2][3];
    auto rd = [&](int buf, int kt, int s) {
        const LAS char* xp = lds + (kt % 3) * P1_XS + xa[s];
        xf[buf][0] = ldsfrag(xp); xf[buf][1] = ldsfrag(xp + 4096);
        const LAS char* wp = lds + wa + (kt & 1) * P1_WS + s * 2048;
#pragma unroll
        for (int c = 0; c < 3; ++c) wf[buf][c] = cat8(ldstr(wp + c * 8192), ldstr(wp + c * 8192 + 512));
    };
    auto mm = [&](int buf) {
#pragma unroll
        for (int a = 0; a < 2; ++a) {
            acc[a][0] = MFMA32(wf[buf][0], xf[buf][a], acc[a][0]);
            acc[a][1] = MFMA32(wf[buf][1], xf[buf][a], acc[a][1]);
            acc[a][2] = MFMA32(xf[buf][a], wf[buf][2], acc[a][2]);
        }
    };
    WAITBAR(11);
    STAMP_MID(0)
    rd(0, 0, 0);
#pragma unroll
    for (int t = 0; t < 16; ++t) {
        rd(1, t, 1); SBAR(); mm(0); SBAR();
        rd(0, t, 2); SBAR(); mm(1); SBAR();
        rd(1, t, 3); SBAR(); mm(0); SBAR();
        if (t < 15) {
            if (t < 14) WAITBAR(4); else WAITBAR(0);
            if (t + 2 < 16) dma_w(t + 2);
            if (t + 3 < 16) dma_x(t + 3);
            rd(0, t + 1, 0);
        }
        SBAR(); mm(1); SBAR();
    }
    STAMP_MID(1)
    const int h = hp * 2 + wn;
#pragma unroll
    for (int a = 0; a < 2; ++a) {
        const int t = mb * 8 + wm * 2 + a;
        char* kv = ws + WS_KV + ((size_t)(b * 32 + h) * 64 + t) * 4096 + lane * 16;
        char* qp = ws + WS_Q + ((size_t)(b * 32 + h) * 64 + t) * 2048 + lane * 16;
#pragma unroll
        for (int s = 0; s < 2; ++s) {
            *(u32x4*)(qp + s * 1024) = pack8(acc[a][0], s, QSCALE);
            *(u32x4*)(kv + s * 1024) = pack8n(acc[a][1], s);
            *(u32x4*)(kv + 2048 + s * 1024) = pack8n(acc[a][2], s);
        }
    }
    STAMP_END(ws, 0)
}

constexpr int P2_SLOT = 32768, P2_LDS = 3 * P2_SLOT + 64;
DI float ex2(float x) { return __builtin_amdgcn_exp2f(x); }
__global__ __launch_bounds__(512, 2) void k_attn(char* ws) {
    extern __shared__ __attribute__((aligned(16))) char lds_[];
    LAS char* lds = (LAS char*)lds_;
    STAMP_BEGIN
    const int tid = threadIdx.x, lane = tid & 63, wid = rfl(tid >> 6);
    const int bid = blockIdx.x, xcd = bid & 7, j = bid >> 3;
    const int b = xcd >> 2, hp = (xcd & 3) * 4 + (j >> 3), mb = j & 7;
    const int wm = wid & 3, wn = wid >> 2, h = hp * 2 + wn;
    const unsigned lds0 = (unsigned)(uintptr_t)lds_;
    const srd_t skv = make_srd(ws + WS_KV + (size_t)(b * 32 + hp * 2) * 64 * 4096, 2 * 64 * 4096);
    const unsigned vl = lane * 16;
    auto dma_chunk = [&](int c, int slot) {
        const unsigned dst = lds0 + slot * P2_SLOT + wid * 4096, so = (wn * 64 + 4 * c + wm) * 4096;
#pragma unroll
        for (int i = 0; i < 4; ++i) dma16(dst + i * 1024, vl, skv, so + i * 1024);
    };
    dma_chunk(0, 0); dma_chunk(1, 1); dma_chunk(2, 2);
    bf16x8 qf[2][2];
#pragma unroll
    for (int a = 0; a < 2; ++a)
#pragma unroll
        for (int s = 0; s < 2; ++s) qf[a][s] = *(const bf16x8*)(ws + WS_Q + ((size_t)(b * 32 + h) * 64 + mb * 8 + wm * 2 + a) * 2048 + s * 1024 + lane * 16);
    bf16x8 sel;
    { const int i16 = lane & 15, g = lane >> 4; const short v = ((i16 == 0 && !(g & 1)) || (i16 == 1 && (g & 1))) ? (short)0x3F80 : (short)0;
      s16x8 t = {v, v, v, v, v, v, v, v}; sel = __builtin_bit_cast(bf16x8, t); }
    const LAS char* kvb = lds + wn * 16384 + lane * 16;
    f32x16 negm[2], o[2], S[2];
    f32x4 lacc[2];
    bf16x8 kf[2][2], vf[2][2];
    WAITBAR(8);
    STAMP_MID(0)
    kf[0][0] = ldsfrag(kvb); kf[0][1] = ldsfrag(kvb + 1024); vf[0][0] = ldsfrag(kvb + 2048); vf[0][1] = ldsfrag(kvb + 3072);
#pragma unroll
    for (int a = 0; a < 2; ++a) {
        f32x16 z;
#pragma unroll
        for (int i = 0; i < 16; ++i) z[i] = 0.f;
        z = MFMA32(kf[0][0], qf[a][0], z); z = MFMA32(kf[0][1], qf[a][1], z);
        float m = z[0];
#pragma unroll
        for (int i = 1; i < 16; ++i) m = fmaxf(m, z[i]);
        m = fmaxf(m, __shfl_xor(m, 32));
#pragma unroll
        for (int i = 0; i < 16; ++i) { negm[a][i] = -m; o[a][i] = 0.f; }
        lacc[a] = (f32x4){0.f, 0.f, 0.f, 0.f};
    }
    S[0] = MFMA32(kf[0][0], qf[0][0], negm[0]); S[0] = MFMA32(kf[0][1], qf[0][1], S[0]);
    u32x4 P0, P1;
    P1 = (u32x4){0u, 0u, 0u, 0u};
    bf16x8 vprev1 = vf[0][1];
#define EXP4(X, i) do { X[i] = ex2(X[i]); X[i + 1] = ex2(X[i + 1]); X[i + 2] = ex2(X[i + 2]); X[i + 3] = ex2(X[i + 3]); } while (0)
#define BF(x) __builtin_bit_cast(bf16x8, x)
#define TILE(cur, nxt, rdp, LOAD) do { \
          \
        o[1] = MFMA32(vprev1, BF(P1), o[1]); lacc[1] = MFMA16(sel, BF(P1), lacc[1]); \
        EXP4(S[0], 0); P0[0] = cvtpk(S[0][0], S[0][1]); P0[1] = cvtpk(S[0][2], S[0][3]); SBAR(); \
        S[1] = MFMA32(kf[cur][0], qf[1][0], negm[1]); \
        if (LOAD) { kf[nxt][0] = ldsfrag(rdp); kf[nxt][1] = ldsfrag(rdp + 1024); } \
        EXP4(S[0], 4); P0[2] = cvtpk(S[0][4], S[0][5]); P0[3] = cvtpk(S[0][6], S[0][7]); SBAR(); \
        S[1] = MFMA32(kf[cur][1], qf[1][1], S[1]); \
        if (LOAD) { vf[nxt][0] = ldsfrag(rdp + 2048); vf[nxt][1] = ldsfrag(rdp + 3072); } \
        EXP4(S[0], 8); P1[0] = cvtpk(S[0][8], S[0][9]); P1[1] = cvtpk(S[0][10], S[0][11]); SBAR(); \
        o[0] = MFMA32(vf[cur][0], BF(P0), o[0]); lacc[0] = MFMA16(sel, BF(P0), lacc[0]); \
        EXP4(S[0], 12); P1[2] = cvtpk(S[0][12], S[0][13]); P1[3] = cvtpk(S[0][14], S[0][15]); SBAR(); \
          \
        o[0] = MFMA32(vf[cur][1], BF(P1), o[0]); lacc[0] = MFMA16(sel, BF(P1), lacc[0]); \
        EXP4(S[1], 0); P0[0] = cvtpk(S[1][0], S[1][1]); P0[1] = cvtpk(S[1][2], S[1][3]); SBAR(); \
        if (LOAD) S[0] = MFMA32(kf[nxt][0], qf[0][0], negm[0]); \
        EXP4(S[1], 4); P0[2] = cvtpk(S[1][4], S[1][5]); P0[3] = cvtpk(S[1][6], S[1][7]); SBAR(); \
        if (LOAD) S[0] = MFMA32(kf[nxt][1], qf[0][1], S[0]); \
        EXP4(S[1], 8); P1[0] = cvtpk(S[1][8], S[1][9]); P1[1] = cvtpk(S[1][10], S[1][11]); SBAR(); \
        o[1] = MFMA32(vf[cur][0], BF(P0), o[1]); lacc[1] = MFMA16(sel, BF(P0), lacc[1]); \
        EXP4(S[1], 12); P1[2] = cvtpk(S[1][12], S[1][13]); P1[3] = cvtpk(S[1][14], S[1][15]); SBAR(); \
        vprev1 = vf[cur][1]; \
    } while (0)
    int slot = 0;
    for (int c = 0; c < 16; ++c) {
        const LAS char* cp = kvb + slot * P2_SLOT;
        const int nslot = slot == 2 ? 0 : slot + 1;
        TILE(0, 1, cp + 4096, true);
        TILE(1, 0, cp + 8192, true);
        TILE(0, 1, cp + 12288, true);
        if (c < 15) {
            if (c < 14) WAITBAR(4); else WAITBAR(0);
            if (c + 3 < 16) dma_chunk(c + 3, slot);
            TILE(1, 0, kvb + nslot * P2_SLOT, true);
        } else {
            TILE(1, 0, cp, false);
        }
        slot = nslot;
    }
    o[1] = MFMA32(vprev1, BF(P1), o[1]); lacc[1] = MFMA16(sel, BF(P1), lacc[1]);
    STAMP_MID(1)
#pragma unroll
    for (int a = 0; a < 2; ++a) {
        const float t0 = __shfl(lacc[a][0], lane & 15), t1 = __shfl(lacc[a][1], lane & 15);
        const float l = (lane & 16) ? t1 : t0;
        const float rl = 1.0f / l;
        char* op = ws + WS_O + ((size_t)(b * 64 + mb * 8 + wm * 2 + a) * 32 + h) * 2048 + lane * 16;
        *(u32x4*)(op) = pack8(o[a], 0, rl);
        *(u32x4*)(op + 1024) = pack8(o[a], 1, rl);
    }
    STAMP_END(ws, 1)
}

constexpr int P3_SLOT = 32768, P3_LDS = 4 * P3_SLOT;
__global__ __launch_bounds__(512, 2) void k_oproj(const char* ws, const uint16_t* woA, const uint16_t* woB, uint16_t* out) {
    extern __shared__ __attribute__((aligned(16))) char lds_[];
    LAS char* lds = (LAS char*)lds_;
    STAMP_BEGIN
    const int tid = threadIdx.x, lane = tid & 63, wid = rfl(tid >> 6);
    const int bid = blockIdx.x, xcd = bid & 7, j = bid >> 3;
    const int b = xcd >> 2, tm = (xcd & 3) * 4 + (j >> 3), tn = j & 7;
    const int wt = wid & 3, wn2 = wid >> 2;
    const unsigned lds0 = (unsigned)(uintptr_t)lds_;
    const srd_t so = make_srd(ws + WS_O + (size_t)(b * 64 + tm * 4) * 65536, 4 * 65536), sw = make_srd(b ? woB : woA, 1024 * 1024 * 2);
    const unsigned vl = lane * 16;
    const unsigned vw = (lane >> 4) * 2048 + (((lane & 15) ^ (((lane >> 4) & 3) << 2)) << 4);
    auto dma = [&](int kt, int slot) {
        if (wid < 4) {
            const unsigned dst = lds0 + slot * P3_SLOT + wid * 4096;
#pragma unroll
            for (int i = 0; i < 4; ++i) dma16(dst + i * 1024, vl, so, wid * 65536 + kt * 4096 + i * 1024);
        } else {
            const unsigned dst = lds0 + slot * P3_SLOT + wid * 4096;
#pragma unroll
            for (int i = 0; i < 4; ++i) dma16(dst + i * 1024, vw, sw, (kt * 64 + (wid - 4) * 16 + i * 4) * 2048 + tn * 256);
        }
    };
    dma(0, 0); dma(1, 1); dma(2, 2); dma(3, 3);
    const int g = lane >> 4, q4 = (lane >> 2) & 3, p4 = lane & 3;
    const unsigned oa = wt * 4096 + lane * 16;
    unsigned wa[2];
#pragma unroll
    for (int n = 0; n < 2; ++n) wa[n] = 16384 + (4 * (g >> 1) + q4) * 256 + (((wn2 * 2 + n) ^ q4) << 6) + (g & 1) * 32 + p4 * 8;
    f32x16 acc[2];
#pragma unroll
    for (int n = 0; n < 2; ++n)
#pragma unroll
        for (int i = 0; i < 16; ++i) acc[n][i] = 0.f;
#pragma unroll 4
    for (int kt = 0; kt < 16; ++kt) {
        const int slot = kt & 3;
        if (kt < 13) WAITBAR(12); else if (kt == 13) WAITBAR(8); else if (kt == 14) WAITBAR(4); else WAITBAR(0);
        const LAS char* sp = lds + slot * P3_SLOT;
#pragma unroll
        for (int ks = 0; ks < 4; ++ks) {
            const bf16x8 of = ldsfrag(sp + oa + ks * 1024);
#pragma unroll
            for (int n = 0; n < 2; ++n) {
                const bf16x8 wf = cat8(ldstr(sp + wa[n] + ks * 16 * 256), ldstr(sp + wa[n] + ks * 16 * 256 + 8 * 256));
                acc[n] = MFMA32(wf, of, acc[n]);
            }
        }
        if (kt + 4 < 16) { asm volatile("s_waitcnt lgkmcnt(0)\n\ts_barrier" ::: "memory"); dma(kt + 4, slot); }
    }
    STAMP_MID(1)
    const int token = tm * 128 + wt * 32 + (lane & 31), hh = lane >> 5;
#pragma unroll
    for (int n = 0; n < 2; ++n) {
        uint16_t* op = out + (size_t)token * 2048 + b * 1024 + tn * 128 + wn2 * 64 + n * 32;
#pragma unroll
        for (int gp = 0; gp < 2; ++gp) {
            unsigned a0 = cvtpk(acc[n][8 * gp], acc[n][8 * gp + 1]), a1 = cvtpk(acc[n][8 * gp + 2], acc[n][8 * gp + 3]);
            unsigned b0 = cvtpk(acc[n][8 * gp + 4], acc[n][8 * gp + 5]), b1 = cvtpk(acc[n][8 * gp + 6], acc[n][8 * gp + 7]);
            auto r0 = __builtin_amdgcn_permlane32_swap(a0, b0, false, false);
            auto r1 = __builtin_amdgcn_permlane32_swap(a1, b1, false, false);
            u32x4 v = {r0[0], r1[0], r0[1], r1[1]};
            *(u32x4*)(op + gp * 16 + hh * 8) = v;
        }
    }
    STAMP_END(ws, 2)
}

extern "C" void kernel_launch(void* const* d_in, const int* in_sizes, int n_in, void* d_out, int out_size, void* d_ws, size_t ws_size, hipStream_t stream) {
    (void)hipFuncSetAttribute((const void*)k_qkv, hipFuncAttributeMaxDynamicSharedMemorySize, P1_LDS);
    (void)hipFuncSetAttribute((const void*)k_attn, hipFuncAttributeMaxDynamicSharedMemorySize, P2_LDS);
    (void)hipFuncSetAttribute((const void*)k_oproj, hipFuncAttributeMaxDynamicSharedMemorySize, P3_LDS);
    char* ws = (char*)d_ws;
    k_qkv<<<256, 512, P1_LDS, stream>>>((const uint16_t*)d_in[0], (const uint16_t*)d_in[1], (const uint16_t*)d_in[3], (const uint16_t*)d_in[4], ws);
    k_attn<<<256, 512, P2_LDS, stream>>>(ws);
    k_oproj<<<256, 512, P3_LDS, stream>>>(ws, (const uint16_t*)d_in[2], (const uint16_t*)d_in[5], (uint16_t*)d_out);
}
```

```cpp
#include <hip/hip_runtime.h>
#include <stdint.h>

typedef __bf16 bf16x8 __attribute__((ext_vector_type(8)));
typedef __bf16 bf16x2 __attribute__((ext_vector_type(2)));
typedef short s16x4 __attribute__((ext_vector_type(4)));
typedef short s16x8 __attribute__((ext_vector_type(8)));
typedef float f32x16 __attribute__((ext_vector_type(16)));
typedef float f32x4 __attribute__((ext_vector_type(4)));
typedef float f32x2 __attribute__((ext_vector_type(2)));
typedef unsigned u32x4 __attribute__((ext_vector_type(4)));
typedef unsigned long long u64;
#define LAS __attribute__((address_space(3)))
#define GAS __attribute__((address_space(1)))
#define DI __device__ __forceinline__
#define MFMA32(a, b, c) __builtin_amdgcn_mfma_f32_32x32x16_bf16((a), (b), (c), 0, 0, 0)
#define MFMA16(a, b, c) __builtin_amdgcn_mfma_f32_16x16x32_bf16((a), (b), (c), 0, 0, 0)
#define SBAR() __builtin_amdgcn_sched_barrier(0)
#define BF(x) __builtin_bit_cast(bf16x8, x)
typedef __amdgpu_buffer_rsrc_t srd_t;

#define MAPSEL 1
constexpr float QSCALE = 0.17677669529663687f * 1.4426950408889634f;
constexpr unsigned WS_KV = 0;
constexpr unsigned WS_O = 16u << 20;
constexpr unsigned WS_FL = 24u << 20;
constexpr unsigned FL_KV = 1024, FL_O = 8192;
constexpr int L_S0 = 0, L_S1 = 32768, L_S2 = 65536, L_R0 = 98304, L_R1 = 131072, L_END = 163840;
constexpr int P1_XS = 32768, P1_WS = 24576, P1_WOFF = 98304;

DI unsigned rfl(unsigned x) { return __builtin_amdgcn_readfirstlane(x); }
DI srd_t make_srd(const void* p, unsigned bytes) { return __builtin_amdgcn_make_buffer_rsrc((void*)p, (short)0, (int)bytes, 0x00020000); }
DI void dma16(unsigned ldsdst, unsigned voff, srd_t srd, unsigned soff) {
    asm volatile("s_mov_b32 m0, %0\n\ts_nop 0\n\tbuffer_load_dwordx4 %1, %2, %3 offen lds" :: "s"(ldsdst), "v"(voff), "s"(srd), "s"(soff) : "memory");
}
DI unsigned cvtpk(float lo, float hi) { f32x2 v = {lo, hi}; bf16x2 b = __builtin_convertvector(v, bf16x2); return __builtin_bit_cast(unsigned, b); }
DI bf16x8 ldsfrag(const LAS char* p) { return *(const LAS bf16x8*)p; }
typedef short v4i16_t __attribute__((ext_vector_type(4)));
DI s16x4 ldstr(const LAS char* p) { return __builtin_amdgcn_ds_read_tr16_b64_v4i16((LAS v4i16_t*)p); }
DI bf16x8 cat8(s16x4 lo, s16x4 hi) { s16x8 r = __builtin_shufflevector(lo, hi, 0, 1, 2, 3, 4, 5, 6, 7); return __builtin_bit_cast(bf16x8, r); }
DI u32x4 pack8(const f32x16& x, int s, float mul) {
    u32x4 p; p[0] = cvtpk(x[8 * s] * mul, x[8 * s + 1] * mul); p[1] = cvtpk(x[8 * s + 2] * mul, x[8 * s + 3] * mul);
    p[2] = cvtpk(x[8 * s + 4] * mul, x[8 * s + 5] * mul); p[3] = cvtpk(x[8 * s + 6] * mul, x[8 * s + 7] * mul); return p; }
DI u32x4 pack8n(const f32x16& x, int s) {
    u32x4 p; p[0] = cvtpk(x[8 * s], x[8 * s + 1]); p[1] = cvtpk(x[8 * s + 2], x[8 * s + 3]);
    p[2] = cvtpk(x[8 * s + 4], x[8 * s + 5]); p[3] = cvtpk(x[8 * s + 6], x[8 * s + 7]); return p; }
#define WAITBAR(vm) asm volatile("s_waitcnt vmcnt(" #vm ") lgkmcnt(0)\n\ts_barrier" ::: "memory")
#define LBAR() asm volatile("s_waitcnt lgkmcnt(0)\n\ts_barrier" ::: "memory")
DI float ex2(float x) { return __builtin_amdgcn_exp2f(x); }
DI u64 mix64(u64 z) { z += 0x9E3779B97F4A7C15ull; z = (z ^ (z >> 30)) * 0xBF58476D1CE4E5B9ull; z = (z ^ (z >> 27)) * 0x94D049BB133111EBull; return z ^ (z >> 31); }
DI u64 ld_agent(const GAS u64* p) { return __hip_atomic_load(p, __ATOMIC_RELAXED, __HIP_MEMORY_SCOPE_AGENT); }
DI void st_agent(GAS u64* p, u64 v) { __hip_atomic_store(p, v, __ATOMIC_RELAXED, __HIP_MEMORY_SCOPE_AGENT); }
DI bool flags_ok(u64 v, u64 tag, int lane, int n) { return __all(lane >= n || v == tag); }
DI void spin_flags(const GAS u64* f, u64 tag, int lane, int n) {
    const u64 t0 = __builtin_amdgcn_s_memrealtime();
    for (;;) {
        const u64 v = ld_agent(f + (lane < n ? lane : 0));
        if (flags_ok(v, tag, lane, n)) break;
        if (__builtin_amdgcn_s_memrealtime() - t0 > 20000000ull) break;
        __builtin_amdgcn_s_sleep(1);
    }
}

#define STAMP_BEGIN
#define STAMP_MID(i)
#define STAMP_END(ws)

__global__ __launch_bounds__(512, 2) void mha_fused(const uint16_t* xA, const uint16_t* wA, const uint16_t* woA, const uint16_t* xB, const uint16_t* wB, const uint16_t* woB,
                                                    char* ws, uint16_t* out) {
    extern __shared__ __attribute__((aligned(16))) char lds_[];
    LAS char* lds = (LAS char*)lds_;
    STAMP_BEGIN
    const int tid = threadIdx.x, lane = tid & 63, wid = rfl(tid >> 6);
    const int bid = blockIdx.x, xcd = bid & 7, j = bid >> 3;
    const int b = xcd >> 2;
    const int mb = (xcd & 1) * 4 + (j & 3), hp = ((xcd >> 1) & 1) * 8 + (j >> 2);
    const int wm = wid & 3, wn = wid >> 2, h = hp * 2 + wn;
    const unsigned lds0 = (unsigned)(uintptr_t)lds_;
    const int r32 = lane & 31, hh = lane >> 5, g4 = lane >> 4, q4 = (lane >> 2) & 3, p4 = lane & 3;
    GAS u64* const flags = (GAS u64*)(ws + WS_FL);
    const u64 epoch = ld_agent(flags + b * 32);

    bf16x8 qf[2][2];
    {
        const srd_t sx = make_srd(b ? xB : xA, 2048 * 1024 * 2), sw = make_srd(b ? wB : wA, 1024 * 3072 * 2);
        const unsigned vx0 = (lane >> 3) * 2048 + (((lane & 7) ^ (lane >> 4)) << 4);
        const unsigned vx1 = (lane >> 3) * 2048 + (((lane & 7) ^ (4 + (lane >> 4))) << 4);
        const unsigned vw = (lane >> 3) * 6144 + (((lane & 7) ^ (((lane >> 4) & 1) << 2)) << 4);
        const unsigned sx0 = (mb * 256 + wid * 32) * 2048, sw0 = wid * 8 * 6144 + hp * 128;
        auto dx = [&](int kt, int i) { dma16(lds0 + (kt % 3) * P1_XS + wid * 4096 + i * 1024, (i & 1) ? vx1 : vx0, sx, sx0 + i * 8 * 2048 + kt * 128); };
        auto dw = [&](int kt, int i) { dma16(lds0 + P1_WOFF + (kt & 1) * P1_WS + wid * 1024 + i * 8192, vw, sw, sw0 + i * 2048 + kt * 64 * 6144); };
        for (int i = 0; i < 3; ++i) dw(0, i);
        for (int i = 0; i < 4; ++i) dx(0, i);
        for (int i = 0; i < 3; ++i) dw(1, i);
        for (int i = 0; i < 4; ++i) dx(1, i);
        for (int i = 0; i < 4; ++i) dx(2, i);
        unsigned xa[4];
#pragma unroll
        for (int s = 0; s < 4; ++s) xa[s] = (wm * 64 + r32) * 128 + (((2 * s + hh) ^ ((r32 >> 1) & 7)) << 4);
        const unsigned wa = P1_WOFF + (8 * (g4 >> 1) + q4) * 128 + ((wn ^ (q4 >> 1)) << 6) + (g4 & 1) * 32 + p4 * 8;
        f32x16 acc[2][3];
#pragma unroll
        for (int a = 0; a < 2; ++a)
#pragma unroll
            for (int c = 0; c < 3; ++c)
#pragma unroll
                for (int i = 0; i < 16; ++i) acc[a][c][i] = 0.f;
        bf16x8 xf[2][2], wf[2][3];
#define P1_RX(buf, kt, s) do { const LAS char* xp_ = lds + ((kt) % 3) * P1_XS + xa[s]; xf[buf][0] = ldsfrag(xp_); xf[buf][1] = ldsfrag(xp_ + 4096); } while (0)
#define P1_RW(buf, kt, s, c) do { const LAS char* wp_ = lds + wa + ((kt) & 1) * P1_WS + (s) * 2048 + (c) * 8192; wf[buf][c] = cat8(ldstr(wp_), ldstr(wp_ + 512)); } while (0)
#define P1_STEP(cur, nxt, RD, kt, s, D0, D1, D2) do { \
            acc[0][0] = MFMA32(wf[cur][0], xf[cur][0], acc[0][0]); if (RD) P1_RX(nxt, kt, s); SBAR(); \
            acc[0][1] = MFMA32(wf[cur][1], xf[cur][0], acc[0][1]); if (RD) P1_RW(nxt, kt, s, 0); SBAR(); \
            acc[0][2] = MFMA32(xf[cur][0], wf[cur][2], acc[0][2]); if (RD) P1_RW(nxt, kt, s, 1); D0; SBAR(); \
            acc[1][0] = MFMA32(wf[cur][0], xf[cur][1], acc[1][0]); if (RD) P1_RW(nxt, kt, s, 2); SBAR(); \
            acc[1][1] = MFMA32(wf[cur][1], xf[cur][1], acc[1][1]); D1; SBAR(); \
            acc[1][2] = MFMA32(xf[cur][1], wf[cur][2], acc[1][2]); D2; SBAR(); \
        } while (0)
        WAITBAR(11);
        STAMP_MID(0)
        P1_RX(0, 0, 0); P1_RW(0, 0, 0, 0); P1_RW(0, 0, 0, 1); P1_RW(0, 0, 0, 2); SBAR();
#pragma unroll
        for (int t = 0; t < 16; ++t) {
            const bool xs = (t >= 1 && t + 2 < 16);
            P1_STEP(0, 1, true, t, 1, if (xs) dx(t + 2, 0), if (xs) dx(t + 2, 1), (void)0);
            P1_STEP(1, 0, true, t, 2, if (xs) dx(t + 2, 2), if (xs) dx(t + 2, 3), (void)0);
            P1_STEP(0, 1, true, t, 3, (void)0, (void)0, (void)0);
            if (t < 15) {
                if (t < 14) WAITBAR(4); else WAITBAR(0);
                const bool wsch = (t + 2 < 16);
                P1_STEP(1, 0, true, t + 1, 0, if (wsch) dw(t + 2, 0), if (wsch) dw(t + 2, 1), if (wsch) dw(t + 2, 2));
            } else {
                P1_STEP(1, 0, false, 0, 0, (void)0, (void)0, (void)0);
            }
        }
        STAMP_MID(1)
        LBAR();
        const srd_t skvw = make_srd(ws + WS_KV + (size_t)(b * 32 + h) * 64 * 4096, 64 * 4096);
#pragma unroll
        for (int a = 0; a < 2; ++a) {
            const int tl = wm * 2 + a;
            LAS char* lp = lds + (tl >> 2) * 32768 + wn * 16384 + (tl & 3) * 4096 + lane * 16;
            const unsigned go = (mb * 8 + tl) * 4096;
#pragma unroll
            for (int s = 0; s < 2; ++s) {
                qf[a][s] = BF(pack8(acc[a][0], s, QSCALE));
                const u32x4 kp = pack8n(acc[a][1], s), vp = pack8n(acc[a][2], s);
                __builtin_amdgcn_raw_buffer_store_b128(kp, skvw, lane * 16 + go + s * 1024, 0, 16);
                __builtin_amdgcn_raw_buffer_store_b128(vp, skvw, lane * 16 + go + 2048 + s * 1024, 0, 16);
                *(LAS u32x4*)(lp + s * 1024) = kp;
                *(LAS u32x4*)(lp + 2048 + s * 1024) = vp;
            }
        }
    }
    const u64 tag = mix64(epoch);
    LBAR();
    STAMP_MID(2)

    const srd_t skv = make_srd(ws + WS_KV + (size_t)(b * 32 + hp * 2) * 64 * 4096, 2 * 64 * 4096);
    const srd_t swo = make_srd(b ? woB : woA, 1024 * 1024 * 2);
    const int tm = (xcd & 3) * 4 + (j >> 3), tn = j & 7;
    const unsigned vl = lane * 16;
    const unsigned vwo = (lane >> 4) * 2048 + (((lane & 15) ^ (((lane >> 4) & 3) << 2)) << 4);
    auto chunk_tile = [&](int c) { const int i = (c - 2) >> 1; const int blk = c < 2 ? mb : (i < mb ? i : i + 1); return blk * 8 + (c & 1) * 4; };
    auto dkv = [&](int c, int slotoff, int i) { dma16(lds0 + slotoff + wid * 4096 + i * 1024, vl, skv, (wn * 64 + chunk_tile(c) + wm) * 4096 + i * 1024); };
    auto dwo = [&](int step, int slotoff, int i) {
        dma16(lds0 + slotoff + wid * 4096 + i * 1024, vwo, swo, ((wid >> 2) * 512 + step * 64 + (wid & 3) * 16 + i * 4) * 2048 + tn * 256); };
    bf16x8 sel;
    { const int i16 = lane & 15; const short v = ((i16 == 0 && !(g4 & 1)) || (i16 == 1 && (g4 & 1))) ? (short)0x3F80 : (short)0;
      s16x8 t = {v, v, v, v, v, v, v, v}; sel = __builtin_bit_cast(bf16x8, t); }
    const LAS char* kvb = lds + wn * 16384 + lane * 16;
    const GAS u64* kvfl = flags + (FL_KV / 8) + (b * 16 + hp) * 16;
    f32x16 o[2]; f32x4 lacc[2];
    float mref[2];
    {
        const bf16x8 k0 = ldsfrag(kvb), k1 = ldsfrag(kvb + 1024);
#pragma unroll
        for (int a = 0; a < 2; ++a) {
            f32x16 z;
#pragma unroll
            for (int i = 0; i < 16; ++i) z[i] = 0.f;
            z = MFMA32(k0, qf[a][0], z); z = MFMA32(k1, qf[a][1], z);
            float m = z[0];
#pragma unroll
            for (int i = 1; i < 16; ++i) m = fmaxf(m, z[i]);
            mref[a] = fmaxf(m, __shfl_xor(m, 32));
        }
    }
    for (int pass = 0;; ++pass) {
        f32x16 negm[2], S[2];
        bf16x8 kf[2][2], vf[2][2];
        u32x4 P0, P1;
        if (pass) {
            for (int i = 0; i < 4; ++i) dkv(0, L_S0, i);
            for (int i = 0; i < 4; ++i) dkv(1, L_S1, i);
            WAITBAR(0);
        }
#pragma unroll
        for (int a = 0; a < 2; ++a) {
#pragma unroll
            for (int i = 0; i < 16; ++i) { negm[a][i] = -mref[a]; o[a][i] = 0.f; }
            lacc[a] = (f32x4){0.f, 0.f, 0.f, 0.f};
        }
        kf[0][0] = ldsfrag(kvb); kf[0][1] = ldsfrag(kvb + 1024); vf[0][0] = ldsfrag(kvb + 2048); vf[0][1] = ldsfrag(kvb + 3072);
        S[0] = MFMA32(kf[0][0], qf[0][0], negm[0]); S[0] = MFMA32(kf[0][1], qf[0][1], S[0]);
        P1 = (u32x4){0u, 0u, 0u, 0u};
        bf16x8 vprev1 = vf[0][1];
        bool acq = false;
        u64 pollv = 0;
#define EXPI(X, i) X[i] = ex2(X[i])
#define UNIT(SX, SN, KA0, KA1, QB0, QB1, NEG, DOQK, OPREV, LPREV, VPREV1, OCUR, LCUR, VCUR0, L0, L1, DM0, DM1) do { \
            if (DOQK) SN = MFMA32(KA0, QB0, NEG); L0; SBAR(); \
            EXPI(SX, 0); EXPI(SX, 1); EXPI(SX, 2); EXPI(SX, 3); P0[0] = cvtpk(SX[0], SX[1]); P0[1] = cvtpk(SX[2], SX[3]); SBAR(); \
            OPREV = MFMA32(VPREV1, BF(P1), OPREV); L1; SBAR(); \
            EXPI(SX, 4); EXPI(SX, 5); EXPI(SX, 6); SBAR(); \
            LPREV = MFMA16(sel, BF(P1), LPREV); SBAR(); \
            EXPI(SX, 7); P0[2] = cvtpk(SX[4], SX[5]); P0[3] = cvtpk(SX[6], SX[7]); SBAR(); \
            if (DOQK) SN = MFMA32(KA1, QB1, SN); DM0; SBAR(); \
            EXPI(SX, 8); EXPI(SX, 9); EXPI(SX, 10); EXPI(SX, 11); P1[0] = cvtpk(SX[8], SX[9]); P1[1] = cvtpk(SX[10], SX[11]); SBAR(); \
            OCUR = MFMA32(VCUR0, BF(P0), OCUR); DM1; SBAR(); \
            EXPI(SX, 12); EXPI(SX, 13); EXPI(SX, 14); SBAR(); \
            LCUR = MFMA16(sel, BF(P0), LCUR); SBAR(); \
            EXPI(SX, 15); P1[2] = cvtpk(SX[12], SX[13]); P1[3] = cvtpk(SX[14], SX[15]); SBAR(); \
        } while (0)
#define TILE(cur, nxt, rdp, LOAD, DA, DB) do { \
            UNIT(S[0], S[1], kf[cur][0], kf[cur][1], qf[1][0], qf[1][1], negm[1], true, o[1], lacc[1], vprev1, o[0], lacc[0], vf[cur][0], \
                 if (LOAD) { kf[nxt][0] = ldsfrag(rdp); kf[nxt][1] = ldsfrag((rdp) + 1024); }, \
                 if (LOAD) { vf[nxt][0] = ldsfrag((rdp) + 2048); vf[nxt][1] = ldsfrag((rdp) + 3072); }, DA, (void)0); \
            UNIT(S[1], S[0], kf[nxt][0], kf[nxt][1], qf[0][0], qf[0][1], negm[0], LOAD, o[0], lacc[0], vf[cur][1], o[1], lacc[1], vf[cur][0], \
                 (void)0, (void)0, DB, (void)0); \
            vprev1 = vf[cur][1]; \
        } while (0)
        TILE(0, 1, kvb + L_S0 + 4096, true, (void)0, (void)0);
        if (pass == 0) { WAITBAR(0); if (wid == 0 && lane == 0) st_agent((GAS u64*)kvfl + mb, tag); }
        TILE(1, 0, kvb + L_S0 + 8192, true, (void)0, (void)0);
        if (pass == 0 && wid == 0) pollv = ld_agent(kvfl + (lane & 7));
        TILE(0, 1, kvb + L_S0 + 12288, true, (void)0, (void)0);
        if (pass == 0 && wid == 0) {
            if (flags_ok(pollv, tag, lane, 8)) { __builtin_amdgcn_fence(__ATOMIC_ACQUIRE, "agent"); acq = true; } else pollv = ld_agent(kvfl + (lane & 7));
        }
        TILE(1, 0, kvb + L_S1, true, (void)0, (void)0);
        if (pass == 0 && wid == 0 && !acq) {
            if (flags_ok(pollv, tag, lane, 8)) { __builtin_amdgcn_fence(__ATOMIC_ACQUIRE, "agent"); acq = true; }
        }
        TILE(0, 1, kvb + L_S1 + 4096, true, (void)0, (void)0);
        if (pass == 0 && wid == 0 && !acq) { spin_flags(kvfl, tag, lane, 8); __builtin_amdgcn_fence(__ATOMIC_ACQUIRE, "agent"); }
        WAITBAR(0);
        for (int i = 0; i < 4; ++i) dkv(2, L_S2, i);
        TILE(1, 0, kvb + L_S1 + 8192, true, dkv(3, L_S0, 0), dkv(3, L_S0, 1));
        TILE(0, 1, kvb + L_S1 + 12288, true, dkv(3, L_S0, 2), dkv(3, L_S0, 3));
        WAITBAR(4);
        TILE(1, 0, kvb + L_S2, true, dkv(4, L_S1, 0), (void)0);
        int sc = L_S2, sn = L_S0, sp = L_S1;
        for (int c = 2; c <= 12; ++c) {
            const LAS char* cp = kvb + sc;
            TILE(0, 1, cp + 4096, true, dkv(c + 2, sp, 1), (void)0);
            TILE(1, 0, cp + 8192, true, dkv(c + 2, sp, 2), (void)0);
            TILE(0, 1, cp + 12288, true, dkv(c + 2, sp, 3), (void)0);
            WAITBAR(4);
            TILE(1, 0, kvb + sn, true, dkv(c + 3, sc, 0), (void)0);
            const int t_ = sc; sc = sn; sn = sp; sp = t_;
        }
        TILE(0, 1, kvb + L_S1 + 4096, true, dkv(15, L_S0, 1), (void)0);
        TILE(1, 0, kvb + L_S1 + 8192, true, dkv(15, L_S0, 2), (void)0);
        TILE(0, 1, kvb + L_S1 + 12288, true, dkv(15, L_S0, 3), (void)0);
        WAITBAR(4);
        TILE(1, 0, kvb + L_S2, true, dwo(0, L_R0, 0), dwo(2, L_S1, 0));
        TILE(0, 1, kvb + L_S2 + 4096, true, dwo(0, L_R0, 1), dwo(2, L_S1, 1));
        TILE(1, 0, kvb + L_S2 + 8192, true, dwo(0, L_R0, 2), dwo(2, L_S1, 2));
        TILE(0, 1, kvb + L_S2 + 12288, true, dwo(0, L_R0, 3), dwo(2, L_S1, 3));
        WAITBAR(8);
        TILE(1, 0, kvb + L_S0, true, dwo(1, L_R1, 0), (void)0);
        TILE(0, 1, kvb + L_S0 + 4096, true, dwo(1, L_R1, 1), (void)0);
        TILE(1, 0, kvb + L_S0 + 8192, true, dwo(1, L_R1, 2), (void)0);
        TILE(0, 1, kvb + L_S0 + 12288, true, dwo(1, L_R1, 3), (void)0);
        TILE(1, 0, kvb, false, (void)0, (void)0);
        o[1] = MFMA32(vprev1, BF(P1), o[1]); lacc[1] = MFMA16(sel, BF(P1), lacc[1]);
        STAMP_MID(3)
        bool bad = false;
        const srd_t sow = make_srd(ws + WS_O + (size_t)(b * 64 + mb * 8 + wm * 2) * 65536, 2 * 65536);
#pragma unroll
        for (int a = 0; a < 2; ++a) {
            const float t0 = __shfl(lacc[a][0], lane & 15), t1 = __shfl(lacc[a][1], lane & 15);
            const float l = (lane & 16) ? t1 : t0;
            float z = l * 0.f;
#pragma unroll
            for (int i = 0; i < 16; ++i) z = __builtin_fmaf(o[a][i], 0.f, z);
            bad |= !(l < 1.0e18f) || !(z == 0.f);
            const float rl = 1.0f / l;
            __builtin_amdgcn_raw_buffer_store_b128(pack8(o[a], 0, rl), sow, lane * 16 + a * 65536 + h * 2048, 0, 16);
            __builtin_amdgcn_raw_buffer_store_b128(pack8(o[a], 1, rl), sow, lane * 16 + a * 65536 + h * 2048 + 1024, 0, 16);
        }
        const bool wbad = __any(bad);
        LBAR();
        if (lane == 0) *(LAS unsigned*)(lds + L_S2 + wid * 4) = wbad ? 1u : 0u;
        WAITBAR(0);
        const unsigned vote = *(const LAS unsigned*)(lds + L_S2 + (lane & 7) * 4);
        if (pass || !__any(vote != 0u)) break;
        {
            const char* kg = ws + WS_KV + (size_t)(b * 32 + h) * 64 * 4096 + lane * 16;
#pragma unroll
            for (int a = 0; a < 2; ++a) {
                float m = -__builtin_inff();
                for (int t = 0; t < 64; ++t) {
                    const bf16x8 k0 = *(const bf16x8*)(kg + t * 4096), k1 = *(const bf16x8*)(kg + t * 4096 + 1024);
                    f32x16 z;
#pragma unroll
                    for (int i = 0; i < 16; ++i) z[i] = 0.f;
                    z = MFMA32(k0, qf[a][0], z); z = MFMA32(k1, qf[a][1], z);
#pragma unroll
                    for (int i = 0; i < 16; ++i) m = fmaxf(m, z[i]);
                }
                mref[a] = fmaxf(m, __shfl_xor(m, 32));
            }
        }
        LBAR();
    }
    STAMP_MID(4)

    {
        GAS u64* ofl = flags + (FL_O / 8);
        if (wid == 0 && lane == 0) st_agent(ofl + (b * 8 + mb) * 16 + hp, tag);
        for (int i = 0; i < 4; ++i) dwo(3, L_S0, i);
        if (wid == 0) {
            spin_flags(ofl + (b * 8 + (tm >> 1)) * 16, tag, lane, 16);
            if (lane == 0 && (xcd & 3) == 0 && j == 0) st_agent(flags + b * 32, tag);
        }
        LBAR();
        STAMP_MID(5)
        const int wt = wid & 3, kh = wid >> 2;
        const srd_t sor = make_srd(ws + WS_O + (size_t)(b * 64 + tm * 4 + wt) * 65536, 65536);
        bf16x8 of[4][4];
#define P3_LO(i) do { _Pragma("unroll") for (int ks = 0; ks < 4; ++ks) of[(i) & 3][ks] = BF(__builtin_amdgcn_raw_buffer_load_b128(sor, lane * 16, kh * 32768 + (i) * 4096 + ks * 1024, 16)); } while (0)
        P3_LO(0); P3_LO(1); P3_LO(2); P3_LO(3);
        for (int i = 0; i < 4; ++i) dwo(4, L_S2, i);
        const unsigned wa3 = kh * 16384 + (4 * (g4 >> 1) + q4) * 256 + (g4 & 1) * 32 + p4 * 8;
        f32x16 acc[4];
#pragma unroll
        for (int n = 0; n < 4; ++n)
#pragma unroll
            for (int i = 0; i < 16; ++i) acc[n][i] = 0.f;
#define P3_STEP(i, slot) do { const LAS char* sp_ = lds + (slot) + wa3; \
            _Pragma("unroll") for (int ks = 0; ks < 4; ++ks) { \
                _Pragma("unroll") for (int n = 0; n < 4; ++n) { \
                    const LAS char* w_ = sp_ + ks * 4096 + ((n ^ q4) << 6); \
                    acc[n] = MFMA32(cat8(ldstr(w_), ldstr(w_ + 2048)), of[(i) & 3][ks], acc[n]); } } } while (0)
        WAITBAR(16); P3_STEP(0, L_R0); SBAR(); P3_LO(4);
        WAITBAR(16); for (int i = 0; i < 4; ++i) dwo(5, L_R0, i); P3_STEP(1, L_R1); SBAR(); P3_LO(5);
        WAITBAR(20); for (int i = 0; i < 4; ++i) dwo(6, L_R1, i); P3_STEP(2, L_S1); SBAR(); P3_LO(6);
        WAITBAR(24); for (int i = 0; i < 4; ++i) dwo(7, L_S1, i); P3_STEP(3, L_S0); SBAR(); P3_LO(7);
        WAITBAR(24); P3_STEP(4, L_S2);
        WAITBAR(16); P3_STEP(5, L_R0);
        WAITBAR(8); P3_STEP(6, L_R1);
        WAITBAR(0); P3_STEP(7, L_S1);
        STAMP_MID(6)
        LAS char* ex = lds + L_S0;
        LAS char* mine = lds + (kh ? L_S2 : L_S0) + wt * 8192 + lane * 16;
        LAS char* theirs = lds + (kh ? L_S0 : L_S2) + wt * 8192 + lane * 16;
        (void)ex;
#pragma unroll
        for (int n2 = 0; n2 < 2; ++n2)
#pragma unroll
            for (int qd = 0; qd < 4; ++qd) {
                const f32x16& src = kh ? acc[n2] : acc[2 + n2];
                *(LAS f32x4*)(mine + n2 * 4096 + qd * 1024) = (f32x4){src[4 * qd], src[4 * qd + 1], src[4 * qd + 2], src[4 * qd + 3]};
            }
        LBAR();
        const int token = tm * 128 + wt * 32 + r32;
#pragma unroll
        for (int n2 = 0; n2 < 2; ++n2) {
            f32x16 v = kh ? acc[2 + n2] : acc[n2];
#pragma unroll
            for (int qd = 0; qd < 4; ++qd) {
                const f32x4 t = *(const LAS f32x4*)(theirs + n2 * 4096 + qd * 1024);
                v[4 * qd] += t[0]; v[4 * qd + 1] += t[1]; v[4 * qd + 2] += t[2]; v[4 * qd + 3] += t[3];
            }
            uint16_t* op = out + (size_t)token * 2048 + b * 1024 + tn * 128 + (2 * kh + n2) * 32;
#pragma unroll
            for (int gp = 0; gp < 2; ++gp) {
                const unsigned a0 = cvtpk(v[8 * gp], v[8 * gp + 1]), a1 = cvtpk(v[8 * gp + 2], v[8 * gp + 3]);
                const unsigned b0 = cvtpk(v[8 * gp + 4], v[8 * gp + 5]), b1 = cvtpk(v[8 * gp + 6], v[8 * gp + 7]);
                auto r0 = __builtin_amdgcn_permlane32_swap(a0, b0, false, false);
                auto r1 = __builtin_amdgcn_permlane32_swap(a1, b1, false, false);
                const u32x4 w = {r0[0], r1[0], r0[1], r1[1]};
                *(u32x4*)(op + gp * 16 + hh * 8) = w;
            }
        }
    }
    STAMP_END(ws)
}

extern "C" void kernel_launch(void* const* d_in, const int* in_sizes, int n_in, void* d_out, int out_size, void* d_ws, size_t ws_size, hipStream_t stream) {
    (void)hipFuncSetAttribute((const void*)mha_fused, hipFuncAttributeMaxDynamicSharedMemorySize, L_END);
    mha_fused<<<256, 512, L_END, stream>>>((const uint16_t*)d_in[0], (const uint16_t*)d_in[1], (const uint16_t*)d_in[2], (const uint16_t*)d_in[3], (const uint16_t*)d_in[4],
                                           (const uint16_t*)d_in[5], (char*)d_ws, (uint16_t*)d_out);
}
```

```cpp
#define PF_TLB
#include <hip/hip_runtime.h>
#include <stdint.h>

typedef __bf16 bf16x8 __attribute__((ext_vector_type(8)));
typedef __bf16 bf16x2 __attribute__((ext_vector_type(2)));
typedef short s16x4 __attribute__((ext_vector_type(4)));
typedef short s16x8 __attribute__((ext_vector_type(8)));
typedef float f32x16 __attribute__((ext_vector_type(16)));
typedef float f32x4 __attribute__((ext_vector_type(4)));
typedef float f32x2 __attribute__((ext_vector_type(2)));
typedef unsigned u32x4 __attribute__((ext_vector_type(4)));
typedef unsigned long long u64;
#define LAS __attribute__((address_space(3)))
#define GAS __attribute__((address_space(1)))
#define DI __device__ __forceinline__
#define MFMA32(a, b, c) __builtin_amdgcn_mfma_f32_32x32x16_bf16((a), (b), (c), 0, 0, 0)
#define MFMA16(a, b, c) __builtin_amdgcn_mfma_f32_16x16x32_bf16((a), (b), (c), 0, 0, 0)
#define SBAR() __builtin_amdgcn_sched_barrier(0)
#define BF(x) __builtin_bit_cast(bf16x8, x)
typedef __amdgpu_buffer_rsrc_t srd_t;

#define MAPSEL 1
constexpr float QSCALE = 0.17677669529663687f * 1.4426950408889634f;
constexpr unsigned WS_KV = 0;
constexpr unsigned WS_O = 16u << 20;
constexpr unsigned WS_FL = 24u << 20;
constexpr unsigned FL_KV = 1024, FL_O = 8192;
constexpr int L_S0 = 0, L_S1 = 32768, L_S2 = 65536, L_R0 = 98304, L_R1 = 131072, L_END = 163840;
constexpr int P1_XS = 32768, P1_WS = 24576, P1_WOFF = 98304;

DI unsigned rfl(unsigned x) { return __builtin_amdgcn_readfirstlane(x); }
DI srd_t make_srd(const void* p, unsigned bytes) { return __builtin_amdgcn_make_buffer_rsrc((void*)p, (short)0, (int)bytes, 0x00020000); }
DI void dma16(unsigned ldsdst, unsigned voff, srd_t srd, unsigned soff) {
    asm volatile("s_mov_b32 m0, %0\n\ts_nop 0\n\tbuffer_load_dwordx4 %1, %2, %3 offen lds" :: "s"(ldsdst), "v"(voff), "s"(srd), "s"(soff) : "memory");
}
DI unsigned cvtpk(float lo, float hi) { f32x2 v = {lo, hi}; bf16x2 b = __builtin_convertvector(v, bf16x2); return __builtin_bit_cast(unsigned, b); }
DI bf16x8 ldsfrag(const LAS char* p) { return *(const LAS bf16x8*)p; }
typedef short v4i16_t __attribute__((ext_vector_type(4)));
DI s16x4 ldstr(const LAS char* p) { return __builtin_amdgcn_ds_read_tr16_b64_v4i16((LAS v4i16_t*)p); }
DI bf16x8 cat8(s16x4 lo, s16x4 hi) { s16x8 r = __builtin_shufflevector(lo, hi, 0, 1, 2, 3, 4, 5, 6, 7); return __builtin_bit_cast(bf16x8, r); }
DI u32x4 pack8(const f32x16& x, int s, float mul) {
    u32x4 p; p[0] = cvtpk(x[8 * s] * mul, x[8 * s + 1] * mul); p[1] = cvtpk(x[8 * s + 2] * mul, x[8 * s + 3] * mul);
    p[2] = cvtpk(x[8 * s + 4] * mul, x[8 * s + 5] * mul); p[3] = cvtpk(x[8 * s + 6] * mul, x[8 * s + 7] * mul); return p; }
DI u32x4 pack8n(const f32x16& x, int s) {
    u32x4 p; p[0] = cvtpk(x[8 * s], x[8 * s + 1]); p[1] = cvtpk(x[8 * s + 2], x[8 * s + 3]);
    p[2] = cvtpk(x[8 * s + 4], x[8 * s + 5]); p[3] = cvtpk(x[8 * s + 6], x[8 * s + 7]); return p; }
#define WAITBAR(vm) asm volatile("s_waitcnt vmcnt(" #vm ") lgkmcnt(0)\n\ts_barrier" ::: "memory")
#define LBAR() asm volatile("s_waitcnt lgkmcnt(0)\n\ts_barrier" ::: "memory")
DI float ex2(float x) { return __builtin_amdgcn_exp2f(x); }
DI u64 mix64(u64 z) { z += 0x9E3779B97F4A7C15ull; z = (z ^ (z >> 30)) * 0xBF58476D1CE4E5B9ull; z = (z ^ (z >> 27)) * 0x94D049BB133111EBull; return z ^ (z >> 31); }
DI u64 ld_agent(const GAS u64* p) { return __hip_atomic_load(p, __ATOMIC_RELAXED, __HIP_MEMORY_SCOPE_AGENT); }
DI void st_agent(GAS u64* p, u64 v) { __hip_atomic_store(p, v, __ATOMIC_RELAXED, __HIP_MEMORY_SCOPE_AGENT); }
DI bool flags_ok(u64 v, u64 tag, int lane, int n) { return __all(lane >= n || v == tag); }
DI void spin_flags(const GAS u64* f, u64 tag, int lane, int n) {
    const u64 t0 = __builtin_amdgcn_s_memrealtime();
    for (;;) {
        const u64 v = ld_agent(f + (lane < n ? lane : 0));
        if (flags_ok(v, tag, lane, n)) break;
        if (__builtin_amdgcn_s_memrealtime() - t0 > 20000000ull) break;
        __builtin_amdgcn_s_sleep(1);
    }
}

#define STAMP_BEGIN
#define STAMP_MID(i)
#define STAMP_END(ws)

__global__ __launch_bounds__(512, 2) void mha_fused(const uint16_t* xA, const uint16_t* wA, const uint16_t* woA, const uint16_t* xB, const uint16_t* wB, const uint16_t* woB,
                                                    char* ws, uint16_t* out) {
    extern __shared__ __attribute__((aligned(16))) char lds_[];
    LAS char* lds = (LAS char*)lds_;
    STAMP_BEGIN
    const int tid = threadIdx.x, lane = tid & 63, wid = rfl(tid >> 6);
    const int bid = blockIdx.x, xcd = bid & 7, j = bid >> 3;
    const int b = xcd >> 2;
    const int mb = (xcd & 1) * 4 + (j & 3), hp = ((xcd >> 1) & 1) * 8 + (j >> 2);
    const int wm = wid & 3, wn = wid >> 2, h = hp * 2 + wn;
    const unsigned lds0 = (unsigned)(uintptr_t)lds_;
    const int r32 = lane & 31, hh = lane >> 5, g4 = lane >> 4, q4 = (lane >> 2) & 3, p4 = lane & 3;
    GAS u64* const flags = (GAS u64*)(ws + WS_FL);
    u64 epoch = ld_agent(flags + b * 32);

    bf16x8 qf[2][2];
    unsigned pf0 = 0, pf1 = 0;
    {
        const srd_t sx = make_srd(b ? xB : xA, 2048 * 1024 * 2), sw = make_srd(b ? wB : wA, 1024 * 3072 * 2);
        const unsigned vx0 = (lane >> 3) * 2048 + (((lane & 7) ^ (lane >> 4)) << 4);
        const unsigned vx1 = (lane >> 3) * 2048 + (((lane & 7) ^ (4 + (lane >> 4))) << 4);
        const unsigned vw = (lane >> 3) * 6144 + (((lane & 7) ^ (((lane >> 4) & 1) << 2)) << 4);
        const unsigned sx0 = (mb * 256 + wid * 32) * 2048, sw0 = wid * 8 * 6144 + hp * 128;
        auto dx = [&](int kt, int i) { dma16(lds0 + (kt % 3) * P1_XS + wid * 4096 + i * 1024, (i & 1) ? vx1 : vx0, sx, sx0 + i * 8 * 2048 + kt * 128); };
        auto dw = [&](int kt, int i) { dma16(lds0 + P1_WOFF + (kt & 1) * P1_WS + wid * 1024 + i * 8192, vw, sw, sw0 + i * 2048 + kt * 64 * 6144); };
        for (int i = 0; i < 3; ++i) dw(0, i);
        for (int i = 0; i < 4; ++i) dx(0, i);
        for (int i = 0; i < 3; ++i) dw(1, i);
        for (int i = 0; i < 4; ++i) dx(1, i);
        for (int i = 0; i < 4; ++i) dx(2, i);
        {
            const char* kvp = ws + WS_KV + (size_t)(b * 32 + hp * 2) * 64 * 4096;
            const char* opp = ws + WS_O + (size_t)b * 64 * 65536;
            const char* wop = (const char*)(b ? woB : woA);
            const char* outp = (const char*)out;
            const uintptr_t p0 = (uintptr_t)kvp + (tid & 7) * 65536, p1 = (uintptr_t)opp + ((tid - 8) & 63) * 65536, p2 = (uintptr_t)wop + ((tid - 72) & 31) * 65536, p3 = (uintptr_t)outp + ((tid - 104) & 127) * 65536;
            const char* p = (const char*)(tid < 8 ? p0 : tid < 72 ? p1 : tid < 104 ? p2 : p3);
            pf1 = *(const GAS unsigned*)p;
        }
        unsigned xa[4];
#pragma unroll
        for (int s = 0; s < 4; ++s) xa[s] = (wm * 64 + r32) * 128 + (((2 * s + hh) ^ ((r32 >> 1) & 7)) << 4);
        const unsigned wa = P1_WOFF + (8 * (g4 >> 1) + q4) * 128 + ((wn ^ (q4 >> 1)) << 6) + (g4 & 1) * 32 + p4 * 8;
        f32x16 acc[2][3];
#pragma unroll
        for (int a = 0; a < 2; ++a)
#pragma unroll
            for (int c = 0; c < 3; ++c)
#pragma unroll
                for (int i = 0; i < 16; ++i) acc[a][c][i] = 0.f;
        bf16x8 xf[2][2], wf[2][3];
#define P1_RX(buf, kt, s) do { const LAS char* xp_ = lds + ((kt) % 3) * P1_XS + xa[s]; xf[buf][0] = ldsfrag(xp_); xf[buf][1] = ldsfrag(xp_ + 4096); } while (0)
#define P1_RW(buf, kt, s, c) do { const LAS char* wp_ = lds + wa + ((kt) & 1) * P1_WS + (s) * 2048 + (c) * 8192; wf[buf][c] = cat8(ldstr(wp_), ldstr(wp_ + 512)); } while (0)
#define P1_STEP(cur, nxt, RD, kt, s, D0, D1, D2) do { \
            acc[0][0] = MFMA32(wf[cur][0], xf[cur][0], acc[0][0]); if (RD) P1_RX(nxt, kt, s); SBAR(); \
            acc[0][1] = MFMA32(wf[cur][1], xf[cur][0], acc[0][1]); if (RD) P1_RW(nxt, kt, s, 0); SBAR(); \
            acc[0][2] = MFMA32(xf[cur][0], wf[cur][2], acc[0][2]); if (RD) P1_RW(nxt, kt, s, 1); D0; SBAR(); \
            acc[1][0] = MFMA32(wf[cur][0], xf[cur][1], acc[1][0]); if (RD) P1_RW(nxt, kt, s, 2); SBAR(); \
            acc[1][1] = MFMA32(wf[cur][1], xf[cur][1], acc[1][1]); D1; SBAR(); \
            acc[1][2] = MFMA32(xf[cur][1], wf[cur][2], acc[1][2]); D2; SBAR(); \
        } while (0)
        WAITBAR(11);
        STAMP_MID(0)
        P1_RX(0, 0, 0); P1_RW(0, 0, 0, 0); P1_RW(0, 0, 0, 1); P1_RW(0, 0, 0, 2); SBAR();
#pragma unroll
        for (int t = 0; t < 16; ++t) {
            const bool xs = (t >= 1 && t + 2 < 16);
            P1_STEP(0, 1, true, t, 1, if (xs) dx(t + 2, 0), if (xs) dx(t + 2, 1), (void)0);
            P1_STEP(1, 0, true, t, 2, if (xs) dx(t + 2, 2), if (xs) dx(t + 2, 3), (void)0);
            P1_STEP(0, 1, true, t, 3, (void)0, (void)0, (void)0);
            if (t < 15) {
                if (t < 14) WAITBAR(4); else WAITBAR(0);
                const bool wsch = (t + 2 < 16);
                P1_STEP(1, 0, true, t + 1, 0, if (wsch) dw(t + 2, 0), if (wsch) dw(t + 2, 1), if (wsch) dw(t + 2, 2));
            } else {
                P1_STEP(1, 0, false, 0, 0, (void)0, (void)0, (void)0);
            }
        }
        STAMP_MID(1)
        LBAR();
        const srd_t skvw = make_srd(ws + WS_KV + (size_t)(b * 32 + h) * 64 * 4096, 64 * 4096);
#pragma unroll
        for (int a = 0; a < 2; ++a) {
            const int tl = wm * 2 + a;
            LAS char* lp = lds + (tl >> 2) * 32768 + wn * 16384 + (tl & 3) * 4096 + lane * 16;
            const unsigned go = (mb * 8 + tl) * 4096;
#pragma unroll
            for (int s = 0; s < 2; ++s) {
                qf[a][s] = BF(pack8(acc[a][0], s, QSCALE));
                const u32x4 kp = pack8n(acc[a][1], s), vp = pack8n(acc[a][2], s);
                __builtin_amdgcn_raw_buffer_store_b128(kp, skvw, lane * 16 + go + s * 1024, 0, 16);
                __builtin_amdgcn_raw_buffer_store_b128(vp, skvw, lane * 16 + go + 2048 + s * 1024, 0, 16);
                *(LAS u32x4*)(lp + s * 1024) = kp;
                *(LAS u32x4*)(lp + 2048 + s * 1024) = vp;
            }
        }
    }
    asm volatile("" : "+v"(epoch) : "v"(pf0), "v"(pf1));
    const u64 tag = mix64(((u64)rfl((unsigned)(epoch >> 32)) << 32) | rfl((unsigned)epoch));
    LBAR();
    STAMP_MID(2)

    const srd_t skv = make_srd(ws + WS_KV + (size_t)(b * 32 + hp * 2) * 64 * 4096, 2 * 64 * 4096);
    const srd_t swo = make_srd(b ? woB : woA, 1024 * 1024 * 2);
    const int tm = (xcd & 3) * 4 + (j >> 3), tn = j & 7;
    const unsigned vl = lane * 16;
    const unsigned vwo = (lane >> 4) * 2048 + (((lane & 15) ^ (((lane >> 4) & 3) << 2)) << 4);
    auto chunk_tile = [&](int c) { const int i = (c - 2) >> 1; const int blk = c < 2 ? mb : (i < mb ? i : i + 1); return blk * 8 + (c & 1) * 4; };
    auto dkv = [&](int c, int slotoff, int i) { dma16(lds0 + slotoff + wid * 4096 + i * 1024, vl, skv, (wn * 64 + chunk_tile(c) + wm) * 4096 + i * 1024); };
    auto dwo = [&](int step, int slotoff, int i) {
        dma16(lds0 + slotoff + wid * 4096 + i * 1024, vwo, swo, ((wid >> 2) * 512 + step * 64 + (wid & 3) * 16 + i * 4) * 2048 + tn * 256); };
    bf16x8 sel;
    { const int i16 = lane & 15; const short v = ((i16 == 0 && !(g4 & 1)) || (i16 == 1 && (g4 & 1))) ? (short)0x3F80 : (short)0;
      s16x8 t = {v, v, v, v, v, v, v, v}; sel = __builtin_bit_cast(bf16x8, t); }
    const LAS char* kvb = lds + wn * 16384 + lane * 16;
    const GAS u64* kvfl = flags + (FL_KV / 8) + (b * 16 + hp) * 16;
    f32x16 o[2]; f32x4 lacc[2];
    float mref[2];
    {
        const bf16x8 k0 = ldsfrag(kvb), k1 = ldsfrag(kvb + 1024);
#pragma unroll
        for (int a = 0; a < 2; ++a) {
            f32x16 z;
#pragma unroll
            for (int i = 0; i < 16; ++i) z[i] = 0.f;
            z = MFMA32(k0, qf[a][0], z); z = MFMA32(k1, qf[a][1], z);
            float m = z[0];
#pragma unroll
            for (int i = 1; i < 16; ++i) m = fmaxf(m, z[i]);
            mref[a] = fmaxf(m, __shfl_xor(m, 32));
        }
    }
    for (int pass = 0;; ++pass) {
        f32x16 negm[2], S[2];
        bf16x8 kf[2][2], vf[2][2];
        u32x4 P0, P1;
        if (pass) {
            for (int i = 0; i < 4; ++i) dkv(0, L_S0, i);
            for (int i = 0; i < 4; ++i) dkv(1, L_S1, i);
            WAITBAR(0);
        }
#pragma unroll
        for (int a = 0; a < 2; ++a) {
#pragma unroll
            for (int i = 0; i < 16; ++i) { negm[a][i] = -mref[a]; o[a][i] = 0.f; }
            lacc[a] = (f32x4){0.f, 0.f, 0.f, 0.f};
        }
        kf[0][0] = ldsfrag(kvb); kf[0][1] = ldsfrag(kvb + 1024); vf[0][0] = ldsfrag(kvb + 2048); vf[0][1] = ldsfrag(kvb + 3072);
        S[0] = MFMA32(kf[0][0], qf[0][0], negm[0]); S[0] = MFMA32(kf[0][1], qf[0][1], S[0]);
        P1 = (u32x4){0u, 0u, 0u, 0u};
        bf16x8 vprev1 = vf[0][1];
        bool acq = false;
        u64 pollv = 0;
#define EXPI(X, i) X[i] = ex2(X[i])
#define UNIT(SX, SN, KA0, KA1, QB0, QB1, NEG, DOQK, OPREV, LPREV, VPREV1, OCUR, LCUR, VCUR0, L0, L1, DM0, DM1) do { \
            if (DOQK) SN = MFMA32(KA0, QB0, NEG); L0; SBAR(); \
            EXPI(SX, 0); EXPI(SX, 1); EXPI(SX, 2); EXPI(SX, 3); P0[0] = cvtpk(SX[0], SX[1]); P0[1] = cvtpk(SX[2], SX[3]); SBAR(); \
            OPREV = MFMA32(VPREV1, BF(P1), OPREV); L1; SBAR(); \
            EXPI(SX, 4); EXPI(SX, 5); EXPI(SX, 6); SBAR(); \
            LPREV = MFMA16(sel, BF(P1), LPREV); SBAR(); \
            EXPI(SX, 7); P0[2] = cvtpk(SX[4], SX[5]); P0[3] = cvtpk(SX[6], SX[7]); SBAR(); \
            if (DOQK) SN = MFMA32(KA1, QB1, SN); DM0; SBAR(); \
            EXPI(SX, 8); EXPI(SX, 9); EXPI(SX, 10); EXPI(SX, 11); P1[0] = cvtpk(SX[8], SX[9]); P1[1] = cvtpk(SX[10], SX[11]); SBAR(); \
            OCUR = MFMA32(VCUR0, BF(P0), OCUR); DM1; SBAR(); \
            EXPI(SX, 12); EXPI(SX, 13); EXPI(SX, 14); SBAR(); \
            LCUR = MFMA16(sel, BF(P0), LCUR); SBAR(); \
            EXPI(SX, 15); P1[2] = cvtpk(SX[12], SX[13]); P1[3] = cvtpk(SX[14], SX[15]); SBAR(); \
        } while (0)
#define TILE(cur, nxt, rdp, LOAD, DA, DB) do { \
            UNIT(S[0], S[1], kf[cur][0], kf[cur][1], qf[1][0], qf[1][1], negm[1], true, o[1], lacc[1], vprev1, o[0], lacc[0], vf[cur][0], \
                 if (LOAD) { kf[nxt][0] = ldsfrag(rdp); kf[nxt][1] = ldsfrag((rdp) + 1024); }, \
                 if (LOAD) { vf[nxt][0] = ldsfrag((rdp) + 2048); vf[nxt][1] = ldsfrag((rdp) + 3072); }, DA, (void)0); \
            UNIT(S[1], S[0], kf[nxt][0], kf[nxt][1], qf[0][0], qf[0][1], negm[0], LOAD, o[0], lacc[0], vf[cur][1], o[1], lacc[1], vf[cur][0], \
                 (void)0, (void)0, DB, (void)0); \
            vprev1 = vf[cur][1]; \
        } while (0)
        TILE(0, 1, kvb + L_S0 + 4096, true, (void)0, (void)0);
        if (pass == 0) { WAITBAR(0); if (wid == 0 && lane == 0) st_agent((GAS u64*)kvfl + mb, tag); }
        TILE(1, 0, kvb + L_S0 + 8192, true, (void)0, (void)0);
        if (pass == 0 && wid == 0) pollv = ld_agent(kvfl + (lane & 7));
        TILE(0, 1, kvb + L_S0 + 12288, true, (void)0, (void)0);
        if (pass == 0 && wid == 0) {
            if (flags_ok(pollv, tag, lane, 8)) { __builtin_amdgcn_fence(__ATOMIC_ACQUIRE, "agent"); acq = true; } else pollv = ld_agent(kvfl + (lane & 7));
        }
        TILE(1, 0, kvb + L_S1, true, (void)0, (void)0);
        if (pass == 0 && wid == 0 && !acq) {
            if (flags_ok(pollv, tag, lane, 8)) { __builtin_amdgcn_fence(__ATOMIC_ACQUIRE, "agent"); acq = true; }
        }
        TILE(0, 1, kvb + L_S1 + 4096, true, (void)0, (void)0);
        if (pass == 0 && wid == 0 && !acq) { spin_flags(kvfl, tag, lane, 8); __builtin_amdgcn_fence(__ATOMIC_ACQUIRE, "agent"); }
        WAITBAR(0);
        for (int i = 0; i < 4; ++i) dkv(2, L_S2, i);
        TILE(1, 0, kvb + L_S1 + 8192, true, dkv(3, L_S0, 0), dkv(3, L_S0, 1));
        TILE(0, 1, kvb + L_S1 + 12288, true, dkv(3, L_S0, 2), dkv(3, L_S0, 3));
        WAITBAR(4);
        TILE(1, 0, kvb + L_S2, true, dkv(4, L_S1, 0), (void)0);
        int sc = L_S2, sn = L_S0, sp = L_S1;
        for (int c = 2; c <= 12; ++c) {
            const LAS char* cp = kvb + sc;
            TILE(0, 1, cp + 4096, true, dkv(c + 2, sp, 1), (void)0);
            TILE(1, 0, cp + 8192, true, dkv(c + 2, sp, 2), (void)0);
            TILE(0, 1, cp + 12288, true, dkv(c + 2, sp, 3), (void)0);
            WAITBAR(4);
            TILE(1, 0, kvb + sn, true, dkv(c + 3, sc, 0), (void)0);
            const int t_ = sc; sc = sn; sn = sp; sp = t_;
        }
        TILE(0, 1, kvb + L_S1 + 4096, true, dkv(15, L_S0, 1), (void)0);
        TILE(1, 0, kvb + L_S1 + 8192, true, dkv(15, L_S0, 2), (void)0);
        TILE(0, 1, kvb + L_S1 + 12288, true, dkv(15, L_S0, 3), (void)0);
        WAITBAR(4);
        TILE(1, 0, kvb + L_S2, true, dwo(0, L_R0, 0), dwo(2, L_S1, 0));
        TILE(0, 1, kvb + L_S2 + 4096, true, dwo(0, L_R0, 1), dwo(2, L_S1, 1));
        TILE(1, 0, kvb + L_S2 + 8192, true, dwo(0, L_R0, 2), dwo(2, L_S1, 2));
        TILE(0, 1, kvb + L_S2 + 12288, true, dwo(0, L_R0, 3), dwo(2, L_S1, 3));
        WAITBAR(8);
        TILE(1, 0, kvb + L_S0, true, dwo(1, L_R1, 0), (void)0);
        TILE(0, 1, kvb + L_S0 + 4096, true, dwo(1, L_R1, 1), (void)0);
        TILE(1, 0, kvb + L_S0 + 8192, true, dwo(1, L_R1, 2), (void)0);
        TILE(0, 1, kvb + L_S0 + 12288, true, dwo(1, L_R1, 3), (void)0);
        TILE(1, 0, kvb, false, (void)0, (void)0);
        o[1] = MFMA32(vprev1, BF(P1), o[1]); lacc[1] = MFMA16(sel, BF(P1), lacc[1]);
        STAMP_MID(3)
        bool bad = false;
        const srd_t sow = make_srd(ws + WS_O + (size_t)(b * 64 + mb * 8 + wm * 2) * 65536, 2 * 65536);
#pragma unroll
        for (int a = 0; a < 2; ++a) {
            const float t0 = __shfl(lacc[a][0], lane & 15), t1 = __shfl(lacc[a][1], lane & 15);
            const float l = (lane & 16) ? t1 : t0;
            float z = l * 0.f;
#pragma unroll
            for (int i = 0; i < 16; ++i) z = __builtin_fmaf(o[a][i], 0.f, z);
            bad |= !(l < 1.0e18f) || !(z == 0.f);
            const float rl = 1.0f / l;
            __builtin_amdgcn_raw_buffer_store_b128(pack8(o[a], 0, rl), sow, lane * 16 + a * 65536 + h * 2048, 0, 16);
            __builtin_amdgcn_raw_buffer_store_b128(pack8(o[a], 1, rl), sow, lane * 16 + a * 65536 + h * 2048 + 1024, 0, 16);
        }
        const bool wbad = __any(bad);
        LBAR();
        if (lane == 0) *(LAS unsigned*)(lds + L_S2 + wid * 4) = wbad ? 1u : 0u;
        WAITBAR(0);
        const unsigned vote = *(const LAS unsigned*)(lds + L_S2 + (lane & 7) * 4);
        if (pass || !__any(vote != 0u)) break;
        {
            const char* kg = ws + WS_KV + (size_t)(b * 32 + h) * 64 * 4096 + lane * 16;
#pragma unroll
            for (int a = 0; a < 2; ++a) {
                float m = -__builtin_inff();
                for (int t = 0; t < 64; ++t) {
                    const bf16x8 k0 = *(const bf16x8*)(kg + t * 4096), k1 = *(const bf16x8*)(kg + t * 4096 + 1024);
                    f32x16 z;
#pragma unroll
                    for (int i = 0; i < 16; ++i) z[i] = 0.f;
                    z = MFMA32(k0, qf[a][0], z); z = MFMA32(k1, qf[a][1], z);
#pragma unroll
                    for (int i = 0; i < 16; ++i) m = fmaxf(m, z[i]);
                }
                mref[a] = fmaxf(m, __shfl_xor(m, 32));
            }
        }
        LBAR();
    }
    STAMP_MID(4)

    {
        GAS u64* ofl = flags + (FL_O / 8);
        if (wid == 0 && lane == 0) st_agent(ofl + (b * 8 + mb) * 16 + hp, tag);
        for (int i = 0; i < 4; ++i) dwo(3, L_S0, i);
        if (wid == 0) {
            spin_flags(ofl + (b * 8 + (tm >> 1)) * 16, tag, lane, 16);
            if (lane == 0 && (xcd & 3) == 0 && j == 0) st_agent(flags + b * 32, tag);
        }
        LBAR();
        STAMP_MID(5)
        const int wt = wid & 3, kh = wid >> 2;
        const srd_t sor = make_srd(ws + WS_O + (size_t)(b * 64 + tm * 4 + wt) * 65536, 65536);
        bf16x8 of[4][4];
#define P3_LO(i) do { _Pragma("unroll") for (int ks = 0; ks < 4; ++ks) of[(i) & 3][ks] = BF(__builtin_amdgcn_raw_buffer_load_b128(sor, lane * 16, kh * 32768 + (i) * 4096 + ks * 1024, 16)); } while (0)
        P3_LO(0); P3_LO(1); P3_LO(2); P3_LO(3);
        for (int i = 0; i < 4; ++i) dwo(4, L_S2, i);
        const unsigned wa3 = kh * 16384 + (4 * (g4 >> 1) + q4) * 256 + (g4 & 1) * 32 + p4 * 8;
        f32x16 acc[4];
#pragma unroll
        for (int n = 0; n < 4; ++n)
#pragma unroll
            for (int i = 0; i < 16; ++i) acc[n][i] = 0.f;
#define P3_STEP(i, slot) do { const LAS char* sp_ = lds + (slot) + wa3; \
            _Pragma("unroll") for (int ks = 0; ks < 4; ++ks) { \
                _Pragma("unroll") for (int n = 0; n < 4; ++n) { \
                    const LAS char* w_ = sp_ + ks * 4096 + ((n ^ q4) << 6); \
                    acc[n] = MFMA32(cat8(ldstr(w_), ldstr(w_ + 2048)), of[(i) & 3][ks], acc[n]); } } } while (0)
        WAITBAR(16); P3_STEP(0, L_R0); SBAR(); P3_LO(4);
        WAITBAR(16); for (int i = 0; i < 4; ++i) dwo(5, L_R0, i); P3_STEP(1, L_R1); SBAR(); P3_LO(5);
        WAITBAR(20); for (int i = 0; i < 4; ++i) dwo(6, L_R1, i); P3_STEP(2, L_S1); SBAR(); P3_LO(6);
        WAITBAR(24); for (int i = 0; i < 4; ++i) dwo(7, L_S1, i); P3_STEP(3, L_S0); SBAR(); P3_LO(7);
        WAITBAR(24); P3_STEP(4, L_S2);
        WAITBAR(16); P3_STEP(5, L_R0);
        WAITBAR(8); P3_STEP(6, L_R1);
        WAITBAR(0); P3_STEP(7, L_S1);
        STAMP_MID(6)
        LAS char* ex = lds + L_S0;
        LAS char* mine = lds + (kh ? L_S2 : L_S0) + wt * 8192 + lane * 16;
        LAS char* theirs = lds + (kh ? L_S0 : L_S2) + wt * 8192 + lane * 16;
        (void)ex;
#pragma unroll
        for (int n2 = 0; n2 < 2; ++n2)
#pragma unroll
            for (int qd = 0; qd < 4; ++qd) {
                const f32x16& src = kh ? acc[n2] : acc[2 + n2];
                *(LAS f32x4*)(mine + n2 * 4096 + qd * 1024) = (f32x4){src[4 * qd], src[4 * qd + 1], src[4 * qd + 2], src[4 * qd + 3]};
            }
        LBAR();
        const int token = tm * 128 + wt * 32 + r32;
#pragma unroll
        for (int n2 = 0; n2 < 2; ++n2) {
            f32x16 v = kh ? acc[2 + n2] : acc[n2];
#pragma unroll
            for (int qd = 0; qd < 4; ++qd) {
                const f32x4 t = *(const LAS f32x4*)(theirs + n2 * 4096 + qd * 1024);
                v[4 * qd] += t[0]; v[4 * qd + 1] += t[1]; v[4 * qd + 2] += t[2]; v[4 * qd + 3] += t[3];
            }
            uint16_t* op = out + (size_t)token * 2048 + b * 1024 + tn * 128 + (2 * kh + n2) * 32;
#pragma unroll
            for (int gp = 0; gp < 2; ++gp) {
                const unsigned a0 = cvtpk(v[8 * gp], v[8 * gp + 1]), a1 = cvtpk(v[8 * gp + 2], v[8 * gp + 3]);
                const unsigned b0 = cvtpk(v[8 * gp + 4], v[8 * gp + 5]), b1 = cvtpk(v[8 * gp + 6], v[8 * gp + 7]);
                auto r0 = __builtin_amdgcn_permlane32_swap(a0, b0, false, false);
                auto r1 = __builtin_amdgcn_permlane32_swap(a1, b1, false, false);
                const u32x4 w = {r0[0], r1[0], r0[1], r1[1]};
                *(u32x4*)(op + gp * 16 + hh * 8) = w;
            }
        }
    }
    STAMP_END(ws)
}

extern "C" void kernel_launch(void* const* d_in, const int* in_sizes, int n_in, void* d_out, int out_size, void* d_ws, size_t ws_size, hipStream_t stream) {
    (void)hipFuncSetAttribute((const void*)mha_fused, hipFuncAttributeMaxDynamicSharedMemorySize, L_END);
    mha_fused<<<256, 512, L_END, stream>>>((const uint16_t*)d_in[0], (const uint16_t*)d_in[1], (const uint16_t*)d_in[2], (const uint16_t*)d_in[3], (const uint16_t*)d_in[4],
                                           (const uint16_t*)d_in[5], (char*)d_ws, (uint16_t*)d_out);
}
```

```cpp
#include <hip/hip_runtime.h>
#include <stdint.h>

typedef __bf16 bf16x8 __attribute__((ext_vector_type(8)));
typedef __bf16 bf16x16 __attribute__((ext_vector_type(16)));
typedef __bf16 bf16x2 __attribute__((ext_vector_type(2)));
typedef short s16x4 __attribute__((ext_vector_type(4)));
typedef short s16x8 __attribute__((ext_vector_type(8)));
typedef float f32x16 __attribute__((ext_vector_type(16)));
typedef float f32x4 __attribute__((ext_vector_type(4)));
typedef float f32x2 __attribute__((ext_vector_type(2)));
typedef unsigned u32x4 __attribute__((ext_vector_type(4)));
typedef unsigned u32x8 __attribute__((ext_vector_type(8)));
typedef unsigned long long u64;
#define LAS __attribute__((address_space(3)))
#define GAS __attribute__((address_space(1)))
#define DI __device__ __forceinline__
#define MFMA32(a, b, c) __builtin_amdgcn_mfma_f32_32x32x16_bf16((a), (b), (c), 0, 0, 0)
#define SMF(a, b, c, idx) __builtin_amdgcn_smfmac_f32_16x16x64_bf16((a), __builtin_bit_cast(bf16x16, b), (c), (idx), 0, 0)
#define PLO(P) __builtin_bit_cast(bf16x8, __builtin_shufflevector(P, P, 0, 1, 2, 3))
#define PHI(P) __builtin_bit_cast(bf16x8, __builtin_shufflevector(P, P, 4, 5, 6, 7))
#define SBAR() __builtin_amdgcn_sched_barrier(0)
#define BF(x) __builtin_bit_cast(bf16x8, x)
#define NOP_ (void)0
typedef __amdgpu_buffer_rsrc_t srd_t;

#define MAPSEL 1
constexpr float QSCALE = 0.17677669529663687f * 1.4426950408889634f;
constexpr unsigned WS_KV = 0;
constexpr unsigned WS_O = 16u << 20;
constexpr unsigned WS_FL = 24u << 20;
constexpr unsigned FL_KV = 4096;
constexpr unsigned FL_O = FL_KV + 16384;
constexpr int L_S0 = 0, L_S1 = 32768, L_S2 = 65536, L_R0 = 98304, L_R1 = 131072, L_END = 163840, L_CNT = L_END - 64;
constexpr int P1_XS = 32768, P1_WS = 24576, P1_WOFF = 98304;

DI unsigned rfl(unsigned x) { return __builtin_amdgcn_readfirstlane(x); }
DI srd_t make_srd(const void* p, unsigned bytes) { return __builtin_amdgcn_make_buffer_rsrc((void*)p, (short)0, (int)bytes, 0x00020000); }
DI void dma16(unsigned ldsdst, unsigned voff, srd_t srd, unsigned soff) {
    asm volatile("s_mov_b32 m0, %0\n\ts_nop 0\n\tbuffer_load_dwordx4 %1, %2, %3 offen lds" :: "s"(ldsdst), "v"(voff), "s"(srd), "s"(soff) : "memory");
}
DI void dma16c(unsigned ldsdst, unsigned voff, srd_t srd, unsigned soff) {
    asm volatile("s_mov_b32 m0, %0\n\ts_nop 0\n\tbuffer_load_dwordx4 %1, %2, %3 offen sc1 lds" :: "s"(ldsdst), "v"(voff), "s"(srd), "s"(soff) : "memory");
}
DI unsigned cvtpk(float lo, float hi) { f32x2 v = {lo, hi}; bf16x2 b = __builtin_convertvector(v, bf16x2); return __builtin_bit_cast(unsigned, b); }
DI bf16x8 ldsfrag(const LAS char* p) { return *(const LAS bf16x8*)p; }
typedef short v4i16_t __attribute__((ext_vector_type(4)));
DI s16x4 ldstr(const LAS char* p) { return __builtin_amdgcn_ds_read_tr16_b64_v4i16((LAS v4i16_t*)p); }
DI bf16x8 cat8(s16x4 lo, s16x4 hi) { s16x8 r = __builtin_shufflevector(lo, hi, 0, 1, 2, 3, 4, 5, 6, 7); return __builtin_bit_cast(bf16x8, r); }
DI u32x4 pack8(const f32x16& x, int s, float mul) {
    u32x4 p; p[0] = cvtpk(x[8 * s] * mul, x[8 * s + 1] * mul); p[1] = cvtpk(x[8 * s + 2] * mul, x[8 * s + 3] * mul);
    p[2] = cvtpk(x[8 * s + 4] * mul, x[8 * s + 5] * mul); p[3] = cvtpk(x[8 * s + 6] * mul, x[8 * s + 7] * mul); return p; }
DI u32x4 pack8n(const f32x16& x, int s) {
    u32x4 p; p[0] = cvtpk(x[8 * s], x[8 * s + 1]); p[1] = cvtpk(x[8 * s + 2], x[8 * s + 3]);
    p[2] = cvtpk(x[8 * s + 4], x[8 * s + 5]); p[3] = cvtpk(x[8 * s + 6], x[8 * s + 7]); return p; }
#define WAITBAR(vm) asm volatile("s_waitcnt vmcnt(" #vm ") lgkmcnt(0)\n\ts_barrier" ::: "memory")
#define LBAR() asm volatile("s_waitcnt lgkmcnt(0)\n\ts_barrier" ::: "memory")
#define WAITV(vm) asm volatile("s_waitcnt vmcnt(" #vm ")" ::: "memory")
DI float ex2(float x) { return __builtin_amdgcn_exp2f(x); }
DI u64 mix64(u64 z) { z += 0x9E3779B97F4A7C15ull; z = (z ^ (z >> 30)) * 0xBF58476D1CE4E5B9ull; z = (z ^ (z >> 27)) * 0x94D049BB133111EBull; return z ^ (z >> 31); }
DI u64 ld_agent(const GAS u64* p) { return __hip_atomic_load(p, __ATOMIC_RELAXED, __HIP_MEMORY_SCOPE_AGENT); }
DI void st_agent(GAS u64* p, u64 v) { __hip_atomic_store(p, v, __ATOMIC_RELAXED, __HIP_MEMORY_SCOPE_AGENT); }
DI void spin_flags(const GAS u64* f, u64 tag) {
    const u64 t0 = __builtin_amdgcn_s_memrealtime();
    for (;;) {
        const u64 v = ld_agent(f);
        if (__all(v == tag)) break;
        if (__builtin_amdgcn_s_memrealtime() - t0 > 20000000ull) break;
        __builtin_amdgcn_s_sleep(1);
    }
    asm volatile("" ::: "memory");
}
DI void lds_signal(unsigned addr) {
    unsigned one = 1;
    asm volatile("s_waitcnt lgkmcnt(0)\n\ts_mov_b64 exec, 1\n\tds_add_u32 %0, %1\n\ts_mov_b64 exec, -1" :: "v"(addr), "v"(one) : "memory");
}
DI unsigned lds_peek(const LAS char* p) { return *(const volatile LAS unsigned*)p; }
DI void lds_await(unsigned seen, const LAS char* p, unsigned need) {
    if (rfl(seen) < need) {
        for (int it = 0; it < (1 << 22); ++it) { if (rfl(lds_peek(p)) >= need) break; __builtin_amdgcn_s_sleep(0); }
    }
    asm volatile("" ::: "memory");
}

#define STAMP_BEGIN
#define STAMP_MID(i)
#define STAMP_END(ws)

__global__ __launch_bounds__(512, 2) void mha_fused(const uint16_t* xA, const uint16_t* wA, const uint16_t* woA, const uint16_t* xB, const uint16_t* wB, const uint16_t* woB,
                                                    char* ws, uint16_t* out) {
    extern __shared__ __attribute__((aligned(16))) char lds_[];
    LAS char* lds = (LAS char*)lds_;
    STAMP_BEGIN
    const int tid = threadIdx.x, lane = tid & 63, wid = rfl(tid >> 6);
    const int bid = blockIdx.x, xcd = bid & 7, j = bid >> 3;
    const int b = xcd >> 2;
    const int mb = (xcd & 1) * 4 + (j & 3), hp = ((xcd >> 1) & 1) * 8 + (j >> 2);
    const int wm = wid & 3, wn = wid >> 2, h = hp * 2 + wn;
    const unsigned lds0 = (unsigned)(uintptr_t)lds_;
    const int r32 = lane & 31, hh = lane >> 5, g4 = lane >> 4, q4 = (lane >> 2) & 3, p4 = lane & 3;
    GAS u64* const flags = (GAS u64*)(ws + WS_FL);
    u64 epoch = ld_agent(flags + b * 32);
    if (tid < 16) *(LAS unsigned*)(lds + L_CNT + tid * 4) = 0u;

    bf16x8 qf[2][2];
    const srd_t skvw = make_srd(ws + WS_KV + (size_t)(b * 32 + h) * 64 * 4096 + (size_t)(mb * 8 + wm * 2) * 4096, 2 * 4096);
    {
        const srd_t sx = make_srd(b ? xB : xA, 2048 * 1024 * 2), sw = make_srd(b ? wB : wA, 1024 * 3072 * 2);
        const unsigned vx0 = (lane >> 3) * 2048 + (((lane & 7) ^ (lane >> 4)) << 4);
        const unsigned vx1 = (lane >> 3) * 2048 + (((lane & 7) ^ (4 + (lane >> 4))) << 4);
        const unsigned vw = (lane >> 3) * 6144 + (((lane & 7) ^ (((lane >> 4) & 1) << 2)) << 4);
        const unsigned sx0 = (mb * 256 + wid * 32) * 2048, sw0 = wid * 8 * 6144 + hp * 128;
        auto dx = [&](int kt, int i) { dma16(lds0 + (kt % 3) * P1_XS + wid * 4096 + i * 1024, (i & 1) ? vx1 : vx0, sx, sx0 + i * 8 * 2048 + kt * 128); };
        auto dw = [&](int kt, int i) { dma16(lds0 + P1_WOFF + (kt & 1) * P1_WS + wid * 1024 + i * 8192, vw, sw, sw0 + i * 2048 + kt * 64 * 6144); };
        for (int i = 0; i < 3; ++i) dw(0, i);
        for (int i = 0; i < 4; ++i) dx(0, i);
        for (int i = 0; i < 3; ++i) dw(1, i);
        for (int i = 0; i < 4; ++i) dx(1, i);
        for (int i = 0; i < 4; ++i) dx(2, i);
        unsigned xa[4];
#pragma unroll
        for (int s = 0; s < 4; ++s) xa[s] = (wm * 64 + r32) * 128 + (((2 * s + hh) ^ ((r32 >> 1) & 7)) << 4);
        const unsigned wa = P1_WOFF + (8 * (g4 >> 1) + q4) * 128 + ((wn ^ (q4 >> 1)) << 6) + (g4 & 1) * 32 + p4 * 8;
        f32x16 acc[2][3];
#pragma unroll
        for (int a = 0; a < 2; ++a)
#pragma unroll
            for (int c = 0; c < 3; ++c)
#pragma unroll
                for (int i = 0; i < 16; ++i) acc[a][c][i] = 0.f;
        bf16x8 xf[2][2], wf[2][3];
#define P1_RX(buf, kt, s) do { const LAS char* xp_ = lds + ((kt) % 3) * P1_XS + xa[s]; xf[buf][0] = ldsfrag(xp_); xf[buf][1] = ldsfrag(xp_ + 4096); } while (0)
#define P1_RW(buf, kt, s, c) do { const LAS char* wp_ = lds + wa + ((kt) & 1) * P1_WS + (s) * 2048 + (c) * 8192; wf[buf][c] = cat8(ldstr(wp_), ldstr(wp_ + 512)); } while (0)
#define P1_STEP(cur, nxt, RD, kt, s, D0, D1, D2) do { \
            acc[0][0] = MFMA32(wf[cur][0], xf[cur][0], acc[0][0]); if (RD) P1_RX(nxt, kt, s); SBAR(); \
            acc[0][1] = MFMA32(wf[cur][1], xf[cur][0], acc[0][1]); if (RD) P1_RW(nxt, kt, s, 0); SBAR(); \
            acc[0][2] = MFMA32(xf[cur][0], wf[cur][2], acc[0][2]); if (RD) P1_RW(nxt, kt, s, 1); D0; SBAR(); \
            acc[1][0] = MFMA32(wf[cur][0], xf[cur][1], acc[1][0]); if (RD) P1_RW(nxt, kt, s, 2); SBAR(); \
            acc[1][1] = MFMA32(wf[cur][1], xf[cur][1], acc[1][1]); D1; SBAR(); \
            acc[1][2] = MFMA32(xf[cur][1], wf[cur][2], acc[1][2]); D2; SBAR(); \
        } while (0)
        WAITBAR(11);
        STAMP_MID(0)
        P1_RX(0, 0, 0); P1_RW(0, 0, 0, 0); P1_RW(0, 0, 0, 1); P1_RW(0, 0, 0, 2); SBAR();
#pragma unroll
        for (int t = 0; t < 16; ++t) {
            const bool xs = (t >= 1 && t + 2 < 16);
            P1_STEP(0, 1, true, t, 1, if (xs) dx(t + 2, 0), if (xs) dx(t + 2, 1), NOP_);
            P1_STEP(1, 0, true, t, 2, if (xs) dx(t + 2, 2), if (xs) dx(t + 2, 3), NOP_);
            P1_STEP(0, 1, true, t, 3, NOP_, NOP_, NOP_);
            if (t < 15) {
                if (t < 14) WAITBAR(4); else WAITBAR(0);
                const bool wsch = (t + 2 < 16);
                P1_STEP(1, 0, true, t + 1, 0, if (wsch) dw(t + 2, 0), if (wsch) dw(t + 2, 1), if (wsch) dw(t + 2, 2));
            } else {
                P1_STEP(1, 0, false, 0, 0, NOP_, NOP_, NOP_);
            }
        }
        STAMP_MID(1)
        u32x4 kvp[2][4];
#pragma unroll
        for (int a = 0; a < 2; ++a)
#pragma unroll
            for (int s = 0; s < 2; ++s) {
                kvp[a][s] = pack8n(acc[a][1], s); kvp[a][2 + s] = pack8n(acc[a][2], s);
                __builtin_amdgcn_raw_buffer_store_b128(kvp[a][s], skvw, lane * 16 + a * 4096 + s * 1024, 0, 16);
                __builtin_amdgcn_raw_buffer_store_b128(kvp[a][2 + s], skvw, lane * 16 + a * 4096 + 2048 + s * 1024, 0, 16);
            }
        LBAR();
#pragma unroll
        for (int a = 0; a < 2; ++a) {
            const int tl = wm * 2 + a;
            LAS char* lp = lds + (tl >> 2) * 32768 + wn * 16384 + (tl & 3) * 4096 + lane * 16;
#pragma unroll
            for (int f = 0; f < 4; ++f) *(LAS u32x4*)(lp + f * 1024) = kvp[a][f];
        }
        SBAR();
#pragma unroll
        for (int a = 0; a < 2; ++a)
#pragma unroll
            for (int s = 0; s < 2; ++s) qf[a][s] = BF(pack8(acc[a][0], s, QSCALE));
    }
    asm volatile("" : "+v"(epoch));
    const u64 tag = mix64(((u64)rfl((unsigned)(epoch >> 32)) << 32) | rfl((unsigned)epoch));
    LBAR();
    STAMP_MID(2)

    const srd_t skv = make_srd(ws + WS_KV + (size_t)(b * 32 + hp * 2) * 64 * 4096, 2 * 64 * 4096);
    const srd_t swo = make_srd(b ? woB : woA, 1024 * 1024 * 2);
    const int tm = (xcd & 3) * 4 + (j >> 3), tn = j & 7;
    const unsigned vl = lane * 16;
    const unsigned vwo = (lane >> 4) * 2048 + (((lane & 15) ^ (((lane >> 4) & 3) << 2)) << 4);
    auto chunk_off = [&](int c) { const int i = (c - 2) >> 1; const int blk = c < 2 ? mb : (i < mb ? i : i + 1); return (unsigned)((wn * 64 + blk * 8 + (c & 1) * 4 + wm) * 4096); };
    const unsigned ldw = lds0 + wid * 4096;
    auto dkv = [&](unsigned coff, int slotoff, int i) { dma16c(ldw + slotoff + i * 1024, vl, skv, coff + i * 1024); };
    const unsigned wo0 = ((wid >> 2) * 512 + (wid & 3) * 16) * 2048 + tn * 256;
    auto dwo = [&](int step, int slotoff, int i) { dma16(ldw + slotoff + i * 1024, vwo, swo, wo0 + (step * 64 + i * 4) * 2048); };
    bf16x8 sel; int selidx;
    { const int i16 = lane & 15; const short lo = (i16 < 2) ? (short)0x3F80 : (short)0, hi = (i16 == 2 || i16 == 3) ? (short)0x3F80 : (short)0;
      s16x8 t = {lo, lo, lo, lo, hi, hi, hi, hi}; sel = __builtin_bit_cast(bf16x8, t); selidx = (i16 & 1) ? 0xEEEE : 0x4444; }
    const LAS char* kvb = lds + wn * 16384 + lane * 16;
    GAS u64* const kvfl = flags + (FL_KV / 8) + (b * 32 + h) * 32;
    const GAS u64* const kvmine = kvfl + ((lane >> 1) & 7) * 4 + (lane & 1) * 2 + (wm >> 1);
    const LAS char* cntp = lds + L_CNT + wn * 16;
    const LAS char* cnto = lds + L_CNT + (wn ^ 1) * 16;
    const unsigned cnta = lds0 + L_CNT + wn * 16;
    f32x16 o[2]; f32x4 lacc[2];
    {
        f32x16 negm[2], S[2];
        bf16x8 kf[2][2], vf[2][2];
        u32x8 P;
        kf[0][0] = ldsfrag(kvb); kf[0][1] = ldsfrag(kvb + 1024); vf[0][0] = ldsfrag(kvb + 2048); vf[0][1] = ldsfrag(kvb + 3072);
#pragma unroll
        for (int a = 0; a < 2; ++a) {
            f32x16 z;
#pragma unroll
            for (int i = 0; i < 16; ++i) z[i] = 0.f;
            z = MFMA32(kf[0][0], qf[a][0], z); z = MFMA32(kf[0][1], qf[a][1], z);
            float m = z[0];
#pragma unroll
            for (int i = 1; i < 16; ++i) m = fmaxf(m, z[i]);
            m = fmaxf(m, __shfl_xor(m, 32));
#pragma unroll
            for (int i = 0; i < 16; ++i) { negm[a][i] = -m; o[a][i] = 0.f; }
            lacc[a] = (f32x4){0.f, 0.f, 0.f, 0.f};
        }
        S[0] = MFMA32(kf[0][0], qf[0][0], negm[0]); S[0] = MFMA32(kf[0][1], qf[0][1], S[0]);
        P = (u32x8){0u, 0u, 0u, 0u, 0u, 0u, 0u, 0u};
        bf16x8 vprev1 = vf[0][1];
        unsigned seen;
#define EXPI(X, i) X[i] = ex2(X[i])
#define UNIT(SX, SN, KA0, KA1, QB0, QB1, NEG, DOQK, OPREV, LPREV, VPREV1, OCUR, VCUR0, H0, H1, H2, H3) do { \
            if (DOQK) SN = MFMA32(KA0, QB0, NEG); H0; SBAR(); \
            EXPI(SX, 0); EXPI(SX, 1); EXPI(SX, 2); SBAR(); \
            LPREV = SMF(sel, P, LPREV, selidx); SBAR(); \
            EXPI(SX, 3); P[0] = cvtpk(SX[0], SX[1]); P[1] = cvtpk(SX[2], SX[3]); SBAR(); \
            OPREV = MFMA32(VPREV1, PHI(P), OPREV); H1; SBAR(); \
            EXPI(SX, 4); EXPI(SX, 5); EXPI(SX, 6); EXPI(SX, 7); P[2] = cvtpk(SX[4], SX[5]); P[3] = cvtpk(SX[6], SX[7]); SBAR(); \
            if (DOQK) SN = MFMA32(KA1, QB1, SN); H2; SBAR(); \
            EXPI(SX, 8); EXPI(SX, 9); EXPI(SX, 10); EXPI(SX, 11); P[4] = cvtpk(SX[8], SX[9]); P[5] = cvtpk(SX[10], SX[11]); SBAR(); \
            OCUR = MFMA32(VCUR0, PLO(P), OCUR); H3; SBAR(); \
            EXPI(SX, 12); EXPI(SX, 13); EXPI(SX, 14); EXPI(SX, 15); P[6] = cvtpk(SX[12], SX[13]); P[7] = cvtpk(SX[14], SX[15]); SBAR(); \
        } while (0)
#define TILEX(cur, nxt, rdp, LOAD, A2, A3, B0, B1, B2, B3) do { \
            UNIT(S[0], S[1], kf[cur][0], kf[cur][1], qf[1][0], qf[1][1], negm[1], true, o[1], lacc[1], vprev1, o[0], vf[cur][0], \
                 if (LOAD) { kf[nxt][0] = ldsfrag(rdp); kf[nxt][1] = ldsfrag((rdp) + 1024); }, \
                 if (LOAD) { vf[nxt][0] = ldsfrag((rdp) + 2048); vf[nxt][1] = ldsfrag((rdp) + 3072); }, A2, A3); \
            UNIT(S[1], S[0], kf[nxt][0], kf[nxt][1], qf[0][0], qf[0][1], negm[0], LOAD, o[0], lacc[0], vf[cur][1], o[1], vf[cur][0], B0, B1, B2, B3); \
            vprev1 = vf[cur][1]; \
        } while (0)
#define TILE(cur, nxt, rdp, DA, DB, PK) TILEX(cur, nxt, rdp, true, DA, NOP_, NOP_, PK, DB, NOP_)
#define PEEK_RD seen = lds_peek(cntp + 4)
#define PEEK_LD seen = lds_peek(cntp)
#define FLIP 13
#define BALANCE(m) do { if (wn == 0 && (m) == FLIP) __builtin_amdgcn_s_setprio(0); } while (0)
        if (wn == 0) __builtin_amdgcn_s_setprio(2); else __builtin_amdgcn_s_setprio(1);
        TILE(0, 1, kvb + L_S0 + 4096, NOP_, NOP_, NOP_);
        TILE(1, 0, kvb + L_S0 + 8192, NOP_, NOP_, NOP_);
        TILE(0, 1, kvb + L_S0 + 12288, NOP_, NOP_, NOP_);
        STAMP_MID(8)
        WAITV(0); if (lane == 0) st_agent(kvfl + mb * 4 + wm, tag);
        STAMP_MID(9)
        lds_signal(cnta + 4);
        TILE(1, 0, kvb + L_S1, NOP_, NOP_, NOP_);
        u64 pollv = ld_agent(kvmine);
        TILE(0, 1, kvb + L_S1 + 4096, NOP_, NOP_, PEEK_RD);
        STAMP_MID(10)
        if (!__all(pollv == tag)) spin_flags(kvmine, tag);
        lds_await(seen, cntp + 4, 4);
        STAMP_MID(11)
        { const unsigned c2 = chunk_off(2); for (int i = 0; i < 4; ++i) dkv(c2, L_S2, i); }
        unsigned cn = chunk_off(3);
        TILEX(1, 0, kvb + L_S1 + 8192, true, dkv(cn, L_S0, 0), dkv(cn, L_S0, 1), NOP_, NOP_, dkv(cn, L_S0, 2), dkv(cn, L_S0, 3));
        TILE(0, 1, kvb + L_S1 + 12288, NOP_, NOP_, NOP_);
        WAITV(4); lds_signal(cnta); lds_await(0, cntp, 4);
        lds_signal(cnta + 4);
        TILE(1, 0, kvb + L_S2, NOP_, NOP_, PEEK_RD);
        STAMP_MID(7)
        int sc = L_S2, sn = L_S0, sp = L_S1;
        for (int m = 2; m <= 12; ++m) {
            const LAS char* cp = kvb + sc;
            cn = chunk_off(m + 2);
            lds_await(seen, cntp + 4, 4 * m);
            BALANCE(m);
            TILEX(0, 1, cp + 4096, true, dkv(cn, sp, 0), dkv(cn, sp, 1), NOP_, NOP_, dkv(cn, sp, 2), dkv(cn, sp, 3));
            TILE(1, 0, cp + 8192, NOP_, NOP_, NOP_);
            WAITV(4); lds_signal(cnta);
            TILE(0, 1, cp + 12288, NOP_, NOP_, PEEK_LD);
            lds_await(seen, cntp, 4 * m);
            lds_signal(cnta + 4);
            TILE(1, 0, kvb + sn, NOP_, NOP_, PEEK_RD);
            const int t_ = sc; sc = sn; sn = sp; sp = t_;
        }
        cn = chunk_off(15);
        lds_await(seen, cntp + 4, 52);
        BALANCE(13);
        TILEX(0, 1, kvb + L_S1 + 4096, true, dkv(cn, L_S0, 0), dkv(cn, L_S0, 1), NOP_, NOP_, dkv(cn, L_S0, 2), dkv(cn, L_S0, 3));
        TILE(1, 0, kvb + L_S1 + 8192, NOP_, NOP_, NOP_);
        WAITV(4); lds_signal(cnta);
        TILE(0, 1, kvb + L_S1 + 12288, NOP_, NOP_, PEEK_LD);
        lds_await(seen, cntp, 52);
        lds_signal(cnta + 4);
        TILEX(1, 0, kvb + L_S2, true, dwo(0, L_R0, 0), dwo(0, L_R0, 1), NOP_, PEEK_RD, dwo(0, L_R0, 2), dwo(0, L_R0, 3));
        lds_await(seen, cntp + 4, 56);
        BALANCE(14);
        TILEX(0, 1, kvb + L_S2 + 4096, true, dwo(2, L_S1, 0), dwo(2, L_S1, 1), NOP_, NOP_, dwo(2, L_S1, 2), dwo(2, L_S1, 3));
        TILE(1, 0, kvb + L_S2 + 8192, NOP_, NOP_, NOP_);
        WAITV(8); lds_signal(cnta);
        TILE(0, 1, kvb + L_S2 + 12288, dwo(1, L_R1, 0), dwo(1, L_R1, 1), PEEK_LD);
        lds_await(seen, cntp, 56);
        lds_signal(cnta + 4);
        TILE(1, 0, kvb + L_S0, dwo(1, L_R1, 2), if (wid != 7) dwo(1, L_R1, 3), PEEK_RD);
        lds_await(seen, cntp + 4, 60);
        TILEX(0, 1, kvb + L_S0 + 4096, true, dwo(3, L_S2, 0), dwo(3, L_S2, 1), NOP_, NOP_, dwo(3, L_S2, 2), dwo(3, L_S2, 3));
        TILE(1, 0, kvb + L_S0 + 8192, NOP_, NOP_, NOP_);
        TILE(0, 1, kvb + L_S0 + 12288, NOP_, NOP_, NOP_);
        TILEX(1, 0, kvb, false, NOP_, NOP_, NOP_, NOP_, NOP_, NOP_);
        lacc[1] = SMF(sel, P, lacc[1], selidx); o[1] = MFMA32(vprev1, PHI(P), o[1]);
    }
    __builtin_amdgcn_s_setprio(0);
    STAMP_MID(3)
    {
        float l[2];
        auto rowsum = [&](int a) { const float t0 = __shfl(lacc[a][0] + lacc[a][1], lane & 15), t1 = __shfl(lacc[a][2] + lacc[a][3], lane & 15); return (lane & 16) ? t1 : t0; };
        bool bad = false;
#pragma unroll
        for (int a = 0; a < 2; ++a) {
            l[a] = rowsum(a);
            float z = l[a] * 0.f, z1 = 0.f;
#pragma unroll
            for (int i = 0; i < 16; i += 2) { z = __builtin_fmaf(o[a][i], 0.f, z); z1 = __builtin_fmaf(o[a][i + 1], 0.f, z1); }
            bad |= !(l[a] < 1.0e18f) || !(z + z1 == 0.f);
        }
        if (__any(bad)) {
            spin_flags(kvfl + (lane & 31), tag);
            const srd_t sk1 = make_srd(ws + WS_KV + (size_t)(b * 32 + h) * 64 * 4096, 64 * 4096);
#define LDKV(t, f) BF(__builtin_amdgcn_raw_buffer_load_b128(sk1, lane * 16 + (f) * 1024, (t) * 4096, 16))
            f32x16 negm[2];
#pragma unroll
            for (int a = 0; a < 2; ++a) {
                float m = -__builtin_inff();
                for (int t = 0; t < 64; ++t) {
                    const bf16x8 k0 = LDKV(t, 0), k1 = LDKV(t, 1);
                    f32x16 z;
#pragma unroll
                    for (int i = 0; i < 16; ++i) z[i] = 0.f;
                    z = MFMA32(k0, qf[a][0], z); z = MFMA32(k1, qf[a][1], z);
#pragma unroll
                    for (int i = 0; i < 16; ++i) m = fmaxf(m, z[i]);
                }
                m = fmaxf(m, __shfl_xor(m, 32));
#pragma unroll
                for (int i = 0; i < 16; ++i) { negm[a][i] = -m; o[a][i] = 0.f; }
                lacc[a] = (f32x4){0.f, 0.f, 0.f, 0.f};
            }
            for (int t = 0; t < 64; ++t) {
                const bf16x8 k0 = LDKV(t, 0), k1 = LDKV(t, 1), v0 = LDKV(t, 2), v1 = LDKV(t, 3);
#pragma unroll
                for (int a = 0; a < 2; ++a) {
                    f32x16 z = MFMA32(k0, qf[a][0], negm[a]); z = MFMA32(k1, qf[a][1], z);
                    u32x8 P;
#pragma unroll
                    for (int i = 0; i < 8; ++i) P[i] = cvtpk(ex2(z[2 * i]), ex2(z[2 * i + 1]));
                    o[a] = MFMA32(v0, PLO(P), o[a]); o[a] = MFMA32(v1, PHI(P), o[a]); lacc[a] = SMF(sel, P, lacc[a], selidx);
                }
            }
            l[0] = rowsum(0); l[1] = rowsum(1);
        }
        const srd_t sow = make_srd(ws + WS_O + (size_t)(b * 64 + mb * 8 + wm * 2) * 65536, 2 * 65536);
#pragma unroll
        for (int a = 0; a < 2; ++a) {
            const float rl = __builtin_amdgcn_rcpf(l[a]);
            __builtin_amdgcn_raw_buffer_store_b128(pack8(o[a], 0, rl), sow, lane * 16 + a * 65536 + h * 2048, 0, 16);
            __builtin_amdgcn_raw_buffer_store_b128(pack8(o[a], 1, rl), sow, lane * 16 + a * 65536 + h * 2048 + 1024, 0, 16);
        }
    }
    GAS u64* const ofl = flags + (FL_O / 8) + b * 1024;
    WAITV(0); if (lane == 0) st_agent(ofl + (mb * 4 + wm) * 32 + h, tag);
    STAMP_MID(4)

    {
        const int wt = wid & 3, kh = wid >> 2;
        LBAR();
        if (wid == 7) dwo(1, L_R1, 3);
        spin_flags(ofl + (tm * 2 + (wt >> 1)) * 32 + kh * 16 + (lane & 15), tag);
        STAMP_MID(5)
        const srd_t sor = make_srd(ws + WS_O + (size_t)(b * 64 + tm * 4 + wt) * 65536 + kh * 32768, 32768);
        bf16x8 of[4][4], wf[2][4];
#define P3AUX 16
#define P3_LO1(i, ks) of[(i) & 3][ks] = BF(__builtin_amdgcn_raw_buffer_load_b128(sor, lane * 16, (i) * 4096 + (ks) * 1024, P3AUX))
#define P3_LO(i) do { P3_LO1(i, 0); P3_LO1(i, 1); P3_LO1(i, 2); P3_LO1(i, 3); } while (0)
        P3_LO(0); P3_LO(1); P3_LO(2); P3_LO(3);
        for (int i = 0; i < 4; ++i) dwo(4, L_S0, i);
        const unsigned wa3 = kh * 16384 + (4 * (g4 >> 1) + q4) * 256 + (g4 & 1) * 32 + p4 * 8;
        unsigned wn3[4];
#pragma unroll
        for (int n = 0; n < 4; ++n) wn3[n] = wa3 + ((n ^ q4) << 6);
        f32x16 acc[4];
#pragma unroll
        for (int n = 0; n < 4; ++n)
#pragma unroll
            for (int i = 0; i < 16; ++i) acc[n][i] = 0.f;
#define P3_RW(buf, slot, ks, n) do { const LAS char* w_ = lds + (slot) + (ks) * 4096 + wn3[n]; wf[buf][n] = cat8(ldstr(w_), ldstr(w_ + 2048)); } while (0)
#define P3_KS(cur, nxt, OF, RD, slot, ks, M0, M1, M2, M3) do { \
            acc[0] = MFMA32(wf[cur][0], OF, acc[0]); if (RD) P3_RW(nxt, slot, ks, 0); M0; SBAR(); \
            acc[1] = MFMA32(wf[cur][1], OF, acc[1]); if (RD) P3_RW(nxt, slot, ks, 1); M1; SBAR(); \
            acc[2] = MFMA32(wf[cur][2], OF, acc[2]); if (RD) P3_RW(nxt, slot, ks, 2); M2; SBAR(); \
            acc[3] = MFMA32(wf[cur][3], OF, acc[3]); if (RD) P3_RW(nxt, slot, ks, 3); M3; SBAR(); \
        } while (0)
#define P3_STEP(i, SL, SN, WAIT, LAST) do { \
            P3_KS(0, 1, of[(i) & 3][0], true, SL, 1, if ((i) >= 1 && (i) + 3 < 8) P3_LO1((i) + 3, 0), if ((i) >= 1 && (i) + 3 < 8) P3_LO1((i) + 3, 1), \
                  if ((i) >= 1 && (i) + 3 < 8) P3_LO1((i) + 3, 2), if ((i) >= 1 && (i) + 3 < 8) P3_LO1((i) + 3, 3)); \
            P3_KS(1, 0, of[(i) & 3][1], true, SL, 2, NOP_, NOP_, NOP_, NOP_); \
            P3_KS(0, 1, of[(i) & 3][2], true, SL, 3, NOP_, NOP_, NOP_, NOP_); \
            if (!(LAST)) { WAIT; } \
            P3_KS(1, 0, of[(i) & 3][3], !(LAST), SN, 0, if ((i) + 5 < 8) dwo((i) + 5, SL, 0), if ((i) + 5 < 8) dwo((i) + 5, SL, 1), if ((i) + 5 < 8) dwo((i) + 5, SL, 2), if ((i) + 5 < 8) dwo((i) + 5, SL, 3)); \
        } while (0)
        P3_RW(0, L_R0, 0, 0); P3_RW(0, L_R0, 0, 1); P3_RW(0, L_R0, 0, 2); P3_RW(0, L_R0, 0, 3); SBAR();
        P3_STEP(0, L_R0, L_R1, LBAR(), false);
        if (tid == 0 && (xcd & 3) == 0 && j == 0) st_agent(flags + b * 32, tag);
        P3_STEP(1, L_R1, L_S1, LBAR(), false);
        P3_STEP(2, L_S1, L_S2, LBAR(), false);
        P3_STEP(3, L_S2, L_S0, WAITBAR(24), false);
        P3_STEP(4, L_S0, L_R0, WAITBAR(24), false);
        P3_STEP(5, L_R0, L_R1, WAITBAR(16), false);
        P3_STEP(6, L_R1, L_S1, WAITBAR(8), false);
        P3_STEP(7, L_S1, L_S1, NOP_, true);
        STAMP_MID(6)
        LAS char* mine = lds + (kh ? L_S2 : L_S0) + wt * 8192 + lane * 16;
        const LAS char* theirs = lds + (kh ? L_S0 : L_S2) + wt * 8192 + lane * 16;
        if (kh) { const f32x16 t0 = acc[0], t1 = acc[1]; acc[0] = acc[2]; acc[1] = acc[3]; acc[2] = t0; acc[3] = t1; }
#pragma unroll
        for (int n2 = 0; n2 < 2; ++n2)
#pragma unroll
            for (int qd = 0; qd < 4; ++qd)
                *(LAS f32x4*)(mine + n2 * 4096 + qd * 1024) = (f32x4){acc[2 + n2][4 * qd], acc[2 + n2][4 * qd + 1], acc[2 + n2][4 * qd + 2], acc[2 + n2][4 * qd + 3]};
        LBAR();
        const int token = tm * 128 + wt * 32 + r32;
#pragma unroll
        for (int n2 = 0; n2 < 2; ++n2) {
            f32x16 v = acc[n2];
#pragma unroll
            for (int qd = 0; qd < 4; ++qd) {
                const f32x4 t = *(const LAS f32x4*)(theirs + n2 * 4096 + qd * 1024);
                v[4 * qd] += t[0]; v[4 * qd + 1] += t[1]; v[4 * qd + 2] += t[2]; v[4 * qd + 3] += t[3];
            }
            uint16_t* op = out + (size_t)token * 2048 + b * 1024 + tn * 128 + (2 * kh + n2) * 32;
#pragma unroll
            for (int gp = 0; gp < 2; ++gp) {
                const unsigned a0 = cvtpk(v[8 * gp], v[8 * gp + 1]), a1 = cvtpk(v[8 * gp + 2], v[8 * gp + 3]);
                const unsigned b0 = cvtpk(v[8 * gp + 4], v[8 * gp + 5]), b1 = cvtpk(v[8 * gp + 6], v[8 * gp + 7]);
                auto r0 = __builtin_amdgcn_permlane32_swap(a0, b0, false, false);
                auto r1 = __builtin_amdgcn_permlane32_swap(a1, b1, false, false);
                const u32x4 w = {r0[0], r1[0], r0[1], r1[1]};
                *(u32x4*)(op + gp * 16 + hh * 8) = w;
            }
        }
    }
    STAMP_END(ws)
}

extern "C" void kernel_launch(void* const* d_in, const int* in_sizes, int n_in, void* d_out, int out_size, void* d_ws, size_t ws_size, hipStream_t stream) {
    (void)hipFuncSetAttribute((const void*)mha_fused, hipFuncAttributeMaxDynamicSharedMemorySize, L_END);
    mha_fused<<<256, 512, L_END, stream>>>((const uint16_t*)d_in[0], (const uint16_t*)d_in[1], (const uint16_t*)d_in[2], (const uint16_t*)d_in[3], (const uint16_t*)d_in[4],
                                           (const uint16_t*)d_in[5], (char*)d_ws, (uint16_t*)d_out);
}
```

```cpp
#include <hip/hip_runtime.h>
#include <stdint.h>

typedef __bf16 bf16x8 __attribute__((ext_vector_type(8)));
typedef __bf16 bf16x16 __attribute__((ext_vector_type(16)));
typedef __bf16 bf16x2 __attribute__((ext_vector_type(2)));
typedef short s16x4 __attribute__((ext_vector_type(4)));
typedef short s16x8 __attribute__((ext_vector_type(8)));
typedef float f32x16 __attribute__((ext_vector_type(16)));
typedef float f32x4 __attribute__((ext_vector_type(4)));
typedef float f32x2 __attribute__((ext_vector_type(2)));
typedef unsigned u32x4 __attribute__((ext_vector_type(4)));
typedef unsigned u32x8 __attribute__((ext_vector_type(8)));
typedef unsigned long long u64;
#define LAS __attribute__((address_space(3)))
#define GAS __attribute__((address_space(1)))
#define DI __device__ __forceinline__
#define MFMA32(a, b, c) __builtin_amdgcn_mfma_f32_32x32x16_bf16((a), (b), (c), 0, 0, 0)
#define SMF(a, b, c, idx) __builtin_amdgcn_smfmac_f32_16x16x64_bf16((a), __builtin_bit_cast(bf16x16, b), (c), (idx), 0, 0)
#define PLO(P) __builtin_bit_cast(bf16x8, __builtin_shufflevector(P, P, 0, 1, 2, 3))
#define PHI(P) __builtin_bit_cast(bf16x8, __builtin_shufflevector(P, P, 4, 5, 6, 7))
#define SBAR() __builtin_amdgcn_sched_barrier(0)
#define BF(x) __builtin_bit_cast(bf16x8, x)
#define NOP_ (void)0
typedef __amdgpu_buffer_rsrc_t srd_t;

#define OUTAUX 0
#define MAPSEL 1
constexpr float QSCALE = 0.17677669529663687f * 1.4426950408889634f;
constexpr unsigned WS_KV = 0;
constexpr unsigned WS_O = 16u << 20;
constexpr unsigned WS_FL = 24u << 20;
constexpr unsigned FL_KV = 4096;
constexpr unsigned FL_O = FL_KV + 16384;
constexpr int L_S0 = 0, L_S1 = 32768, L_S2 = 65536, L_R0 = 98304, L_R1 = 131072, L_END = 163840, L_CNT = L_END - 64;
constexpr int P1_XS = 32768, P1_WS = 24576, P1_WOFF = 98304;

DI unsigned rfl(unsigned x) { return __builtin_amdgcn_readfirstlane(x); }
DI srd_t make_srd(const void* p, unsigned bytes) { return __builtin_amdgcn_make_buffer_rsrc((void*)p, (short)0, (int)bytes, 0x00020000); }
DI void dma16(unsigned ldsdst, unsigned voff, srd_t srd, unsigned soff) {
    asm volatile("s_mov_b32 m0, %0\n\ts_nop 0\n\tbuffer_load_dwordx4 %1, %2, %3 offen lds" :: "s"(ldsdst), "v"(voff), "s"(srd), "s"(soff) : "memory");
}
DI void dma16c(unsigned ldsdst, unsigned voff, srd_t srd, unsigned soff) {
    asm volatile("s_mov_b32 m0, %0\n\ts_nop 0\n\tbuffer_load_dwordx4 %1, %2, %3 offen sc1 lds" :: "s"(ldsdst), "v"(voff), "s"(srd), "s"(soff) : "memory");
}
DI unsigned cvtpk(float lo, float hi) { f32x2 v = {lo, hi}; bf16x2 b = __builtin_convertvector(v, bf16x2); return __builtin_bit_cast(unsigned, b); }
DI bf16x8 ldsfrag(const LAS char* p) { return *(const LAS bf16x8*)p; }
typedef short v4i16_t __attribute__((ext_vector_type(4)));
DI s16x4 ldstr(const LAS char* p) { return __builtin_amdgcn_ds_read_tr16_b64_v4i16((LAS v4i16_t*)p); }
DI bf16x8 cat8(s16x4 lo, s16x4 hi) { s16x8 r = __builtin_shufflevector(lo, hi, 0, 1, 2, 3, 4, 5, 6, 7); return __builtin_bit_cast(bf16x8, r); }
DI u32x4 pack8(const f32x16& x, int s, float mul) {
    u32x4 p; p[0] = cvtpk(x[8 * s] * mul, x[8 * s + 1] * mul); p[1] = cvtpk(x[8 * s + 2] * mul, x[8 * s + 3] * mul);
    p[2] = cvtpk(x[8 * s + 4] * mul, x[8 * s + 5] * mul); p[3] = cvtpk(x[8 * s + 6] * mul, x[8 * s + 7] * mul); return p; }
DI u32x4 pack8n(const f32x16& x, int s) {
    u32x4 p; p[0] = cvtpk(x[8 * s], x[8 * s + 1]); p[1] = cvtpk(x[8 * s + 2], x[8 * s + 3]);
    p[2] = cvtpk(x[8 * s + 4], x[8 * s + 5]); p[3] = cvtpk(x[8 * s + 6], x[8 * s + 7]); return p; }
#define WAITBAR(vm) asm volatile("s_waitcnt vmcnt(" #vm ") lgkmcnt(0)\n\ts_barrier" ::: "memory")
#define LBAR() asm volatile("s_waitcnt lgkmcnt(0)\n\ts_barrier" ::: "memory")
#define WAITV(vm) asm volatile("s_waitcnt vmcnt(" #vm ")" ::: "memory")
DI float ex2(float x) { return __builtin_amdgcn_exp2f(x); }
DI u64 mix64(u64 z) { z += 0x9E3779B97F4A7C15ull; z = (z ^ (z >> 30)) * 0xBF58476D1CE4E5B9ull; z = (z ^ (z >> 27)) * 0x94D049BB133111EBull; return z ^ (z >> 31); }
DI u64 ld_agent(const GAS u64* p) { return __hip_atomic_load(p, __ATOMIC_RELAXED, __HIP_MEMORY_SCOPE_AGENT); }
DI void st_agent(GAS u64* p, u64 v) { __hip_atomic_store(p, v, __ATOMIC_RELAXED, __HIP_MEMORY_SCOPE_AGENT); }
DI void spin_flags(const GAS u64* f, u64 tag) {
    const u64 t0 = __builtin_amdgcn_s_memrealtime();
    for (;;) {
        const u64 v = ld_agent(f);
        if (__all(v == tag)) break;
        if (__builtin_amdgcn_s_memrealtime() - t0 > 20000000ull) break;
        __builtin_amdgcn_s_sleep(1);
    }
    asm volatile("" ::: "memory");
}
DI void lds_signal(unsigned addr) {
    unsigned one = 1;
    asm volatile("s_waitcnt lgkmcnt(0)\n\ts_mov_b64 exec, 1\n\tds_add_u32 %0, %1\n\ts_mov_b64 exec, -1" :: "v"(addr), "v"(one) : "memory");
}
DI unsigned lds_peek(const LAS char* p) { return *(const volatile LAS unsigned*)p; }
DI void lds_await(unsigned seen, const LAS char* p, unsigned need) {
    if (rfl(seen) < need) {
        _Pragma("nounroll") for (int it = 0; it < (1 << 22); ++it) { if (rfl(lds_peek(p)) >= need) break; __builtin_amdgcn_s_sleep(0); }
    }
    asm volatile("" ::: "memory");
}

#define STAMP_BEGIN
#define STAMP_MID(i)
#define STAMP_END(ws)

__global__ __launch_bounds__(512, 2) void mha_fused(const uint16_t* xA, const uint16_t* wA, const uint16_t* woA, const uint16_t* xB, const uint16_t* wB, const uint16_t* woB,
                                                    char* ws, uint16_t* out) {
    extern __shared__ __attribute__((aligned(16))) char lds_[];
    LAS char* lds = (LAS char*)lds_;
    STAMP_BEGIN
    const int tid = threadIdx.x, lane = tid & 63, wid = rfl(tid >> 6);
    const int bid = blockIdx.x, xcd = bid & 7, j = bid >> 3;
    const int b = xcd >> 2;
    const int mb = (xcd & 1) * 4 + (j & 3), hp = ((xcd >> 1) & 1) * 8 + (j >> 2);
    const int wm = wid & 3, wn = wid >> 2, h = hp * 2 + wn;
    const unsigned lds0 = (unsigned)(uintptr_t)lds_;
    const int r32 = lane & 31, hh = lane >> 5, g4 = lane >> 4, q4 = (lane >> 2) & 3, p4 = lane & 3;
    GAS u64* const flags = (GAS u64*)(ws + WS_FL);
    u64 epoch = ld_agent(flags + b * 32);
    if (tid < 16) *(LAS unsigned*)(lds + L_CNT + tid * 4) = 0u;

    bf16x8 qf[2][2];
    const srd_t skvw = make_srd(ws + WS_KV + (size_t)(b * 32 + h) * 64 * 4096 + (size_t)(mb * 8 + wm * 2) * 4096, 2 * 4096);
    {
        const srd_t sx = make_srd(b ? xB : xA, 2048 * 1024 * 2), sw = make_srd(b ? wB : wA, 1024 * 3072 * 2);
        const unsigned vx0 = (lane >> 3) * 2048 + (((lane & 7) ^ (lane >> 4)) << 4);
        const unsigned vx1 = (lane >> 3) * 2048 + (((lane & 7) ^ (4 + (lane >> 4))) << 4);
        const unsigned vw = (lane >> 3) * 6144 + (((lane & 7) ^ (((lane >> 4) & 1) << 2)) << 4);
        const unsigned sx0 = (mb * 256 + wid * 32) * 2048, sw0 = wid * 8 * 6144 + hp * 128;
        auto dx = [&](int kt, int i) { dma16(lds0 + (kt % 3) * P1_XS + wid * 4096 + i * 1024, (i & 1) ? vx1 : vx0, sx, sx0 + i * 8 * 2048 + kt * 128); };
        auto dw = [&](int kt, int i) { dma16(lds0 + P1_WOFF + (kt & 1) * P1_WS + wid * 1024 + i * 8192, vw, sw, sw0 + i * 2048 + kt * 64 * 6144); };
        for (int i = 0; i < 3; ++i) dw(0, i);
        for (int i = 0; i < 4; ++i) dx(0, i);
        for (int i = 0; i < 3; ++i) dw(1, i);
        for (int i = 0; i < 4; ++i) dx(1, i);
        for (int i = 0; i < 4; ++i) dx(2, i);
        unsigned xa[4];
#pragma unroll
        for (int s = 0; s < 4; ++s) xa[s] = (wm * 64 + r32) * 128 + (((2 * s + hh) ^ ((r32 >> 1) & 7)) << 4);
        const unsigned wa = P1_WOFF + (8 * (g4 >> 1) + q4) * 128 + ((wn ^ (q4 >> 1)) << 6) + (g4 & 1) * 32 + p4 * 8;
        f32x16 acc[2][3];
#pragma unroll
        for (int a = 0; a < 2; ++a)
#pragma unroll
            for (int c = 0; c < 3; ++c)
#pragma unroll
                for (int i = 0; i < 16; ++i) acc[a][c][i] = 0.f;
        bf16x8 xf[2][2], wf[2][3];
#define P1_RX(buf, kt, s) do { const LAS char* xp_ = lds + ((kt) % 3) * P1_XS + xa[s]; xf[buf][0] = ldsfrag(xp_); xf[buf][1] = ldsfrag(xp_ + 4096); } while (0)
#define P1_RW(buf, kt, s, c) do { const LAS char* wp_ = lds + wa + ((kt) & 1) * P1_WS + (s) * 2048 + (c) * 8192; wf[buf][c] = cat8(ldstr(wp_), ldstr(wp_ + 512)); } while (0)
#define P1_STEP(cur, nxt, RD, kt, s, D0, D1, D2) do { \
            acc[0][0] = MFMA32(wf[cur][0], xf[cur][0], acc[0][0]); if (RD) P1_RX(nxt, kt, s); SBAR(); \
            acc[0][1] = MFMA32(wf[cur][1], xf[cur][0], acc[0][1]); if (RD) P1_RW(nxt, kt, s, 0); SBAR(); \
            acc[0][2] = MFMA32(xf[cur][0], wf[cur][2], acc[0][2]); if (RD) P1_RW(nxt, kt, s, 1); D0; SBAR(); \
            acc[1][0] = MFMA32(wf[cur][0], xf[cur][1], acc[1][0]); if (RD) P1_RW(nxt, kt, s, 2); SBAR(); \
            acc[1][1] = MFMA32(wf[cur][1], xf[cur][1], acc[1][1]); D1; SBAR(); \
            acc[1][2] = MFMA32(xf[cur][1], wf[cur][2], acc[1][2]); D2; SBAR(); \
        } while (0)
        WAITBAR(11);
        STAMP_MID(0)
        P1_RX(0, 0, 0); P1_RW(0, 0, 0, 0); P1_RW(0, 0, 0, 1); P1_RW(0, 0, 0, 2); SBAR();
#pragma unroll
        for (int t = 0; t < 16; ++t) {
            const bool xs = (t >= 1 && t + 2 < 16);
            P1_STEP(0, 1, true, t, 1, if (xs) dx(t + 2, 0), if (xs) dx(t + 2, 1), NOP_);
            P1_STEP(1, 0, true, t, 2, if (xs) dx(t + 2, 2), if (xs) dx(t + 2, 3), NOP_);
            P1_STEP(0, 1, true, t, 3, NOP_, NOP_, NOP_);
            if (t < 15) {
                if (t < 14) WAITBAR(4); else WAITBAR(0);
                const bool wsch = (t + 2 < 16);
                P1_STEP(1, 0, true, t + 1, 0, if (wsch) dw(t + 2, 0), if (wsch) dw(t + 2, 1), if (wsch) dw(t + 2, 2));
            } else {
                P1_STEP(1, 0, false, 0, 0, NOP_, NOP_, NOP_);
            }
        }
        STAMP_MID(1)
        u32x4 kvp[2][4];
#pragma unroll
        for (int a = 0; a < 2; ++a)
#pragma unroll
            for (int s = 0; s < 2; ++s) {
                kvp[a][s] = pack8n(acc[a][1], s); kvp[a][2 + s] = pack8n(acc[a][2], s);
                __builtin_amdgcn_raw_buffer_store_b128(kvp[a][s], skvw, lane * 16 + a * 4096 + s * 1024, 0, 16);
                __builtin_amdgcn_raw_buffer_store_b128(kvp[a][2 + s], skvw, lane * 16 + a * 4096 + 2048 + s * 1024, 0, 16);
            }
        LBAR();
#pragma unroll
        for (int a = 0; a < 2; ++a) {
            const int tl = wm * 2 + a;
            LAS char* lp = lds + (tl >> 2) * 32768 + wn * 16384 + (tl & 3) * 4096 + lane * 16;
#pragma unroll
            for (int f = 0; f < 4; ++f) *(LAS u32x4*)(lp + f * 1024) = kvp[a][f];
        }
        SBAR();
#pragma unroll
        for (int a = 0; a < 2; ++a)
#pragma unroll
            for (int s = 0; s < 2; ++s) qf[a][s] = BF(pack8(acc[a][0], s, QSCALE));
    }
    asm volatile("" : "+v"(epoch));
    const u64 tag = mix64(((u64)rfl((unsigned)(epoch >> 32)) << 32) | rfl((unsigned)epoch));
    LBAR();
    STAMP_MID(2)

    const srd_t skv = make_srd(ws + WS_KV + (size_t)(b * 32 + hp * 2) * 64 * 4096, 2 * 64 * 4096);
    const srd_t swo = make_srd(b ? woB : woA, 1024 * 1024 * 2);
    const int tm = (xcd & 3) * 4 + (j >> 3), tn = j & 7;
    const unsigned vl = lane * 16;
    const unsigned vwo = (lane >> 4) * 2048 + (((lane & 15) ^ (((lane >> 4) & 3) << 2)) << 4);
    auto chunk_off = [&](int c) { const int i = (c - 2) >> 1; const int blk = c < 2 ? mb : (i < mb ? i : i + 1); return (unsigned)((wn * 64 + blk * 8 + (c & 1) * 4 + wm) * 4096); };
    const unsigned ldw = lds0 + wid * 4096;
    auto dkv = [&](unsigned coff, int slotoff, int i) { dma16c(ldw + slotoff + i * 1024, vl, skv, coff + i * 1024); };
    const unsigned wo0 = ((wid >> 2) * 512 + (wid & 3) * 16) * 2048 + tn * 256;
    auto dwo = [&](int step, int slotoff, int i) { dma16(ldw + slotoff + i * 1024, vwo, swo, wo0 + (step * 64 + i * 4) * 2048); };
    bf16x8 sel; int selidx;
    { const int i16 = lane & 15; const short lo = (i16 < 2) ? (short)0x3F80 : (short)0, hi = (i16 == 2 || i16 == 3) ? (short)0x3F80 : (short)0;
      s16x8 t = {lo, lo, lo, lo, hi, hi, hi, hi}; sel = __builtin_bit_cast(bf16x8, t); selidx = (i16 & 1) ? 0xEEEE : 0x4444; }
    const LAS char* kvb = lds + wn * 16384 + lane * 16;
    GAS u64* const kvfl = flags + (FL_KV / 8) + (b * 32 + h) * 32;
    const GAS u64* const kvmine = kvfl + ((lane >> 1) & 7) * 4 + (lane & 1) * 2 + (wm >> 1);
    const LAS char* cntp = lds + L_CNT + wn * 16;
    const LAS char* cnto = lds + L_CNT + (wn ^ 1) * 16;
    const unsigned cnta = lds0 + L_CNT + wn * 16;
    f32x16 o[2]; f32x4 lacc[2];
    {
        f32x16 negm[2], S[2];
        bf16x8 kf[2][2], vf[2][2];
        u32x8 P;
        kf[0][0] = ldsfrag(kvb); kf[0][1] = ldsfrag(kvb + 1024); vf[0][0] = ldsfrag(kvb + 2048); vf[0][1] = ldsfrag(kvb + 3072);
#pragma unroll
        for (int a = 0; a < 2; ++a) {
            f32x16 z;
#pragma unroll
            for (int i = 0; i < 16; ++i) z[i] = 0.f;
            z = MFMA32(kf[0][0], qf[a][0], z); z = MFMA32(kf[0][1], qf[a][1], z);
            float m = z[0];
#pragma unroll
            for (int i = 1; i < 16; ++i) m = fmaxf(m, z[i]);
            m = fmaxf(m, __shfl_xor(m, 32));
#pragma unroll
            for (int i = 0; i < 16; ++i) { negm[a][i] = -m; o[a][i] = 0.f; }
            lacc[a] = (f32x4){0.f, 0.f, 0.f, 0.f};
        }
        S[0] = MFMA32(kf[0][0], qf[0][0], negm[0]); S[0] = MFMA32(kf[0][1], qf[0][1], S[0]);
        P = (u32x8){0u, 0u, 0u, 0u, 0u, 0u, 0u, 0u};
        bf16x8 vprev1 = vf[0][1];
        unsigned seen;
#define EXPI(X, i) X[i] = ex2(X[i])
#define UNIT(SX, SN, KA0, KA1, QB0, QB1, NEG, DOQK, OPREV, LPREV, VPREV1, OCUR, VCUR0, H0, H1, H2, H3) do { \
            if (DOQK) SN = MFMA32(KA0, QB0, NEG); H0; SBAR(); \
            EXPI(SX, 0); EXPI(SX, 1); EXPI(SX, 2); SBAR(); \
            LPREV = SMF(sel, P, LPREV, selidx); SBAR(); \
            EXPI(SX, 3); P[0] = cvtpk(SX[0], SX[1]); P[1] = cvtpk(SX[2], SX[3]); SBAR(); \
            OPREV = MFMA32(VPREV1, PHI(P), OPREV); H1; SBAR(); \
            EXPI(SX, 4); EXPI(SX, 5); EXPI(SX, 6); EXPI(SX, 7); P[2] = cvtpk(SX[4], SX[5]); P[3] = cvtpk(SX[6], SX[7]); SBAR(); \
            if (DOQK) SN = MFMA32(KA1, QB1, SN); H2; SBAR(); \
            EXPI(SX, 8); EXPI(SX, 9); EXPI(SX, 10); EXPI(SX, 11); P[4] = cvtpk(SX[8], SX[9]); P[5] = cvtpk(SX[10], SX[11]); SBAR(); \
            OCUR = MFMA32(VCUR0, PLO(P), OCUR); H3; SBAR(); \
            EXPI(SX, 12); EXPI(SX, 13); EXPI(SX, 14); EXPI(SX, 15); P[6] = cvtpk(SX[12], SX[13]); P[7] = cvtpk(SX[14], SX[15]); SBAR(); \
        } while (0)
#define TILEX(cur, nxt, rdp, LOAD, A2, A3, B0, B1, B2, B3) do { \
            UNIT(S[0], S[1], kf[cur][0], kf[cur][1], qf[1][0], qf[1][1], negm[1], true, o[1], lacc[1], vprev1, o[0], vf[cur][0], \
                 if (LOAD) { kf[nxt][0] = ldsfrag(rdp); kf[nxt][1] = ldsfrag((rdp) + 1024); }, \
                 if (LOAD) { vf[nxt][0] = ldsfrag((rdp) + 2048); vf[nxt][1] = ldsfrag((rdp) + 3072); }, A2, A3); \
            UNIT(S[1], S[0], kf[nxt][0], kf[nxt][1], qf[0][0], qf[0][1], negm[0], LOAD, o[0], lacc[0], vf[cur][1], o[1], vf[cur][0], B0, B1, B2, B3); \
            vprev1 = vf[cur][1]; \
        } while (0)
#define TILE(cur, nxt, rdp, DA, DB, PK) TILEX(cur, nxt, rdp, true, DA, NOP_, NOP_, PK, DB, NOP_)
#define PEEK_RD seen = lds_peek(cntp + 4)
#define PEEK_LD seen = lds_peek(cntp)
#define FLIP 13
#define BALANCE(m) do { if (wn == 0 && (m) == FLIP) __builtin_amdgcn_s_setprio(0); } while (0)
        if (wn == 0) __builtin_amdgcn_s_setprio(2); else __builtin_amdgcn_s_setprio(1);
        TILE(0, 1, kvb + L_S0 + 4096, NOP_, NOP_, NOP_);
        TILE(1, 0, kvb + L_S0 + 8192, NOP_, NOP_, NOP_);
        TILE(0, 1, kvb + L_S0 + 12288, NOP_, NOP_, NOP_);
        STAMP_MID(8)
        WAITV(0); if (lane == 0) st_agent(kvfl + mb * 4 + wm, tag);
        STAMP_MID(9)
        lds_signal(cnta + 4);
        TILE(1, 0, kvb + L_S1, NOP_, NOP_, NOP_);
        u64 pollv = ld_agent(kvmine);
        TILE(0, 1, kvb + L_S1 + 4096, NOP_, NOP_, PEEK_RD);
        STAMP_MID(10)
        if (!__all(pollv == tag)) spin_flags(kvmine, tag);
        lds_await(seen, cntp + 4, 4);
        STAMP_MID(11)
        { const unsigned c2 = chunk_off(2); for (int i = 0; i < 4; ++i) dkv(c2, L_S2, i); }
        unsigned cn = chunk_off(3);
        TILEX(1, 0, kvb + L_S1 + 8192, true, dkv(cn, L_S0, 0), dkv(cn, L_S0, 1), NOP_, NOP_, dkv(cn, L_S0, 2), dkv(cn, L_S0, 3));
        TILE(0, 1, kvb + L_S1 + 12288, NOP_, NOP_, NOP_);
        WAITV(4); lds_signal(cnta); lds_await(0, cntp, 4);
        lds_signal(cnta + 4);
        TILE(1, 0, kvb + L_S2, NOP_, NOP_, PEEK_RD);
        STAMP_MID(7)
        int sc = L_S2, sn = L_S0, sp = L_S1;
        for (int m = 2; m <= 12; ++m) {
            const LAS char* cp = kvb + sc;
            cn = chunk_off(m + 2);
            lds_await(seen, cntp + 4, 4 * m);
            BALANCE(m);
            TILEX(0, 1, cp + 4096, true, dkv(cn, sp, 0), dkv(cn, sp, 1), NOP_, NOP_, dkv(cn, sp, 2), dkv(cn, sp, 3));
            TILE(1, 0, cp + 8192, NOP_, NOP_, NOP_);
            WAITV(4); lds_signal(cnta);
            TILE(0, 1, cp + 12288, NOP_, NOP_, PEEK_LD);
            lds_await(seen, cntp, 4 * m);
            lds_signal(cnta + 4);
            TILE(1, 0, kvb + sn, NOP_, NOP_, PEEK_RD);
            const int t_ = sc; sc = sn; sn = sp; sp = t_;
        }
        cn = chunk_off(15);
        lds_await(seen, cntp + 4, 52);
        BALANCE(13);
        TILEX(0, 1, kvb + L_S1 + 4096, true, dkv(cn, L_S0, 0), dkv(cn, L_S0, 1), NOP_, NOP_, dkv(cn, L_S0, 2), dkv(cn, L_S0, 3));
        TILE(1, 0, kvb + L_S1 + 8192, NOP_, NOP_, NOP_);
        WAITV(4); lds_signal(cnta);
        TILE(0, 1, kvb + L_S1 + 12288, NOP_, NOP_, PEEK_LD);
        lds_await(seen, cntp, 52);
        lds_signal(cnta + 4);
        TILEX(1, 0, kvb + L_S2, true, dwo(0, L_R0, 0), dwo(0, L_R0, 1), NOP_, PEEK_RD, dwo(0, L_R0, 2), dwo(0, L_R0, 3));
        lds_await(seen, cntp + 4, 56);
        BALANCE(14);
        TILEX(0, 1, kvb + L_S2 + 4096, true, dwo(2, L_S1, 0), dwo(2, L_S1, 1), NOP_, NOP_, dwo(2, L_S1, 2), dwo(2, L_S1, 3));
        TILE(1, 0, kvb + L_S2 + 8192, NOP_, NOP_, NOP_);
        WAITV(8); lds_signal(cnta);
        TILE(0, 1, kvb + L_S2 + 12288, dwo(1, L_R1, 0), dwo(1, L_R1, 1), PEEK_LD);
        lds_await(seen, cntp, 56);
        lds_signal(cnta + 4);
        TILE(1, 0, kvb + L_S0, dwo(1, L_R1, 2), if (wid != 7) dwo(1, L_R1, 3), PEEK_RD);
        lds_await(seen, cntp + 4, 60);
        TILEX(0, 1, kvb + L_S0 + 4096, true, dwo(3, L_S2, 0), dwo(3, L_S2, 1), NOP_, NOP_, dwo(3, L_S2, 2), dwo(3, L_S2, 3));
        TILE(1, 0, kvb + L_S0 + 8192, NOP_, NOP_, NOP_);
        TILE(0, 1, kvb + L_S0 + 12288, NOP_, NOP_, NOP_);
        TILEX(1, 0, kvb, false, NOP_, NOP_, NOP_, NOP_, NOP_, NOP_);
        lacc[1] = SMF(sel, P, lacc[1], selidx); o[1] = MFMA32(vprev1, PHI(P), o[1]);
    }
    __builtin_amdgcn_s_setprio(0);
    STAMP_MID(3)
    {
        float l[2];
        auto rowsum = [&](int a) { const float t0 = __shfl(lacc[a][0] + lacc[a][1], lane & 15), t1 = __shfl(lacc[a][2] + lacc[a][3], lane & 15); return (lane & 16) ? t1 : t0; };
        bool bad = false;
#pragma unroll
        for (int a = 0; a < 2; ++a) {
            l[a] = rowsum(a);
            float z = l[a] * 0.f, z1 = 0.f;
#pragma unroll
            for (int i = 0; i < 16; i += 2) { z = __builtin_fmaf(o[a][i], 0.f, z); z1 = __builtin_fmaf(o[a][i + 1], 0.f, z1); }
            bad |= !(l[a] < 1.0e18f) || !(z + z1 == 0.f);
        }
        if (__any(bad)) {
            spin_flags(kvfl + (lane & 31), tag);
            const srd_t sk1 = make_srd(ws + WS_KV + (size_t)(b * 32 + h) * 64 * 4096, 64 * 4096);
#define LDKV(t, f) BF(__builtin_amdgcn_raw_buffer_load_b128(sk1, lane * 16 + (f) * 1024, (t) * 4096, 16))
            f32x16 negm[2];
#pragma unroll
            for (int a = 0; a < 2; ++a) {
                float m = -__builtin_inff();
                for (int t = 0; t < 64; ++t) {
                    const bf16x8 k0 = LDKV(t, 0), k1 = LDKV(t, 1);
                    f32x16 z;
#pragma unroll
                    for (int i = 0; i < 16; ++i) z[i] = 0.f;
                    z = MFMA32(k0, qf[a][0], z); z = MFMA32(k1, qf[a][1], z);
#pragma unroll
                    for (int i = 0; i < 16; ++i) m = fmaxf(m, z[i]);
                }
                m = fmaxf(m, __shfl_xor(m, 32));
#pragma unroll
                for (int i = 0; i < 16; ++i) { negm[a][i] = -m; o[a][i] = 0.f; }
                lacc[a] = (f32x4){0.f, 0.f, 0.f, 0.f};
            }
            for (int t = 0; t < 64; ++t) {
                const bf16x8 k0 = LDKV(t, 0), k1 = LDKV(t, 1), v0 = LDKV(t, 2), v1 = LDKV(t, 3);
#pragma unroll
                for (int a = 0; a < 2; ++a) {
                    f32x16 z = MFMA32(k0, qf[a][0], negm[a]); z = MFMA32(k1, qf[a][1], z);
                    u32x8 P;
#pragma unroll
                    for (int i = 0; i < 8; ++i) P[i] = cvtpk(ex2(z[2 * i]), ex2(z[2 * i + 1]));
                    o[a] = MFMA32(v0, PLO(P), o[a]); o[a] = MFMA32(v1, PHI(P), o[a]); lacc[a] = SMF(sel, P, lacc[a], selidx);
                }
            }
            l[0] = rowsum(0); l[1] = rowsum(1);
        }
        const srd_t sow = make_srd(ws + WS_O + (size_t)(b * 64 + mb * 8 + wm * 2) * 65536, 2 * 65536);
#pragma unroll
        for (int a = 0; a < 2; ++a) {
            const float rl = __builtin_amdgcn_rcpf(l[a]);
            __builtin_amdgcn_raw_buffer_store_b128(pack8(o[a], 0, rl), sow, lane * 16 + a * 65536 + h * 2048, 0, 16);
            __builtin_amdgcn_raw_buffer_store_b128(pack8(o[a], 1, rl), sow, lane * 16 + a * 65536 + h * 2048 + 1024, 0, 16);
        }
    }
    GAS u64* const ofl = flags + (FL_O / 8) + b * 1024;
    WAITV(0); if (lane == 0) st_agent(ofl + (mb * 4 + wm) * 32 + h, tag);
    STAMP_MID(4)

    {
        const int wt = wid & 3, kh = wid >> 2;
        LBAR();
        if (wid == 7) dwo(1, L_R1, 3);
        spin_flags(ofl + (tm * 2 + (wt >> 1)) * 32 + kh * 16 + (lane & 15), tag);
        STAMP_MID(5)
        const srd_t sor = make_srd(ws + WS_O + (size_t)(b * 64 + tm * 4 + wt) * 65536 + kh * 32768, 32768);
        bf16x8 of[4][4], wf[2][4];
#define P3AUX 16
#define P3_LO1(i, ks) of[(i) & 3][ks] = BF(__builtin_amdgcn_raw_buffer_load_b128(sor, lane * 16, (i) * 4096 + (ks) * 1024, P3AUX))
#define P3_LO(i) do { P3_LO1(i, 0); P3_LO1(i, 1); P3_LO1(i, 2); P3_LO1(i, 3); } while (0)
        P3_LO(0); P3_LO(1); P3_LO(2); P3_LO(3);
        for (int i = 0; i < 4; ++i) dwo(4, L_S0, i);
        const unsigned wa3 = kh * 16384 + (4 * (g4 >> 1) + q4) * 256 + (g4 & 1) * 32 + p4 * 8;
        unsigned wn3[4];
#pragma unroll
        for (int n = 0; n < 4; ++n) wn3[n] = wa3 + ((n ^ q4) << 6);
        f32x16 acc[4];
#pragma unroll
        for (int n = 0; n < 4; ++n)
#pragma unroll
            for (int i = 0; i < 16; ++i) acc[n][i] = 0.f;
#define P3_RW(buf, slot, ks, n) do { const LAS char* w_ = lds + (slot) + (ks) * 4096 + wn3[n]; wf[buf][n] = cat8(ldstr(w_), ldstr(w_ + 2048)); } while (0)
#define P3_KS(cur, nxt, OF, RD, slot, ks, M0, M1, M2, M3) do { \
            acc[0] = MFMA32(wf[cur][0], OF, acc[0]); if (RD) P3_RW(nxt, slot, ks, 0); M0; SBAR(); \
            acc[1] = MFMA32(wf[cur][1], OF, acc[1]); if (RD) P3_RW(nxt, slot, ks, 1); M1; SBAR(); \
            acc[2] = MFMA32(wf[cur][2], OF, acc[2]); if (RD) P3_RW(nxt, slot, ks, 2); M2; SBAR(); \
            acc[3] = MFMA32(wf[cur][3], OF, acc[3]); if (RD) P3_RW(nxt, slot, ks, 3); M3; SBAR(); \
        } while (0)
#define P3_STEP(i, SL, SN, WAIT, LAST) do { \
            P3_KS(0, 1, of[(i) & 3][0], true, SL, 1, if ((i) >= 1 && (i) + 3 < 8) P3_LO1((i) + 3, 0), if ((i) >= 1 && (i) + 3 < 8) P3_LO1((i) + 3, 1), \
                  if ((i) >= 1 && (i) + 3 < 8) P3_LO1((i) + 3, 2), if ((i) >= 1 && (i) + 3 < 8) P3_LO1((i) + 3, 3)); \
            P3_KS(1, 0, of[(i) & 3][1], true, SL, 2, NOP_, NOP_, NOP_, NOP_); \
            P3_KS(0, 1, of[(i) & 3][2], true, SL, 3, NOP_, NOP_, NOP_, NOP_); \
            if (!(LAST)) { WAIT; } \
            P3_KS(1, 0, of[(i) & 3][3], !(LAST), SN, 0, if ((i) + 5 < 8) dwo((i) + 5, SL, 0), if ((i) + 5 < 8) dwo((i) + 5, SL, 1), if ((i) + 5 < 8) dwo((i) + 5, SL, 2), if ((i) + 5 < 8) dwo((i) + 5, SL, 3)); \
        } while (0)
        P3_RW(0, L_R0, 0, 0); P3_RW(0, L_R0, 0, 1); P3_RW(0, L_R0, 0, 2); P3_RW(0, L_R0, 0, 3); SBAR();
        P3_STEP(0, L_R0, L_R1, LBAR(), false);
        if (tid == 0 && (xcd & 3) == 0 && j == 0) st_agent(flags + b * 32, tag);
        P3_STEP(1, L_R1, L_S1, LBAR(), false);
        P3_STEP(2, L_S1, L_S2, LBAR(), false);
        P3_STEP(3, L_S2, L_S0, WAITBAR(24), false);
        P3_STEP(4, L_S0, L_R0, WAITBAR(24), false);
        P3_STEP(5, L_R0, L_R1, WAITBAR(16), false);
        P3_STEP(6, L_R1, L_S1, WAITBAR(8), false);
        P3_STEP(7, L_S1, L_S1, NOP_, true);
        STAMP_MID(6)
        LAS char* mine = lds + (kh ? L_S2 : L_S0) + wt * 8192 + lane * 16;
        const LAS char* theirs = lds + (kh ? L_S0 : L_S2) + wt * 8192 + lane * 16;
        if (kh) { const f32x16 t0 = acc[0], t1 = acc[1]; acc[0] = acc[2]; acc[1] = acc[3]; acc[2] = t0; acc[3] = t1; }
#pragma unroll
        for (int n2 = 0; n2 < 2; ++n2)
#pragma unroll
            for (int qd = 0; qd < 4; ++qd)
                *(LAS f32x4*)(mine + n2 * 4096 + qd * 1024) = (f32x4){acc[2 + n2][4 * qd], acc[2 + n2][4 * qd + 1], acc[2 + n2][4 * qd + 2], acc[2 + n2][4 * qd + 3]};
        LBAR();
        const int token = tm * 128 + wt * 32 + r32;
        const srd_t sout = make_srd(out, 2048 * 2048 * 2);
#pragma unroll
        for (int n2 = 0; n2 < 2; ++n2) {
            f32x16 v = acc[n2];
#pragma unroll
            for (int qd = 0; qd < 4; ++qd) {
                const f32x4 t = *(const LAS f32x4*)(theirs + n2 * 4096 + qd * 1024);
                v[4 * qd] += t[0]; v[4 * qd + 1] += t[1]; v[4 * qd + 2] += t[2]; v[4 * qd + 3] += t[3];
            }
            const unsigned oo = (unsigned)token * 4096u + (unsigned)(b * 1024 + tn * 128 + (2 * kh + n2) * 32) * 2u + hh * 16;
#pragma unroll
            for (int gp = 0; gp < 2; ++gp) {
                const unsigned a0 = cvtpk(v[8 * gp], v[8 * gp + 1]), a1 = cvtpk(v[8 * gp + 2], v[8 * gp + 3]);
                const unsigned b0 = cvtpk(v[8 * gp + 4], v[8 * gp + 5]), b1 = cvtpk(v[8 * gp + 6], v[8 * gp + 7]);
                auto r0 = __builtin_amdgcn_permlane32_swap(a0, b0, false, false);
                auto r1 = __builtin_amdgcn_permlane32_swap(a1, b1, false, false);
                const u32x4 w = {r0[0], r1[0], r0[1], r1[1]};
                __builtin_amdgcn_raw_buffer_store_b128(w, sout, oo + gp * 32, 0, OUTAUX);
            }
        }
    }
    STAMP_END(ws)
}

extern "C" void kernel_launch(void* const* d_in, const int* in_sizes, int n_in, void* d_out, int out_size, void* d_ws, size_t ws_size, hipStream_t stream) {
    (void)hipFuncSetAttribute((const void*)mha_fused, hipFuncAttributeMaxDynamicSharedMemorySize, L_END);
    mha_fused<<<256, 512, L_END, stream>>>((const uint16_t*)d_in[0], (const uint16_t*)d_in[1], (const uint16_t*)d_in[2], (const uint16_t*)d_in[3], (const uint16_t*)d_in[4],
                                           (const uint16_t*)d_in[5], (char*)d_ws, (uint16_t*)d_out);
}
```

```cpp
#define P1FINE
#include <hip/hip_runtime.h>
#include <stdint.h>

typedef __bf16 bf16x8 __attribute__((ext_vector_type(8)));
typedef __bf16 bf16x16 __attribute__((ext_vector_type(16)));
typedef __bf16 bf16x2 __attribute__((ext_vector_type(2)));
typedef short s16x4 __attribute__((ext_vector_type(4)));
typedef short s16x8 __attribute__((ext_vector_type(8)));
typedef float f32x16 __attribute__((ext_vector_type(16)));
typedef float f32x4 __attribute__((ext_vector_type(4)));
typedef float f32x2 __attribute__((ext_vector_type(2)));
typedef unsigned u32x4 __attribute__((ext_vector_type(4)));
typedef unsigned u32x8 __attribute__((ext_vector_type(8)));
typedef unsigned long long u64;
#define LAS __attribute__((address_space(3)))
#define GAS __attribute__((address_space(1)))
#define DI __device__ __forceinline__
#define MFMA32(a, b, c) __builtin_amdgcn_mfma_f32_32x32x16_bf16((a), (b), (c), 0, 0, 0)
#define SMF(a, b, c, idx) __builtin_amdgcn_smfmac_f32_16x16x64_bf16((a), __builtin_bit_cast(bf16x16, b), (c), (idx), 0, 0)
#define PLO(P) __builtin_bit_cast(bf16x8, __builtin_shufflevector(P, P, 0, 1, 2, 3))
#define PHI(P) __builtin_bit_cast(bf16x8, __builtin_shufflevector(P, P, 4, 5, 6, 7))
#define SBAR() __builtin_amdgcn_sched_barrier(0)
#define BF(x) __builtin_bit_cast(bf16x8, x)
#define NOP_ (void)0
typedef __amdgpu_buffer_rsrc_t srd_t;

#define OUTAUX 0
#define MAPSEL 1
constexpr float QSCALE = 0.17677669529663687f * 1.4426950408889634f;
constexpr unsigned WS_KV = 0;
constexpr unsigned WS_O = 16u << 20;
constexpr unsigned WS_FL = 24u << 20;
constexpr unsigned FL_KV = 4096;
constexpr unsigned FL_O = FL_KV + 16384;
constexpr int L_S0 = 32768, L_S1 = 65536, L_S2 = 0, L_R0 = 98304, L_R1 = 131072, L_END = 163840, L_CNT = L_END - 64;
constexpr int P1_XS = 32768, P1_WS = 24576, P1_WOFF = 98304;

DI unsigned rfl(unsigned x) { return __builtin_amdgcn_readfirstlane(x); }
DI srd_t make_srd(const void* p, unsigned bytes) { return __builtin_amdgcn_make_buffer_rsrc((void*)p, (short)0, (int)bytes, 0x00020000); }
DI void dma16(unsigned ldsdst, unsigned voff, srd_t srd, unsigned soff) {
    asm volatile("s_mov_b32 m0, %0\n\ts_nop 0\n\tbuffer_load_dwordx4 %1, %2, %3 offen lds" :: "s"(ldsdst), "v"(voff), "s"(srd), "s"(soff) : "memory");
}
DI void dma16c(unsigned ldsdst, unsigned voff, srd_t srd, unsigned soff) {
    asm volatile("s_mov_b32 m0, %0\n\ts_nop 0\n\tbuffer_load_dwordx4 %1, %2, %3 offen sc1 lds" :: "s"(ldsdst), "v"(voff), "s"(srd), "s"(soff) : "memory");
}
template <int I> DI void dma4c(unsigned ldsdst, unsigned voff, srd_t srd, unsigned soff) {
    if constexpr (I == 0) asm volatile("s_mov_b32 m0, %0\n\ts_nop 0\n\tbuffer_load_dwordx4 %1, %2, %3 offen sc1 lds" :: "s"(ldsdst), "v"(voff), "s"(srd), "s"(soff) : "memory");
    else asm volatile("buffer_load_dwordx4 %0, %1, %2 offen offset:%3 sc1 lds" :: "v"(voff), "s"(srd), "s"(soff), "i"(I * 1024) : "memory");
}
DI unsigned cvtpk(float lo, float hi) { f32x2 v = {lo, hi}; bf16x2 b = __builtin_convertvector(v, bf16x2); return __builtin_bit_cast(unsigned, b); }
DI bf16x8 ldsfrag(const LAS char* p) { return *(const LAS bf16x8*)p; }
typedef short v4i16_t __attribute__((ext_vector_type(4)));
DI s16x4 ldstr(const LAS char* p) { return __builtin_amdgcn_ds_read_tr16_b64_v4i16((LAS v4i16_t*)p); }
DI bf16x8 cat8(s16x4 lo, s16x4 hi) { s16x8 r = __builtin_shufflevector(lo, hi, 0, 1, 2, 3, 4, 5, 6, 7); return __builtin_bit_cast(bf16x8, r); }
DI u32x4 pack8(const f32x16& x, int s, float mul) {
    u32x4 p; p[0] = cvtpk(x[8 * s] * mul, x[8 * s + 1] * mul); p[1] = cvtpk(x[8 * s + 2] * mul, x[8 * s + 3] * mul);
    p[2] = cvtpk(x[8 * s + 4] * mul, x[8 * s + 5] * mul); p[3] = cvtpk(x[8 * s + 6] * mul, x[8 * s + 7] * mul); return p; }
DI u32x4 pack8n(const f32x16& x, int s) {
    u32x4 p; p[0] = cvtpk(x[8 * s], x[8 * s + 1]); p[1] = cvtpk(x[8 * s + 2], x[8 * s + 3]);
    p[2] = cvtpk(x[8 * s + 4], x[8 * s + 5]); p[3] = cvtpk(x[8 * s + 6], x[8 * s + 7]); return p; }
#define WAITBAR(vm) asm volatile("s_waitcnt vmcnt(" #vm ") lgkmcnt(0)\n\ts_barrier" ::: "memory")
#define LBAR() asm volatile("s_waitcnt lgkmcnt(0)\n\ts_barrier" ::: "memory")
#define WAITV(vm) asm volatile("s_waitcnt vmcnt(" #vm ")" ::: "memory")
DI float ex2(float x) { return __builtin_amdgcn_exp2f(x); }
DI u64 mix64(u64 z) { z += 0x9E3779B97F4A7C15ull; z = (z ^ (z >> 30)) * 0xBF58476D1CE4E5B9ull; z = (z ^ (z >> 27)) * 0x94D049BB133111EBull; return z ^ (z >> 31); }
DI u64 ld_agent(const GAS u64* p) { return __hip_atomic_load(p, __ATOMIC_RELAXED, __HIP_MEMORY_SCOPE_AGENT); }
DI void st_agent(GAS u64* p, u64 v) { __hip_atomic_store(p, v, __ATOMIC_RELAXED, __HIP_MEMORY_SCOPE_AGENT); }
DI void spin_flags(const GAS u64* f, u64 tag) {
    const u64 t0 = __builtin_amdgcn_s_memrealtime();
    u64 v0 = ld_agent(f);
    for (;;) {
        __builtin_amdgcn_s_sleep(6);
        const u64 v1 = ld_agent(f);
        if (__all(v0 == tag)) break;
        __builtin_amdgcn_s_sleep(6);
        v0 = ld_agent(f);
        if (__all(v1 == tag)) break;
        if (__builtin_amdgcn_s_memrealtime() - t0 > 20000000ull) break;
    }
    asm volatile("" ::: "memory");
}
DI void lds_signal(unsigned addr) {
    unsigned one = 1;
    asm volatile("s_waitcnt lgkmcnt(0)\n\ts_mov_b64 exec, 1\n\tds_add_u32 %0, %1\n\ts_mov_b64 exec, -1" :: "v"(addr), "v"(one) : "memory");
}
DI unsigned lds_peek(const LAS char* p) { return *(const volatile LAS unsigned*)p; }
DI void lds_await(unsigned seen, const LAS char* p, unsigned need) {
    if (rfl(seen) < need) {
        _Pragma("nounroll") for (int it = 0; it < (1 << 22); ++it) { if (rfl(lds_peek(p)) >= need) break; __builtin_amdgcn_s_sleep(0); }
    }
    asm volatile("" ::: "memory");
}

#define STAMP_BEGIN
#define STAMP_MID(i)
#define STAMP_END(ws)

__global__ __launch_bounds__(512, 2) void mha_fused(const uint16_t* xA, const uint16_t* wA, const uint16_t* woA, const uint16_t* xB, const uint16_t* wB, const uint16_t* woB,
                                                    char* ws, uint16_t* out) {
    extern __shared__ __attribute__((aligned(16))) char lds_[];
    LAS char* lds = (LAS char*)lds_;
    STAMP_BEGIN
    const int tid = threadIdx.x, lane = tid & 63, wid = rfl(tid >> 6);
    const int bid = blockIdx.x, xcd = bid & 7, j = bid >> 3;
    const int b = xcd >> 2;
    const int mb = (xcd & 1) * 4 + (j & 3), hp = ((xcd >> 1) & 1) * 8 + (j >> 2);
    const int wm = wid & 3, wn = wid >> 2, h = hp * 2 + wn;
    const unsigned lds0 = (unsigned)(uintptr_t)lds_;
    const int r32 = lane & 31, hh = lane >> 5, g4 = lane >> 4, q4 = (lane >> 2) & 3, p4 = lane & 3;
    GAS u64* const flags = (GAS u64*)(ws + WS_FL);
    u64 epoch = ld_agent(flags + b * 32);
    if (tid < 16) *(LAS unsigned*)(lds + L_CNT + tid * 4) = 0u;

    bf16x8 qf[2][2];
    u32x4 kvp[2][4];
    const srd_t skvw = make_srd(ws + WS_KV + (size_t)(b * 32 + h) * 64 * 4096 + (size_t)(mb * 8 + wm * 2) * 4096, 2 * 4096);
    {
        const srd_t sx = make_srd(b ? xB : xA, 2048 * 1024 * 2), sw = make_srd(b ? wB : wA, 1024 * 3072 * 2);
        const unsigned vx0 = (lane >> 3) * 2048 + (((lane & 7) ^ (lane >> 4)) << 4);
        const unsigned vx1 = (lane >> 3) * 2048 + (((lane & 7) ^ (4 + (lane >> 4))) << 4);
        const unsigned vw = (lane >> 3) * 6144 + (((lane & 7) ^ (((lane >> 4) & 1) << 2) ^ ((wid & 1) << 1)) << 4);
        const unsigned sx0 = (mb * 256 + wid * 32) * 2048, sw0 = wid * 8 * 6144 + hp * 128;
        auto dx = [&](int kt, int i) { dma16(lds0 + (kt % 3) * P1_XS + wid * 4096 + i * 1024, (i & 1) ? vx1 : vx0, sx, sx0 + i * 8 * 2048 + kt * 128); };
        auto dw = [&](int kt, int i) { dma16(lds0 + P1_WOFF + (kt & 1) * P1_WS + wid * 1024 + i * 8192, vw, sw, sw0 + i * 2048 + kt * 64 * 6144); };
        for (int i = 0; i < 3; ++i) dw(0, i);
        for (int i = 0; i < 4; ++i) dx(0, i);
        for (int i = 0; i < 3; ++i) dw(1, i);
        for (int i = 0; i < 4; ++i) dx(1, i);
        for (int i = 0; i < 4; ++i) dx(2, i);
        const int r16 = lane & 15;
        unsigned xa[2], wb[2];
#pragma unroll
        for (int s = 0; s < 2; ++s) xa[s] = (wm * 64 + r16) * 128 + (((4 * s + g4) ^ (r16 >> 1)) << 4);
#pragma unroll
        for (int c = 0; c < 2; ++c) wb[c] = P1_WOFF + (8 * g4 + q4) * 128 + ((wn ^ (q4 >> 1)) << 6) + ((c ^ (g4 & 1)) << 5) + p4 * 8;
        f32x4 acc[4][6];
#pragma unroll
        for (int a = 0; a < 4; ++a)
#pragma unroll
            for (int c = 0; c < 6; ++c) acc[a][c] = (f32x4){0.f, 0.f, 0.f, 0.f};
        bf16x8 xf[2][4][2], wf[3][2];
#define MFMA16(a, b, c) __builtin_amdgcn_mfma_f32_16x16x32_bf16((a), (b), (c), 0, 0, 0)
#define P1_RX(kt, tb, s) xf[(kt) & 1][tb][s] = ldsfrag(lds + ((kt) % 3) * P1_XS + xa[s] + (tb) * 2048)
#define P1_RW(kt, fb, s) do { const LAS char* wp_ = lds + wb[(fb) & 1] + ((kt) & 1) * P1_WS + ((fb) >> 1) * 8192 + (s) * 4096; wf[(fb) % 3][s] = cat8(ldstr(wp_), ldstr(wp_ + 512)); } while (0)
#define P1_MM(kt, tb, fb) do { if ((fb) < 4) { acc[tb][fb] = MFMA16(wf[(fb) % 3][0], xf[(kt) & 1][tb][0], acc[tb][fb]); acc[tb][fb] = MFMA16(wf[(fb) % 3][1], xf[(kt) & 1][tb][1], acc[tb][fb]); } \
                               else { acc[tb][fb] = MFMA16(xf[(kt) & 1][tb][0], wf[(fb) % 3][0], acc[tb][fb]); acc[tb][fb] = MFMA16(xf[(kt) & 1][tb][1], wf[(fb) % 3][1], acc[tb][fb]); } } while (0)
        s16x4 wlo[3][2], whi[3][2];
#define P1_WL(kt, fb, s) wlo[(fb) % 3][s] = ldstr(lds + wb[(fb) & 1] + ((kt) & 1) * P1_WS + ((fb) >> 1) * 8192 + (s) * 4096)
#define P1_WH(kt, fb, s) whi[(fb) % 3][s] = ldstr(lds + wb[(fb) & 1] + ((kt) & 1) * P1_WS + ((fb) >> 1) * 8192 + (s) * 4096 + 512)
#define P1_M1(kt, tb, fb, s) do { const bf16x8 w_ = cat8(wlo[(fb) % 3][s], whi[(fb) % 3][s]); if ((fb) < 4) acc[tb][fb] = MFMA16(w_, xf[(kt) & 1][tb][s], acc[tb][fb]); else acc[tb][fb] = MFMA16(xf[(kt) & 1][tb][s], w_, acc[tb][fb]); } while (0)
#define P1_FB8(kt, fb, R0, R1, R2, R3, R4, R5, R6, R7) do { \
            P1_M1(kt, 0, fb, 0); R0; SBAR(); P1_M1(kt, 1, fb, 0); R1; SBAR(); P1_M1(kt, 2, fb, 0); R2; SBAR(); P1_M1(kt, 3, fb, 0); R3; SBAR(); \
            P1_M1(kt, 0, fb, 1); R4; SBAR(); P1_M1(kt, 1, fb, 1); R5; SBAR(); P1_M1(kt, 2, fb, 1); R6; SBAR(); P1_M1(kt, 3, fb, 1); R7; SBAR(); } while (0)
#define P1_FB(kt, fb, R0, R1, R2, R3) do { \
            P1_MM(kt, 0, fb); R0; SBAR(); P1_MM(kt, 1, fb); R1; SBAR(); P1_MM(kt, 2, fb); R2; SBAR(); P1_MM(kt, 3, fb); R3; SBAR(); } while (0)
        WAITBAR(11);
        STAMP_MID(0)
        P1_RX(0, 0, 0); P1_WL(0, 0, 0); P1_WH(0, 0, 0); P1_RX(0, 1, 0); P1_RX(0, 2, 0); P1_RX(0, 3, 0); P1_WL(0, 0, 1); P1_WH(0, 0, 1);
        P1_RX(0, 0, 1); P1_RX(0, 1, 1); P1_RX(0, 2, 1); P1_RX(0, 3, 1); P1_WL(0, 1, 0); P1_WH(0, 1, 0); P1_WL(0, 1, 1); P1_WH(0, 1, 1); SBAR();
#pragma unroll
        for (int t = 0; t < 16; ++t) {
            const bool xs = (t >= 1 && t + 2 < 16);
            P1_FB8(t, 0, P1_WL(t, 2, 0), NOP_, P1_WH(t, 2, 0), NOP_, P1_WL(t, 2, 1), if (xs) dx(t + 2, 0), P1_WH(t, 2, 1), NOP_);
            P1_FB8(t, 1, P1_WL(t, 3, 0), NOP_, P1_WH(t, 3, 0), NOP_, P1_WL(t, 3, 1), if (xs) dx(t + 2, 1), P1_WH(t, 3, 1), NOP_);
            P1_FB8(t, 2, P1_WL(t, 4, 0), NOP_, P1_WH(t, 4, 0), NOP_, P1_WL(t, 4, 1), if (xs) dx(t + 2, 2), P1_WH(t, 4, 1), NOP_);
            P1_FB8(t, 3, P1_WL(t, 5, 0), NOP_, P1_WH(t, 5, 0), NOP_, P1_WL(t, 5, 1), if (xs) dx(t + 2, 3), P1_WH(t, 5, 1), NOP_);
            if (t < 15) {
                if (t < 14) WAITBAR(4); else WAITBAR(0);
                const bool wsch = (t + 2 < 16);
                P1_FB8(t, 4, P1_RX(t + 1, 0, 0), P1_WL(t + 1, 0, 0), P1_WH(t + 1, 0, 0), P1_RX(t + 1, 1, 0), P1_RX(t + 1, 2, 0), P1_RX(t + 1, 3, 0), P1_WL(t + 1, 0, 1), { P1_WH(t + 1, 0, 1); if (wsch) dw(t + 2, 0); });
                P1_FB8(t, 5, P1_RX(t + 1, 0, 1), P1_RX(t + 1, 1, 1), P1_RX(t + 1, 2, 1), P1_RX(t + 1, 3, 1), P1_WL(t + 1, 1, 0), { P1_WH(t + 1, 1, 0); if (wsch) dw(t + 2, 1); }, P1_WL(t + 1, 1, 1), { P1_WH(t + 1, 1, 1); if (wsch) dw(t + 2, 2); });
            } else {
                P1_FB8(t, 4, NOP_, NOP_, NOP_, NOP_, NOP_, NOP_, NOP_, NOP_);
                P1_FB8(t, 5, NOP_, NOP_, NOP_, NOP_, NOP_, NOP_, NOP_, NOP_);
            }
        }
        STAMP_MID(1)
        auto swap16 = [](unsigned& x, unsigned& y) { auto r = __builtin_amdgcn_permlane16_swap(x, y, false, false); x = r[0]; y = r[1]; };
        auto swap32 = [](unsigned& x, unsigned& y) { auto r = __builtin_amdgcn_permlane32_swap(x, y, false, false); x = r[0]; y = r[1]; };
        auto qkfrag = [&](const f32x4& t0, const f32x4& t1, float mul) {
            unsigned a0 = cvtpk(t0[0] * mul, t0[1] * mul), a1 = cvtpk(t0[2] * mul, t0[3] * mul), b0 = cvtpk(t1[0] * mul, t1[1] * mul), b1 = cvtpk(t1[2] * mul, t1[3] * mul);
            swap16(a0, b0); swap16(a1, b1);
            return (u32x4){a0, a1, b0, b1}; };
        auto vfrag = [&](const f32x4& x0, const f32x4& x1) {
            unsigned a0 = cvtpk(x0[0], x0[1]), a1 = cvtpk(x0[2], x0[3]), b0 = cvtpk(x1[0], x1[1]), b1 = cvtpk(x1[2], x1[3]);
            swap16(a0, b0); swap16(a1, b1); swap32(a0, b0); swap32(a1, b1);
            return (u32x4){a0, a1, b0, b1}; };
#pragma unroll
        for (int a = 0; a < 2; ++a)
#pragma unroll
            for (int s = 0; s < 2; ++s) {
                kvp[a][s] = qkfrag(acc[2 * a][2 + s], acc[2 * a + 1][2 + s], 1.0f); kvp[a][2 + s] = vfrag(acc[2 * a + s][4], acc[2 * a + s][5]);
                __builtin_amdgcn_raw_buffer_store_b128(kvp[a][s], skvw, lane * 16 + a * 4096 + s * 1024, 0, 16);
                __builtin_amdgcn_raw_buffer_store_b128(kvp[a][2 + s], skvw, lane * 16 + a * 4096 + 2048 + s * 1024, 0, 16);
            }
#define P1_QF(a, s) BF(qkfrag(acc[2 * (a)][s], acc[2 * (a) + 1][s], QSCALE))
#pragma unroll
        for (int a = 0; a < 2; ++a) {
            const int tl = wm * 2 + a;
            LAS char* lp = lds + L_S0 + (tl >> 2) * 32768 + wn * 16384 + (tl & 3) * 4096 + lane * 16;
#pragma unroll
            for (int f = 0; f < 4; ++f) *(LAS u32x4*)(lp + f * 1024) = kvp[a][f];
        }
        SBAR();
#pragma unroll
        for (int a = 0; a < 2; ++a)
#pragma unroll
            for (int s = 0; s < 2; ++s) qf[a][s] = P1_QF(a, s);
    }
    asm volatile("" : "+v"(epoch));
    const u64 tag = mix64(((u64)rfl((unsigned)(epoch >> 32)) << 32) | rfl((unsigned)epoch));
    STAMP_MID(2)

    const srd_t skv = make_srd(ws + WS_KV + (size_t)(b * 32 + hp * 2) * 64 * 4096, 2 * 64 * 4096);
    const srd_t swo = make_srd(b ? woB : woA, 1024 * 1024 * 2);
    const int tm = (xcd & 3) * 4 + (j >> 3), tn = j & 7;
    const unsigned vl = lane * 16;
    const unsigned vwo = (lane >> 4) * 2048 + (((lane & 15) ^ (((lane >> 4) & 3) << 2)) << 4);
    auto chunk_off = [&](int c) { const int i = (c - 2) >> 1; const int blk = c < 2 ? mb : (i < mb ? i : i + 1); return (unsigned)((wn * 64 + blk * 8 + (c & 1) * 4 + wm) * 4096); };
    const unsigned ldw = lds0 + wid * 4096;
#define DKV(coff, slotoff, i) dma4c<i>(ldw + (slotoff), vl, skv, coff)
    const unsigned wo0 = (wn * 32 + wm * 8) * 2048 + tn * 256;
    auto dwu = [&](int u, unsigned slotaddr, int p) { dma16(lds0 + slotaddr + wm * 2048 + p * 1024, vwo, swo, wo0 + (u * 64 + p * 4) * 2048); };
    auto wo_rs = [&](int k) { return (unsigned)(L_R0 + wn * 24576 + k * 8192); };
    auto wo_hs = [&](int k) { return (unsigned)((k >> 1 == 0 ? L_S0 : k >> 1 == 1 ? L_S1 : L_S2) + wn * 16384 + (k & 1) * 8192); };
    bf16x8 sel; int selidx;
    { const int i16 = lane & 15; const short lo = (i16 < 2) ? (short)0x3F80 : (short)0, hi = (i16 == 2 || i16 == 3) ? (short)0x3F80 : (short)0;
      s16x8 t = {lo, lo, lo, lo, hi, hi, hi, hi}; sel = __builtin_bit_cast(bf16x8, t); selidx = (i16 & 1) ? 0xEEEE : 0x4444; }
    const LAS char* kvb = lds + wn * 16384 + lane * 16;
    GAS u64* const kvfl = flags + (FL_KV / 8) + (b * 32 + h) * 32;
    const GAS u64* const kvmine = kvfl + ((lane >> 1) & 7) * 4 + (lane & 1) * 2 + (wm >> 1);
    const LAS char* cntp = lds + L_CNT + wn * 16;
    const unsigned cnta = lds0 + L_CNT + wn * 16;
    f32x16 o[2]; f32x4 lacc[2];
    {
        f32x16 negm[2], S[2];
        bf16x8 kf[2][2], vf[2][2];
        u32x8 P;
        const f32x16 zero16 = {0.f,0.f,0.f,0.f,0.f,0.f,0.f,0.f,0.f,0.f,0.f,0.f,0.f,0.f,0.f,0.f};
        kf[0][0] = BF(kvp[0][0]); kf[0][1] = BF(kvp[0][1]); vf[0][0] = BF(kvp[0][2]); vf[0][1] = BF(kvp[0][3]);
#pragma unroll
        for (int a = 0; a < 2; ++a) {
            f32x16 z;
#pragma unroll
            for (int i = 0; i < 16; ++i) z[i] = 0.f;
            z = MFMA32(kf[0][0], qf[a][0], z); z = MFMA32(kf[0][1], qf[a][1], z);
            float m = z[0];
#pragma unroll
            for (int i = 1; i < 16; ++i) m = fmaxf(m, z[i]);
            m = fmaxf(m, __shfl_xor(m, 32));
#pragma unroll
            for (int i = 0; i < 16; ++i) { negm[a][i] = -m; o[a][i] = 0.f; }
            lacc[a] = (f32x4){0.f, 0.f, 0.f, 0.f};
        }
        S[0] = MFMA32(kf[0][0], qf[0][0], negm[0]); S[0] = MFMA32(kf[0][1], qf[0][1], S[0]);
        P = (u32x8){0u, 0u, 0u, 0u, 0u, 0u, 0u, 0u};
        bf16x8 vprev1 = vf[0][1];
        unsigned seen;
#define EXPI(X, i) X[i] = ex2(X[i])
#define PVM(O, V, PP) O = MFMA32(V, PP, O)
#define PKA(L, X, i) NOP_
#define NEGSEL(N) N
#define UNIT(SX, SN, KA0, KA1, QB0, QB1, NEG, DOQK, OPREV, LPREV, VPREV1, OCUR, LCUR, VCUR0, H0, H1, H2, H3) do { \
            if (DOQK) SN = MFMA32(KA0, QB0, NEGSEL(NEG)); H0; SBAR(); \
            EXPI(SX, 0); EXPI(SX, 1); EXPI(SX, 2); SBAR(); \
            LPREV = SMF(sel, P, LPREV, selidx); SBAR(); \
            EXPI(SX, 3); P[0] = cvtpk(SX[0], SX[1]); P[1] = cvtpk(SX[2], SX[3]); PKA(LCUR, SX, 0); PKA(LCUR, SX, 2); SBAR(); \
            PVM(OPREV, VPREV1, PHI(P)); H1; SBAR(); \
            EXPI(SX, 4); EXPI(SX, 5); EXPI(SX, 6); EXPI(SX, 7); P[2] = cvtpk(SX[4], SX[5]); P[3] = cvtpk(SX[6], SX[7]); PKA(LCUR, SX, 4); PKA(LCUR, SX, 6); SBAR(); \
            if (DOQK) SN = MFMA32(KA1, QB1, SN); H2; SBAR(); \
            EXPI(SX, 8); EXPI(SX, 9); EXPI(SX, 10); EXPI(SX, 11); P[4] = cvtpk(SX[8], SX[9]); P[5] = cvtpk(SX[10], SX[11]); PKA(LCUR, SX, 8); PKA(LCUR, SX, 10); SBAR(); \
            PVM(OCUR, VCUR0, PLO(P)); H3; SBAR(); \
            EXPI(SX, 12); EXPI(SX, 13); EXPI(SX, 14); EXPI(SX, 15); P[6] = cvtpk(SX[12], SX[13]); P[7] = cvtpk(SX[14], SX[15]); PKA(LCUR, SX, 12); PKA(LCUR, SX, 14); SBAR(); \
        } while (0)
#define TILEG(cur, nxt, LK, LV, LOAD, A2, A3, B0, B1, B2, B3) do { \
            UNIT(S[0], S[1], kf[cur][0], kf[cur][1], qf[1][0], qf[1][1], negm[1], true, o[1], lacc[1], vprev1, o[0], lacc[0], vf[cur][0], LK, LV, A2, A3); \
            UNIT(S[1], S[0], kf[nxt][0], kf[nxt][1], qf[0][0], qf[0][1], negm[0], LOAD, o[0], lacc[0], vf[cur][1], o[1], lacc[1], vf[cur][0], B0, B1, B2, B3); \
            vprev1 = vf[cur][1]; \
        } while (0)
#define TILEX(cur, nxt, rdp, LOAD, A2, A3, B0, B1, B2, B3) TILEG(cur, nxt, \
            if (LOAD) { kf[nxt][0] = ldsfrag(rdp); kf[nxt][1] = ldsfrag((rdp) + 1024); }, if (LOAD) { vf[nxt][0] = ldsfrag((rdp) + 2048); vf[nxt][1] = ldsfrag((rdp) + 3072); }, \
            LOAD, A2, A3, B0, B1, B2, B3)
#define TILE(cur, nxt, rdp, PK) TILEX(cur, nxt, rdp, true, NOP_, NOP_, NOP_, PK, NOP_, NOP_)
#define TILED(cur, nxt, rdp, coff, slot) TILEX(cur, nxt, rdp, true, DKV(coff, slot, 0), DKV(coff, slot, 1), NOP_, NOP_, DKV(coff, slot, 2), DKV(coff, slot, 3))
#define PEEK seen = lds_peek(cntp)
#define SIGNAL() lds_signal(cnta)
#define FLIP 56
#define BALANCE(m) do { if (wn == 0 && (m) == FLIP) __builtin_amdgcn_s_setprio(0); } while (0)
        if (wn == 0) __builtin_amdgcn_s_setprio(2); else __builtin_amdgcn_s_setprio(1);
        auto own = [&](int p) {
            const int lo = wm < 2;
            const int off = lo ? (p < 4 ? L_S0 + ((1 - wm) * 2 + p - 2) * 4096 : L_S1 + (p - 4) * 4096) : (p < 6 ? L_S0 + (p - 2) * 4096 : L_S1 + ((3 - wm) * 2 + p - 6) * 4096);
            return kvb + off; };
        TILEG(0, 1, { kf[1][0] = BF(kvp[1][0]); kf[1][1] = BF(kvp[1][1]); }, { vf[1][0] = BF(kvp[1][2]); vf[1][1] = BF(kvp[1][3]); }, true, NOP_, NOP_, NOP_, NOP_, NOP_, NOP_);
        LBAR();
        TILE(1, 0, own(2), NOP_);
        TILE(0, 1, own(3), NOP_);
        STAMP_MID(8)
        WAITV(0); if (lane == 0) st_agent(kvfl + mb * 4 + wm, tag);
        STAMP_MID(9)
        TILE(1, 0, own(4), NOP_);
        u64 pollv = ld_agent(kvmine);
        TILE(0, 1, own(5), NOP_);
        STAMP_MID(10)
        if (!__all(pollv == tag)) spin_flags(kvmine, tag);
        STAMP_MID(11)
        SIGNAL();
        unsigned cn = chunk_off(2);
        TILEX(1, 0, own(6), true, DKV(cn, L_S2, 0), DKV(cn, L_S2, 1), NOP_, PEEK, DKV(cn, L_S2, 2), DKV(cn, L_S2, 3));
        lds_await(seen, cntp, 4);
        cn = chunk_off(3);
        TILED(0, 1, own(7), cn, L_S0);
        WAITV(4); SIGNAL(); lds_await(0, cntp, 8);
        TILE(1, 0, kvb + L_S2, NOP_);
        cn = chunk_off(4);
        TILED(0, 1, kvb + L_S2 + 4096, cn, L_S1);
        WAITV(4); SIGNAL();
        TILE(1, 0, kvb + L_S2 + 8192, NOP_);
        TILE(0, 1, kvb + L_S2 + 12288, PEEK);
        lds_await(seen, cntp, 12);
        WAITV(0); SIGNAL();
        TILE(1, 0, kvb + L_S0, PEEK);
        STAMP_MID(7)
        int sc = L_S0, sn = L_S1, sp = L_S2;
        for (int m = 3; m <= 13; ++m) {
            const LAS char* cp = kvb + sc;
            cn = chunk_off(m + 2);
            lds_await(seen, cntp, 4 * (m + 1));
            BALANCE(4 * m);
            TILED(0, 1, cp + 4096, cn, sp);
            TILE(1, 0, cp + 8192, NOP_);
            TILE(0, 1, cp + 12288, NOP_);
            WAITV(0); SIGNAL();
            TILE(1, 0, kvb + sn, PEEK);
            const int t_ = sc; sc = sn; sn = sp; sp = t_;
        }
        lds_await(seen, cntp, 60);
        BALANCE(56);
        TILEX(0, 1, kvb + L_S2 + 4096, true, dwu(0, wo_rs(0), 0), dwu(0, wo_rs(0), 1), NOP_, NOP_, NOP_, NOP_);
        BALANCE(57);
        TILEX(1, 0, kvb + L_S2 + 8192, true, dwu(1, wo_rs(1), 0), dwu(1, wo_rs(1), 1), NOP_, NOP_, NOP_, NOP_);
        BALANCE(58);
        TILEX(0, 1, kvb + L_S2 + 12288, true, dwu(2, wo_rs(2), 0), dwu(2, wo_rs(2), 1), NOP_, NOP_, NOP_, NOP_);
        BALANCE(59);
        TILE(1, 0, kvb + L_S0, NOP_);
        BALANCE(60);
        TILE(0, 1, kvb + L_S0 + 4096, NOP_);
        BALANCE(61);
        TILE(1, 0, kvb + L_S0 + 8192, NOP_);
        BALANCE(62);
        TILE(0, 1, kvb + L_S0 + 12288, NOP_);
        TILEX(1, 0, kvb, false, NOP_, NOP_, NOP_, NOP_, NOP_, NOP_);
        lacc[1] = SMF(sel, P, lacc[1], selidx); o[1] = MFMA32(vprev1, PHI(P), o[1]);
    }
#define P3PRIO 0
    if (wn == 0) __builtin_amdgcn_s_setprio(P3PRIO);
    STAMP_MID(3)
    {
        float l[2];
        auto rowsum = [&](int a) { const float t0 = __shfl(lacc[a][0] + lacc[a][1], lane & 15), t1 = __shfl(lacc[a][2] + lacc[a][3], lane & 15); return (lane & 16) ? t1 : t0; };
        bool bad = false;
#pragma unroll
        for (int a = 0; a < 2; ++a) {
            l[a] = rowsum(a);
            float z = l[a] * 0.f, z1 = 0.f;
#pragma unroll
            for (int i = 0; i < 16; i += 2) { z = __builtin_fmaf(o[a][i], 0.f, z); z1 = __builtin_fmaf(o[a][i + 1], 0.f, z1); }
            bad |= !(l[a] < 1.0e18f) || !(z + z1 == 0.f);
        }
        if (__any(bad)) {
            spin_flags(kvfl + (lane & 31), tag);
            const srd_t sk1 = make_srd(ws + WS_KV + (size_t)(b * 32 + h) * 64 * 4096, 64 * 4096);
#define LDKV(t, f) BF(__builtin_amdgcn_raw_buffer_load_b128(sk1, lane * 16 + (f) * 1024, (t) * 4096, 16))
            f32x16 negm[2];
#pragma unroll
            for (int a = 0; a < 2; ++a) {
                float m = -__builtin_inff();
                for (int t = 0; t < 64; ++t) {
                    const bf16x8 k0 = LDKV(t, 0), k1 = LDKV(t, 1);
                    f32x16 z;
#pragma unroll
                    for (int i = 0; i < 16; ++i) z[i] = 0.f;
                    z = MFMA32(k0, qf[a][0], z); z = MFMA32(k1, qf[a][1], z);
#pragma unroll
                    for (int i = 0; i < 16; ++i) m = fmaxf(m, z[i]);
                }
                m = fmaxf(m, __shfl_xor(m, 32));
#pragma unroll
                for (int i = 0; i < 16; ++i) { negm[a][i] = -m; o[a][i] = 0.f; }
                lacc[a] = (f32x4){0.f, 0.f, 0.f, 0.f};
            }
            for (int t = 0; t < 64; ++t) {
                const bf16x8 k0 = LDKV(t, 0), k1 = LDKV(t, 1), v0 = LDKV(t, 2), v1 = LDKV(t, 3);
#pragma unroll
                for (int a = 0; a < 2; ++a) {
                    f32x16 z = MFMA32(k0, qf[a][0], negm[a]); z = MFMA32(k1, qf[a][1], z);
                    u32x8 P;
#pragma unroll
                    for (int i = 0; i < 8; ++i) P[i] = cvtpk(ex2(z[2 * i]), ex2(z[2 * i + 1]));
                    o[a] = MFMA32(v0, PLO(P), o[a]); o[a] = MFMA32(v1, PHI(P), o[a]); lacc[a] = SMF(sel, P, lacc[a], selidx);
                }
            }
            l[0] = rowsum(0); l[1] = rowsum(1);
        }
        const srd_t sow = make_srd(ws + WS_O + (size_t)(b * 64 + mb * 8 + wm * 2) * 65536, 2 * 65536);
#pragma unroll
        for (int a = 0; a < 2; ++a) {
            const float rl = __builtin_amdgcn_rcpf(l[a]);
            __builtin_amdgcn_raw_buffer_store_b128(pack8(o[a], 0, rl), sow, lane * 16 + a * 65536 + h * 2048, 0, 16);
            __builtin_amdgcn_raw_buffer_store_b128(pack8(o[a], 1, rl), sow, lane * 16 + a * 65536 + h * 2048 + 1024, 0, 16);
        }
    }
    GAS u64* const ofl = flags + (FL_O / 8) + b * 1024;
    WAITV(0); if (lane == 0) st_agent(ofl + (mb * 4 + wm) * 32 + h, tag);
    STAMP_MID(4)

    {
        const int wt = wm, kh = wn;
        unsigned seen;
        SIGNAL(); lds_await(0, cntp, 64);
        for (int i = 0; i < 6; ++i) { dwu(3 + i, wo_hs(i), 0); dwu(3 + i, wo_hs(i), 1); }
        spin_flags(ofl + (tm * 2 + (wt >> 1)) * 32 + (lane & 15) * 2 + kh, tag);
        STAMP_MID(5)
        const srd_t sor = make_srd(ws + WS_O + (size_t)(b * 64 + tm * 4 + wt) * 65536 + kh * 2048, 65536 - 2048);
        bf16x8 of[8][2], wf[3][4];
#define P3_LO(u) do { of[(u) & 7][0] = BF(__builtin_amdgcn_raw_buffer_load_b128(sor, lane * 16, (u) * 4096, 16)); \
                      of[(u) & 7][1] = BF(__builtin_amdgcn_raw_buffer_load_b128(sor, lane * 16, (u) * 4096 + 1024, 16)); } while (0)
        P3_LO(0); P3_LO(1); P3_LO(2); P3_LO(3); P3_LO(4); P3_LO(5); P3_LO(6); P3_LO(7);
        SIGNAL();
        unsigned wn3[4];
#pragma unroll
        for (int n = 0; n < 4; ++n) wn3[n] = (4 * (g4 >> 1) + q4) * 256 + ((n ^ (kh << 1) ^ q4) << 6) + (g4 & 1) * 32 + p4 * 8;
        f32x16 acc[4];
#pragma unroll
        for (int n = 0; n < 4; ++n)
#pragma unroll
            for (int i = 0; i < 16; ++i) acc[n][i] = 0.f;
        auto uslot = [&](int u) { return u < 3 ? wo_rs(u) : u < 9 ? wo_hs(u - 3) : u < 12 ? wo_rs(u - 9) : wo_hs(u - 12); };
#define P3_RW(kk, n) do { const LAS char* w_ = lds + uslot((kk) >> 1) + ((kk) & 1) * 4096 + wn3[n]; wf[(kk) % 3][n] = cat8(ldstr(w_), ldstr(w_ + 2048)); } while (0)
#define P3_KS(kk, M0, M1) do { \
            acc[0] = MFMA32(wf[(kk) % 3][0], of[((kk) >> 1) & 7][(kk) & 1], acc[0]); if ((kk) + 2 < 32) P3_RW((kk) + 2, 0); SBAR(); \
            acc[1] = MFMA32(wf[(kk) % 3][1], of[((kk) >> 1) & 7][(kk) & 1], acc[1]); if ((kk) + 2 < 32) P3_RW((kk) + 2, 1); M0; SBAR(); \
            acc[2] = MFMA32(wf[(kk) % 3][2], of[((kk) >> 1) & 7][(kk) & 1], acc[2]); if ((kk) + 2 < 32) P3_RW((kk) + 2, 2); SBAR(); \
            acc[3] = MFMA32(wf[(kk) % 3][3], of[((kk) >> 1) & 7][(kk) & 1], acc[3]); if ((kk) + 2 < 32) P3_RW((kk) + 2, 3); M1; SBAR(); \
        } while (0)
#define P3_UNIT(u, M0) do { P3_KS(2 * (u), M0, NOP_); P3_KS(2 * (u) + 1, NOP_, if ((u) + 8 < 16) P3_LO((u) + 8)); } while (0)
#define DWU(u, slot) do { dwu(u, slot, 0); dwu(u, slot, 1); } while (0)
        P3_RW(0, 0); P3_RW(0, 1); P3_RW(0, 2); P3_RW(0, 3); P3_RW(1, 0); P3_RW(1, 1); P3_RW(1, 2); P3_RW(1, 3); SBAR();
        P3_UNIT(0, NOP_);
        P3_UNIT(1, PEEK);
        lds_await(seen, cntp, 68);
        SIGNAL();
        P3_UNIT(2, PEEK);
        lds_await(seen, cntp, 72); DWU(9, wo_rs(0)); DWU(10, wo_rs(1)); DWU(11, wo_rs(2));
        P3_UNIT(3, NOP_);
        P3_UNIT(4, NOP_);
        SIGNAL();
        P3_UNIT(5, PEEK);
        lds_await(seen, cntp, 76); DWU(12, wo_hs(0)); DWU(13, wo_hs(1)); DWU(14, wo_hs(2));
        P3_UNIT(6, NOP_);
        WAITV(14); SIGNAL();
        P3_UNIT(7, PEEK);
        lds_await(seen, cntp, 80);
        SIGNAL();
        P3_UNIT(8, PEEK);
        lds_await(seen, cntp, 84); DWU(15, wo_hs(3));
        P3_UNIT(9, NOP_);
        WAITV(6); SIGNAL();
        P3_UNIT(10, PEEK);
        lds_await(seen, cntp, 88);
        P3_UNIT(11, NOP_);
        P3_UNIT(12, NOP_);
        WAITV(0); SIGNAL();
        P3_UNIT(13, PEEK);
        lds_await(seen, cntp, 92);
        P3_UNIT(14, NOP_);
        P3_UNIT(15, NOP_);
        STAMP_MID(6)
        LBAR();
        if (tid == 0 && (xcd & 3) == 0 && j == 0) st_agent(flags + b * 32, tag);
        LAS char* mine = lds + L_S0 + wid * 8192 + lane * 16;
        const LAS char* theirs = lds + L_S0 + (wid ^ 4) * 8192 + lane * 16;
#pragma unroll
        for (int n2 = 0; n2 < 2; ++n2)
#pragma unroll
            for (int qd = 0; qd < 4; ++qd)
                *(LAS f32x4*)(mine + n2 * 4096 + qd * 1024) = (f32x4){acc[2 + n2][4 * qd], acc[2 + n2][4 * qd + 1], acc[2 + n2][4 * qd + 2], acc[2 + n2][4 * qd + 3]};
        LBAR();
        const int token = tm * 128 + wt * 32 + r32;
        const srd_t sout = make_srd(out, 2048 * 2048 * 2);
#pragma unroll
        for (int n2 = 0; n2 < 2; ++n2) {
            f32x16 v = acc[n2];
#pragma unroll
            for (int qd = 0; qd < 4; ++qd) {
                const f32x4 t = *(const LAS f32x4*)(theirs + n2 * 4096 + qd * 1024);
                v[4 * qd] += t[0]; v[4 * qd + 1] += t[1]; v[4 * qd + 2] += t[2]; v[4 * qd + 3] += t[3];
            }
            const unsigned oo = (unsigned)token * 4096u + (unsigned)(b * 1024 + tn * 128 + (2 * kh + n2) * 32) * 2u + hh * 16;
#pragma unroll
            for (int gp = 0; gp < 2; ++gp) {
                const unsigned a0 = cvtpk(v[8 * gp], v[8 * gp + 1]), a1 = cvtpk(v[8 * gp + 2], v[8 * gp + 3]);
                const unsigned b0 = cvtpk(v[8 * gp + 4], v[8 * gp + 5]), b1 = cvtpk(v[8 * gp + 6], v[8 * gp + 7]);
                auto r0 = __builtin_amdgcn_permlane32_swap(a0, b0, false, false);
                auto r1 = __builtin_amdgcn_permlane32_swap(a1, b1, false, false);
                const u32x4 w = {r0[0], r1[0], r0[1], r1[1]};
                __builtin_amdgcn_raw_buffer_store_b128(w, sout, oo + gp * 32, 0, OUTAUX);
            }
        }
    }
    STAMP_END(ws)
}

extern "C" void kernel_launch(void* const* d_in, const int* in_sizes, int n_in, void* d_out, int out_size, void* d_ws, size_t ws_size, hipStream_t stream) {
    (void)hipFuncSetAttribute((const void*)mha_fused, hipFuncAttributeMaxDynamicSharedMemorySize, L_END);
    mha_fused<<<256, 512, L_END, stream>>>((const uint16_t*)d_in[0], (const uint16_t*)d_in[1], (const uint16_t*)d_in[2], (const uint16_t*)d_in[3], (const uint16_t*)d_in[4],
                                           (const uint16_t*)d_in[5], (char*)d_ws, (uint16_t*)d_out);
}
```

```cpp
#define PFD 4
#define PFPRO 0
#define P1FINE
#include <hip/hip_runtime.h>
#include <stdint.h>

typedef __bf16 bf16x8 __attribute__((ext_vector_type(8)));
typedef __bf16 bf16x16 __attribute__((ext_vector_type(16)));
typedef __bf16 bf16x2 __attribute__((ext_vector_type(2)));
typedef short s16x4 __attribute__((ext_vector_type(4)));
typedef short s16x8 __attribute__((ext_vector_type(8)));
typedef float f32x16 __attribute__((ext_vector_type(16)));
typedef float f32x4 __attribute__((ext_vector_type(4)));
typedef float f32x2 __attribute__((ext_vector_type(2)));
typedef unsigned u32x4 __attribute__((ext_vector_type(4)));
typedef unsigned u32x8 __attribute__((ext_vector_type(8)));
typedef unsigned long long u64;
#define LAS __attribute__((address_space(3)))
#define GAS __attribute__((address_space(1)))
#define DI __device__ __forceinline__
#define MFMA32(a, b, c) __builtin_amdgcn_mfma_f32_32x32x16_bf16((a), (b), (c), 0, 0, 0)
#define SMF(a, b, c, idx) __builtin_amdgcn_smfmac_f32_16x16x64_bf16((a), __builtin_bit_cast(bf16x16, b), (c), (idx), 0, 0)
#define PLO(P) __builtin_bit_cast(bf16x8, __builtin_shufflevector(P, P, 0, 1, 2, 3))
#define PHI(P) __builtin_bit_cast(bf16x8, __builtin_shufflevector(P, P, 4, 5, 6, 7))
#define SBAR() __builtin_amdgcn_sched_barrier(0)
#define BF(x) __builtin_bit_cast(bf16x8, x)
#define NOP_ (void)0
typedef __amdgpu_buffer_rsrc_t srd_t;

#define OUTAUX 0
#define MAPSEL 1
constexpr float QSCALE = 0.17677669529663687f * 1.4426950408889634f;
constexpr unsigned WS_KV = 0;
constexpr unsigned WS_O = 16u << 20;
constexpr unsigned WS_FL = 24u << 20;
constexpr unsigned FL_KV = 4096;
constexpr unsigned FL_O = FL_KV + 16384;
constexpr int L_S0 = 32768, L_S1 = 65536, L_S2 = 0, L_R0 = 98304, L_R1 = 131072, L_END = 163840, L_CNT = L_END - 64;
constexpr int P1_XS = 32768, P1_WS = 24576, P1_WOFF = 98304;

DI unsigned rfl(unsigned x) { return __builtin_amdgcn_readfirstlane(x); }
DI srd_t make_srd(const void* p, unsigned bytes) { return __builtin_amdgcn_make_buffer_rsrc((void*)p, (short)0, (int)bytes, 0x00020000); }
DI void dma16(unsigned ldsdst, unsigned voff, srd_t srd, unsigned soff) {
    asm volatile("s_mov_b32 m0, %0\n\ts_nop 0\n\tbuffer_load_dwordx4 %1, %2, %3 offen lds" :: "s"(ldsdst), "v"(voff), "s"(srd), "s"(soff) : "memory");
}
DI void dma16c(unsigned ldsdst, unsigned voff, srd_t srd, unsigned soff) {
    asm volatile("s_mov_b32 m0, %0\n\ts_nop 0\n\tbuffer_load_dwordx4 %1, %2, %3 offen sc1 lds" :: "s"(ldsdst), "v"(voff), "s"(srd), "s"(soff) : "memory");
}
template <int I> DI void dma4c(unsigned ldsdst, unsigned voff, srd_t srd, unsigned soff) {
    if constexpr (I == 0) asm volatile("s_mov_b32 m0, %0\n\ts_nop 0\n\tbuffer_load_dwordx4 %1, %2, %3 offen sc1 lds" :: "s"(ldsdst), "v"(voff), "s"(srd), "s"(soff) : "memory");
    else asm volatile("buffer_load_dwordx4 %0, %1, %2 offen offset:%3 sc1 lds" :: "v"(voff), "s"(srd), "s"(soff), "i"(I * 1024) : "memory");
}
DI unsigned cvtpk(float lo, float hi) { f32x2 v = {lo, hi}; bf16x2 b = __builtin_convertvector(v, bf16x2); return __builtin_bit_cast(unsigned, b); }
DI bf16x8 ldsfrag(const LAS char* p) { return *(const LAS bf16x8*)p; }
typedef short v4i16_t __attribute__((ext_vector_type(4)));
DI s16x4 ldstr(const LAS char* p) { return __builtin_amdgcn_ds_read_tr16_b64_v4i16((LAS v4i16_t*)p); }
DI bf16x8 cat8(s16x4 lo, s16x4 hi) { s16x8 r = __builtin_shufflevector(lo, hi, 0, 1, 2, 3, 4, 5, 6, 7); return __builtin_bit_cast(bf16x8, r); }
DI u32x4 pack8(const f32x16& x, int s, float mul) {
    u32x4 p; p[0] = cvtpk(x[8 * s] * mul, x[8 * s + 1] * mul); p[1] = cvtpk(x[8 * s + 2] * mul, x[8 * s + 3] * mul);
    p[2] = cvtpk(x[8 * s + 4] * mul, x[8 * s + 5] * mul); p[3] = cvtpk(x[8 * s + 6] * mul, x[8 * s + 7] * mul); return p; }
DI u32x4 pack8n(const f32x16& x, int s) {
    u32x4 p; p[0] = cvtpk(x[8 * s], x[8 * s + 1]); p[1] = cvtpk(x[8 * s + 2], x[8 * s + 3]);
    p[2] = cvtpk(x[8 * s + 4], x[8 * s + 5]); p[3] = cvtpk(x[8 * s + 6], x[8 * s + 7]); return p; }
#define WAITBAR(vm) asm volatile("s_waitcnt vmcnt(" #vm ") lgkmcnt(0)\n\ts_barrier" ::: "memory")
#define LBAR() asm volatile("s_waitcnt lgkmcnt(0)\n\ts_barrier" ::: "memory")
#define WAITV(vm) asm volatile("s_waitcnt vmcnt(" #vm ")" ::: "memory")
DI float ex2(float x) { return __builtin_amdgcn_exp2f(x); }
DI u64 mix64(u64 z) { z += 0x9E3779B97F4A7C15ull; z = (z ^ (z >> 30)) * 0xBF58476D1CE4E5B9ull; z = (z ^ (z >> 27)) * 0x94D049BB133111EBull; return z ^ (z >> 31); }
DI u64 ld_agent(const GAS u64* p) { return __hip_atomic_load(p, __ATOMIC_RELAXED, __HIP_MEMORY_SCOPE_AGENT); }
DI void st_agent(GAS u64* p, u64 v) { __hip_atomic_store(p, v, __ATOMIC_RELAXED, __HIP_MEMORY_SCOPE_AGENT); }
DI void spin_flags(const GAS u64* f, u64 tag) {
    const u64 t0 = __builtin_amdgcn_s_memrealtime();
    u64 v0 = ld_agent(f);
    for (;;) {
        __builtin_amdgcn_s_sleep(6);
        const u64 v1 = ld_agent(f);
        if (__all(v0 == tag)) break;
        __builtin_amdgcn_s_sleep(6);
        v0 = ld_agent(f);
        if (__all(v1 == tag)) break;
        if (__builtin_amdgcn_s_memrealtime() - t0 > 20000000ull) break;
    }
    asm volatile("" ::: "memory");
}
DI void lds_signal(unsigned addr) {
    unsigned one = 1;
    asm volatile("s_waitcnt lgkmcnt(0)\n\ts_mov_b64 exec, 1\n\tds_add_u32 %0, %1\n\ts_mov_b64 exec, -1" :: "v"(addr), "v"(one) : "memory");
}
DI unsigned lds_peek(const LAS char* p) { return *(const volatile LAS unsigned*)p; }
DI void lds_await(unsigned seen, const LAS char* p, unsigned need) {
    if (rfl(seen) < need) {
        _Pragma("nounroll") for (int it = 0; it < (1 << 22); ++it) { if (rfl(lds_peek(p)) >= need) break; __builtin_amdgcn_s_sleep(0); }
    }
    asm volatile("" ::: "memory");
}

#define STAMP_BEGIN
#define STAMP_MID(i)
#define STAMP_END(ws)

__global__ __launch_bounds__(512, 2) void mha_fused(const uint16_t* xA, const uint16_t* wA, const uint16_t* woA, const uint16_t* xB, const uint16_t* wB, const uint16_t* woB,
                                                    char* ws, uint16_t* out) {
    extern __shared__ __attribute__((aligned(16))) char lds_[];
    LAS char* lds = (LAS char*)lds_;
    STAMP_BEGIN
    const int tid = threadIdx.x, lane = tid & 63, wid = rfl(tid >> 6);
    const int bid = blockIdx.x, xcd = bid & 7, j = bid >> 3;
    const int b = xcd >> 2;
    const int mb = (xcd & 1) * 4 + (j & 3), hp = ((xcd >> 1) & 1) * 8 + (j >> 2);
    const int wm = wid & 3, wn = wid >> 2, h = hp * 2 + wn;
    const unsigned lds0 = (unsigned)(uintptr_t)lds_;
    const int r32 = lane & 31, hh = lane >> 5, g4 = lane >> 4, q4 = (lane >> 2) & 3, p4 = lane & 3;
    GAS u64* const flags = (GAS u64*)(ws + WS_FL);
    u64 epoch = ld_agent(flags + b * 32);
    if (tid < 16) *(LAS unsigned*)(lds + L_CNT + tid * 4) = 0u;

    bf16x8 qf[2][2];
    u32x4 kvp[2][4];
    const srd_t skvw = make_srd(ws + WS_KV + (size_t)(b * 32 + h) * 64 * 4096 + (size_t)(mb * 8 + wm * 2) * 4096, 2 * 4096);
    {
        const srd_t sx = make_srd(b ? xB : xA, 2048 * 1024 * 2), sw = make_srd(b ? wB : wA, 1024 * 3072 * 2);
        const unsigned vx0 = (lane >> 3) * 2048 + (((lane & 7) ^ (lane >> 4)) << 4);
        const unsigned vx1 = (lane >> 3) * 2048 + (((lane & 7) ^ (4 + (lane >> 4))) << 4);
        const unsigned vw = (lane >> 3) * 6144 + (((lane & 7) ^ (((lane >> 4) & 1) << 2) ^ ((wid & 1) << 1)) << 4);
        const unsigned sx0 = (mb * 256 + wid * 32) * 2048, sw0 = wid * 8 * 6144 + hp * 128;
        auto dx = [&](int kt, int i) { dma16(lds0 + (kt % 3) * P1_XS + wid * 4096 + i * 1024, (i & 1) ? vx1 : vx0, sx, sx0 + i * 8 * 2048 + kt * 128); };
        auto dw = [&](int kt, int i) { dma16(lds0 + P1_WOFF + (kt & 1) * P1_WS + wid * 1024 + i * 8192, vw, sw, sw0 + i * 2048 + kt * 64 * 6144); };
        for (int i = 0; i < 3; ++i) dw(0, i);
        for (int i = 0; i < 4; ++i) dx(0, i);
        for (int i = 0; i < 3; ++i) dw(1, i);
        for (int i = 0; i < 4; ++i) dx(1, i);
        for (int i = 0; i < 4; ++i) dx(2, i);
        const char* pfp; unsigned pfstep;
        {
            const int l = lane < 10 ? lane : 9;
            const int xrow = (xcd & 1) * 1024 + j * 32 + wid * 4 + (l & 3), wl = wid * 6 + (l - 4);
            const size_t woff = (size_t)(j * 2 + wl / 24) * 6144 + ((wl % 24) >> 3) * 2048 + (((xcd >> 1) & 1) * 8 + (wl & 7)) * 128;
            pfp = l < 4 ? (const char*)(b ? xB : xA) + (size_t)xrow * 2048 : (const char*)(b ? wB : wA) + woff;
            pfstep = l < 4 ? 128u : 64u * 6144u;
        }
        const unsigned pfdst = lds0 + 147456 + wid * 1024;
        auto pf = [&](int kt) {
            const char* g = pfp + (size_t)kt * pfstep;
            asm volatile("s_mov_b32 m0, %0\n\ts_nop 0\n\tglobal_load_lds_dword %1, off" :: "s"(pfdst), "v"(g) : "memory"); };
        if (PFD) for (int i = 0; i < PFPRO; ++i) pf(3 + i);
        const int r16 = lane & 15;
        unsigned xa[2], wb[2];
#pragma unroll
        for (int s = 0; s < 2; ++s) xa[s] = (wm * 64 + r16) * 128 + (((4 * s + g4) ^ (r16 >> 1)) << 4);
#pragma unroll
        for (int c = 0; c < 2; ++c) wb[c] = P1_WOFF + (8 * g4 + q4) * 128 + ((wn ^ (q4 >> 1)) << 6) + ((c ^ (g4 & 1)) << 5) + p4 * 8;
        f32x4 acc[4][6];
#pragma unroll
        for (int a = 0; a < 4; ++a)
#pragma unroll
            for (int c = 0; c < 6; ++c) acc[a][c] = (f32x4){0.f, 0.f, 0.f, 0.f};
        bf16x8 xf[2][4][2], wf[3][2];
#define MFMA16(a, b, c) __builtin_amdgcn_mfma_f32_16x16x32_bf16((a), (b), (c), 0, 0, 0)
#define P1_RX(kt, tb, s) xf[(kt) & 1][tb][s] = ldsfrag(lds + ((kt) % 3) * P1_XS + xa[s] + (tb) * 2048)
#define P1_RW(kt, fb, s) do { const LAS char* wp_ = lds + wb[(fb) & 1] + ((kt) & 1) * P1_WS + ((fb) >> 1) * 8192 + (s) * 4096; wf[(fb) % 3][s] = cat8(ldstr(wp_), ldstr(wp_ + 512)); } while (0)
#define P1_MM(kt, tb, fb) do { if ((fb) < 4) { acc[tb][fb] = MFMA16(wf[(fb) % 3][0], xf[(kt) & 1][tb][0], acc[tb][fb]); acc[tb][fb] = MFMA16(wf[(fb) % 3][1], xf[(kt) & 1][tb][1], acc[tb][fb]); } \
                               else { acc[tb][fb] = MFMA16(xf[(kt) & 1][tb][0], wf[(fb) % 3][0], acc[tb][fb]); acc[tb][fb] = MFMA16(xf[(kt) & 1][tb][1], wf[(fb) % 3][1], acc[tb][fb]); } } while (0)
        s16x4 wlo[3][2], whi[3][2];
#define P1_WL(kt, fb, s) wlo[(fb) % 3][s] = ldstr(lds + wb[(fb) & 1] + ((kt) & 1) * P1_WS + ((fb) >> 1) * 8192 + (s) * 4096)
#define P1_WH(kt, fb, s) whi[(fb) % 3][s] = ldstr(lds + wb[(fb) & 1] + ((kt) & 1) * P1_WS + ((fb) >> 1) * 8192 + (s) * 4096 + 512)
#define P1_M1(kt, tb, fb, s) do { const bf16x8 w_ = cat8(wlo[(fb) % 3][s], whi[(fb) % 3][s]); if ((fb) < 4) acc[tb][fb] = MFMA16(w_, xf[(kt) & 1][tb][s], acc[tb][fb]); else acc[tb][fb] = MFMA16(xf[(kt) & 1][tb][s], w_, acc[tb][fb]); } while (0)
#define P1_FB8(kt, fb, R0, R1, R2, R3, R4, R5, R6, R7) do { \
            P1_M1(kt, 0, fb, 0); R0; SBAR(); P1_M1(kt, 1, fb, 0); R1; SBAR(); P1_M1(kt, 2, fb, 0); R2; SBAR(); P1_M1(kt, 3, fb, 0); R3; SBAR(); \
            P1_M1(kt, 0, fb, 1); R4; SBAR(); P1_M1(kt, 1, fb, 1); R5; SBAR(); P1_M1(kt, 2, fb, 1); R6; SBAR(); P1_M1(kt, 3, fb, 1); R7; SBAR(); } while (0)
#define P1_FB(kt, fb, R0, R1, R2, R3) do { \
            P1_MM(kt, 0, fb); R0; SBAR(); P1_MM(kt, 1, fb); R1; SBAR(); P1_MM(kt, 2, fb); R2; SBAR(); P1_MM(kt, 3, fb); R3; SBAR(); } while (0)
        asm volatile("s_waitcnt vmcnt(%0) lgkmcnt(0)\n\ts_barrier" :: "n"(11 + (PFD ? PFPRO : 0)) : "memory");
        STAMP_MID(0)
        P1_RX(0, 0, 0); P1_WL(0, 0, 0); P1_WH(0, 0, 0); P1_RX(0, 1, 0); P1_RX(0, 2, 0); P1_RX(0, 3, 0); P1_WL(0, 0, 1); P1_WH(0, 0, 1);
        P1_RX(0, 0, 1); P1_RX(0, 1, 1); P1_RX(0, 2, 1); P1_RX(0, 3, 1); P1_WL(0, 1, 0); P1_WH(0, 1, 0); P1_WL(0, 1, 1); P1_WH(0, 1, 1); SBAR();
#pragma unroll
        for (int t = 0; t < 16; ++t) {
            const bool xs = (t >= 1 && t + 2 < 16);
            P1_FB8(t, 0, P1_WL(t, 2, 0), NOP_, P1_WH(t, 2, 0), NOP_, P1_WL(t, 2, 1), if (xs) dx(t + 2, 0), P1_WH(t, 2, 1), NOP_);
            P1_FB8(t, 1, P1_WL(t, 3, 0), NOP_, P1_WH(t, 3, 0), NOP_, P1_WL(t, 3, 1), if (xs) dx(t + 2, 1), P1_WH(t, 3, 1), NOP_);
            P1_FB8(t, 2, P1_WL(t, 4, 0), NOP_, P1_WH(t, 4, 0), NOP_, P1_WL(t, 4, 1), if (xs) dx(t + 2, 2), P1_WH(t, 4, 1), NOP_);
            P1_FB8(t, 3, P1_WL(t, 5, 0), NOP_, P1_WH(t, 5, 0), NOP_, P1_WL(t, 5, 1), if (xs) dx(t + 2, 3), P1_WH(t, 5, 1), NOP_);
            if (t < 15) {
                { const int npf = !PFD ? 0 : t == 0 ? PFPRO : (t - 1 + PFD < 16 ? 1 : 0);
                  const int nw = (t < 14 ? 4 : 0) + (t < 14 ? npf : 0);
                  if (nw == 0) WAITBAR(0); else if (nw == 4) WAITBAR(4); else if (nw == 5) WAITBAR(5); else asm volatile("s_waitcnt vmcnt(%0) lgkmcnt(0)\n\ts_barrier" :: "n"(4 + PFPRO) : "memory"); }
                const bool wsch = (t + 2 < 16);
                P1_FB8(t, 4, P1_RX(t + 1, 0, 0), P1_WL(t + 1, 0, 0), P1_WH(t + 1, 0, 0), P1_RX(t + 1, 1, 0), P1_RX(t + 1, 2, 0), P1_RX(t + 1, 3, 0), P1_WL(t + 1, 0, 1), { P1_WH(t + 1, 0, 1); if (wsch) dw(t + 2, 0); });
                P1_FB8(t, 5, P1_RX(t + 1, 0, 1), P1_RX(t + 1, 1, 1), P1_RX(t + 1, 2, 1), { P1_RX(t + 1, 3, 1); if (wsch) dw(t + 2, 1); }, P1_WL(t + 1, 1, 0), { P1_WH(t + 1, 1, 0); if (wsch) dw(t + 2, 2); }, P1_WL(t + 1, 1, 1), { P1_WH(t + 1, 1, 1); if (PFD && t + PFD < 16) pf(t + PFD); });
            } else {
                P1_FB8(t, 4, NOP_, NOP_, NOP_, NOP_, NOP_, NOP_, NOP_, NOP_);
                P1_FB8(t, 5, NOP_, NOP_, NOP_, NOP_, NOP_, NOP_, NOP_, NOP_);
            }
        }
        STAMP_MID(1)
        auto swap16 = [](unsigned& x, unsigned& y) { auto r = __builtin_amdgcn_permlane16_swap(x, y, false, false); x = r[0]; y = r[1]; };
        auto swap32 = [](unsigned& x, unsigned& y) { auto r = __builtin_amdgcn_permlane32_swap(x, y, false, false); x = r[0]; y = r[1]; };
        auto qkfrag = [&](const f32x4& t0, const f32x4& t1, float mul) {
            unsigned a0 = cvtpk(t0[0] * mul, t0[1] * mul), a1 = cvtpk(t0[2] * mul, t0[3] * mul), b0 = cvtpk(t1[0] * mul, t1[1] * mul), b1 = cvtpk(t1[2] * mul, t1[3] * mul);
            swap16(a0, b0); swap16(a1, b1);
            return (u32x4){a0, a1, b0, b1}; };
        auto vfrag = [&](const f32x4& x0, const f32x4& x1) {
            unsigned a0 = cvtpk(x0[0], x0[1]), a1 = cvtpk(x0[2], x0[3]), b0 = cvtpk(x1[0], x1[1]), b1 = cvtpk(x1[2], x1[3]);
            swap16(a0, b0); swap16(a1, b1); swap32(a0, b0); swap32(a1, b1);
            return (u32x4){a0, a1, b0, b1}; };
#pragma unroll
        for (int a = 0; a < 2; ++a)
#pragma unroll
            for (int s = 0; s < 2; ++s) {
                kvp[a][s] = qkfrag(acc[2 * a][2 + s], acc[2 * a + 1][2 + s], 1.0f); kvp[a][2 + s] = vfrag(acc[2 * a + s][4], acc[2 * a + s][5]);
                __builtin_amdgcn_raw_buffer_store_b128(kvp[a][s], skvw, lane * 16 + a * 4096 + s * 1024, 0, 16);
                __builtin_amdgcn_raw_buffer_store_b128(kvp[a][2 + s], skvw, lane * 16 + a * 4096 + 2048 + s * 1024, 0, 16);
            }
#define P1_QF(a, s) BF(qkfrag(acc[2 * (a)][s], acc[2 * (a) + 1][s], QSCALE))
#pragma unroll
        for (int a = 0; a < 2; ++a) {
            const int tl = wm * 2 + a;
            LAS char* lp = lds + L_S0 + (tl >> 2) * 32768 + wn * 16384 + (tl & 3) * 4096 + lane * 16;
#pragma unroll
            for (int f = 0; f < 4; ++f) *(LAS u32x4*)(lp + f * 1024) = kvp[a][f];
        }
        SBAR();
#pragma unroll
        for (int a = 0; a < 2; ++a)
#pragma unroll
            for (int s = 0; s < 2; ++s) qf[a][s] = P1_QF(a, s);
    }
    asm volatile("" : "+v"(epoch));
    const u64 tag = mix64(((u64)rfl((unsigned)(epoch >> 32)) << 32) | rfl((unsigned)epoch));
    STAMP_MID(2)

    const srd_t skv = make_srd(ws + WS_KV + (size_t)(b * 32 + hp * 2) * 64 * 4096, 2 * 64 * 4096);
    const srd_t swo = make_srd(b ? woB : woA, 1024 * 1024 * 2);
    const int tm = (xcd & 3) * 4 + (j >> 3), tn = j & 7;
    const unsigned vl = lane * 16;
    const unsigned vwo = (lane >> 4) * 2048 + (((lane & 15) ^ (((lane >> 4) & 3) << 2)) << 4);
    auto chunk_off = [&](int c) { const int i = (c - 2) >> 1; const int blk = c < 2 ? mb : (i < mb ? i : i + 1); return (unsigned)((wn * 64 + blk * 8 + (c & 1) * 4 + wm) * 4096); };
    const unsigned ldw = lds0 + wid * 4096;
#define DKV(coff, slotoff, i) dma4c<i>(ldw + (slotoff), vl, skv, coff)
    const unsigned wo0 = (wn * 32 + wm * 8) * 2048 + tn * 256;
    auto dwu = [&](int u, unsigned slotaddr, int p) { dma16(lds0 + slotaddr + wm * 2048 + p * 1024, vwo, swo, wo0 + (u * 64 + p * 4) * 2048); };
    auto wo_rs = [&](int k) { return (unsigned)(L_R0 + wn * 24576 + k * 8192); };
    auto wo_hs = [&](int k) { return (unsigned)((k >> 1 == 0 ? L_S0 : k >> 1 == 1 ? L_S1 : L_S2) + wn * 16384 + (k & 1) * 8192); };
    bf16x8 sel; int selidx;
    { const int i16 = lane & 15; const short lo = (i16 < 2) ? (short)0x3F80 : (short)0, hi = (i16 == 2 || i16 == 3) ? (short)0x3F80 : (short)0;
      s16x8 t = {lo, lo, lo, lo, hi, hi, hi, hi}; sel = __builtin_bit_cast(bf16x8, t); selidx = (i16 & 1) ? 0xEEEE : 0x4444; }
    const LAS char* kvb = lds + wn * 16384 + lane * 16;
    GAS u64* const kvfl = flags + (FL_KV / 8) + (b * 32 + h) * 32;
    const GAS u64* const kvmine = kvfl + ((lane >> 1) & 7) * 4 + (lane & 1) * 2 + (wm >> 1);
    const LAS char* cntp = lds + L_CNT + wn * 16;
    const unsigned cnta = lds0 + L_CNT + wn * 16;
    f32x16 o[2]; f32x4 lacc[2];
    {
        f32x16 negm[2], S[2];
        bf16x8 kf[2][2], vf[2][2];
        u32x8 P;
        const f32x16 zero16 = {0.f,0.f,0.f,0.f,0.f,0.f,0.f,0.f,0.f,0.f,0.f,0.f,0.f,0.f,0.f,0.f};
        kf[0][0] = BF(kvp[0][0]); kf[0][1] = BF(kvp[0][1]); vf[0][0] = BF(kvp[0][2]); vf[0][1] = BF(kvp[0][3]);
#pragma unroll
        for (int a = 0; a < 2; ++a) {
            f32x16 z;
#pragma unroll
            for (int i = 0; i < 16; ++i) z[i] = 0.f;
            z = MFMA32(kf[0][0], qf[a][0], z); z = MFMA32(kf[0][1], qf[a][1], z);
            float m = z[0];
#pragma unroll
            for (int i = 1; i < 16; ++i) m = fmaxf(m, z[i]);
            m = fmaxf(m, __shfl_xor(m, 32));
#pragma unroll
            for (int i = 0; i < 16; ++i) { negm[a][i] = -m; o[a][i] = 0.f; }
            lacc[a] = (f32x4){0.f, 0.f, 0.f, 0.f};
        }
        S[0] = MFMA32(kf[0][0], qf[0][0], negm[0]); S[0] = MFMA32(kf[0][1], qf[0][1], S[0]);
        P = (u32x8){0u, 0u, 0u, 0u, 0u, 0u, 0u, 0u};
        bf16x8 vprev1 = vf[0][1];
        unsigned seen;
#define EXPI(X, i) X[i] = ex2(X[i])
#define PVM(O, V, PP) O = MFMA32(V, PP, O)
#define PKA(L, X, i) NOP_
#define NEGSEL(N) N
#define UNIT(SX, SN, KA0, KA1, QB0, QB1, NEG, DOQK, OPREV, LPREV, VPREV1, OCUR, LCUR, VCUR0, H0, H1, H2, H3) do { \
            if (DOQK) SN = MFMA32(KA0, QB0, NEGSEL(NEG)); H0; SBAR(); \
            EXPI(SX, 0); EXPI(SX, 1); EXPI(SX, 2); SBAR(); \
            LPREV = SMF(sel, P, LPREV, selidx); SBAR(); \
            EXPI(SX, 3); P[0] = cvtpk(SX[0], SX[1]); P[1] = cvtpk(SX[2], SX[3]); PKA(LCUR, SX, 0); PKA(LCUR, SX, 2); SBAR(); \
            PVM(OPREV, VPREV1, PHI(P)); H1; SBAR(); \
            EXPI(SX, 4); EXPI(SX, 5); EXPI(SX, 6); EXPI(SX, 7); P[2] = cvtpk(SX[4], SX[5]); P[3] = cvtpk(SX[6], SX[7]); PKA(LCUR, SX, 4); PKA(LCUR, SX, 6); SBAR(); \
            if (DOQK) SN = MFMA32(KA1, QB1, SN); H2; SBAR(); \
            EXPI(SX, 8); EXPI(SX, 9); EXPI(SX, 10); EXPI(SX, 11); P[4] = cvtpk(SX[8], SX[9]); P[5] = cvtpk(SX[10], SX[11]); PKA(LCUR, SX, 8); PKA(LCUR, SX, 10); SBAR(); \
            PVM(OCUR, VCUR0, PLO(P)); H3; SBAR(); \
            EXPI(SX, 12); EXPI(SX, 13); EXPI(SX, 14); EXPI(SX, 15); P[6] = cvtpk(SX[12], SX[13]); P[7] = cvtpk(SX[14], SX[15]); PKA(LCUR, SX, 12); PKA(LCUR, SX, 14); SBAR(); \
        } while (0)
#define TILEG(cur, nxt, LK, LV, LOAD, A2, A3, B0, B1, B2, B3) do { \
            UNIT(S[0], S[1], kf[cur][0], kf[cur][1], qf[1][0], qf[1][1], negm[1], true, o[1], lacc[1], vprev1, o[0], lacc[0], vf[cur][0], LK, LV, A2, A3); \
            UNIT(S[1], S[0], kf[nxt][0], kf[nxt][1], qf[0][0], qf[0][1], negm[0], LOAD, o[0], lacc[0], vf[cur][1], o[1], lacc[1], vf[cur][0], B0, B1, B2, B3); \
            vprev1 = vf[cur][1]; \
        } while (0)
#define TILEX(cur, nxt, rdp, LOAD, A2, A3, B0, B1, B2, B3) TILEG(cur, nxt, \
            if (LOAD) { kf[nxt][0] = ldsfrag(rdp); kf[nxt][1] = ldsfrag((rdp) + 1024); }, if (LOAD) { vf[nxt][0] = ldsfrag((rdp) + 2048); vf[nxt][1] = ldsfrag((rdp) + 3072); }, \
            LOAD, A2, A3, B0, B1, B2, B3)
#define TILE(cur, nxt, rdp, PK) TILEX(cur, nxt, rdp, true, NOP_, NOP_, NOP_, PK, NOP_, NOP_)
#define TILED(cur, nxt, rdp, coff, slot) TILEX(cur, nxt, rdp, true, DKV(coff, slot, 0), DKV(coff, slot, 1), NOP_, NOP_, DKV(coff, slot, 2), DKV(coff, slot, 3))
#define PEEK seen = lds_peek(cntp)
#define SIGNAL() lds_signal(cnta)
#define FLIP 56
#define BALANCE(m) do { if (wn == 0 && (m) == FLIP) __builtin_amdgcn_s_setprio(0); } while (0)
        if (wn == 0) __builtin_amdgcn_s_setprio(2); else __builtin_amdgcn_s_setprio(1);
        auto own = [&](int p) {
            const int lo = wm < 2;
            const int off = lo ? (p < 4 ? L_S0 + ((1 - wm) * 2 + p - 2) * 4096 : L_S1 + (p - 4) * 4096) : (p < 6 ? L_S0 + (p - 2) * 4096 : L_S1 + ((3 - wm) * 2 + p - 6) * 4096);
            return kvb + off; };
        TILEG(0, 1, { kf[1][0] = BF(kvp[1][0]); kf[1][1] = BF(kvp[1][1]); }, { vf[1][0] = BF(kvp[1][2]); vf[1][1] = BF(kvp[1][3]); }, true, NOP_, NOP_, NOP_, NOP_, NOP_, NOP_);
        LBAR();
        TILE(1, 0, own(2), NOP_);
        TILE(0, 1, own(3), NOP_);
        STAMP_MID(8)
        WAITV(0); if (lane == 0) st_agent(kvfl + mb * 4 + wm, tag);
        STAMP_MID(9)
        TILE(1, 0, own(4), NOP_);
        u64 pollv = ld_agent(kvmine);
        TILE(0, 1, own(5), NOP_);
        STAMP_MID(10)
        if (!__all(pollv == tag)) spin_flags(kvmine, tag);
        STAMP_MID(11)
        SIGNAL();
        unsigned cn = chunk_off(2);
        TILEX(1, 0, own(6), true, DKV(cn, L_S2, 0), DKV(cn, L_S2, 1), NOP_, PEEK, DKV(cn, L_S2, 2), DKV(cn, L_S2, 3));
        lds_await(seen, cntp, 4);
        cn = chunk_off(3);
        TILED(0, 1, own(7), cn, L_S0);
        WAITV(4); SIGNAL(); lds_await(0, cntp, 8);
        TILE(1, 0, kvb + L_S2, NOP_);
        cn = chunk_off(4);
        TILED(0, 1, kvb + L_S2 + 4096, cn, L_S1);
        WAITV(4); SIGNAL();
        TILE(1, 0, kvb + L_S2 + 8192, NOP_);
        TILE(0, 1, kvb + L_S2 + 12288, PEEK);
        lds_await(seen, cntp, 12);
        WAITV(0); SIGNAL();
        TILE(1, 0, kvb + L_S0, PEEK);
        STAMP_MID(7)
        int sc = L_S0, sn = L_S1, sp = L_S2;
        for (int m = 3; m <= 13; ++m) {
            const LAS char* cp = kvb + sc;
            cn = chunk_off(m + 2);
            lds_await(seen, cntp, 4 * (m + 1));
            BALANCE(4 * m);
            TILED(0, 1, cp + 4096, cn, sp);
            TILE(1, 0, cp + 8192, NOP_);
            TILE(0, 1, cp + 12288, NOP_);
            WAITV(0); SIGNAL();
            TILE(1, 0, kvb + sn, PEEK);
            const int t_ = sc; sc = sn; sn = sp; sp = t_;
        }
        lds_await(seen, cntp, 60);
        BALANCE(56);
        TILEX(0, 1, kvb + L_S2 + 4096, true, dwu(0, wo_rs(0), 0), dwu(0, wo_rs(0), 1), NOP_, NOP_, NOP_, NOP_);
        BALANCE(57);
        TILEX(1, 0, kvb + L_S2 + 8192, true, dwu(1, wo_rs(1), 0), dwu(1, wo_rs(1), 1), NOP_, NOP_, NOP_, NOP_);
        BALANCE(58);
        TILEX(0, 1, kvb + L_S2 + 12288, true, dwu(2, wo_rs(2), 0), dwu(2, wo_rs(2), 1), NOP_, NOP_, NOP_, NOP_);
        BALANCE(59);
        TILE(1, 0, kvb + L_S0, NOP_);
        BALANCE(60);
        TILE(0, 1, kvb + L_S0 + 4096, NOP_);
        BALANCE(61);
        TILE(1, 0, kvb + L_S0 + 8192, NOP_);
        BALANCE(62);
        TILE(0, 1, kvb + L_S0 + 12288, NOP_);
        TILEX(1, 0, kvb, false, NOP_, NOP_, NOP_, NOP_, NOP_, NOP_);
        lacc[1] = SMF(sel, P, lacc[1], selidx); o[1] = MFMA32(vprev1, PHI(P), o[1]);
    }
#define P3PRIO 0
    if (wn == 0) __builtin_amdgcn_s_setprio(P3PRIO);
    STAMP_MID(3)
    {
        float l[2];
        auto rowsum = [&](int a) { const float t0 = __shfl(lacc[a][0] + lacc[a][1], lane & 15), t1 = __shfl(lacc[a][2] + lacc[a][3], lane & 15); return (lane & 16) ? t1 : t0; };
        bool bad = false;
#pragma unroll
        for (int a = 0; a < 2; ++a) {
            l[a] = rowsum(a);
            float z = l[a] * 0.f, z1 = 0.f;
#pragma unroll
            for (int i = 0; i < 16; i += 2) { z = __builtin_fmaf(o[a][i], 0.f, z); z1 = __builtin_fmaf(o[a][i + 1], 0.f, z1); }
            bad |= !(l[a] < 1.0e18f) || !(z + z1 == 0.f);
        }
        if (__any(bad)) {
            spin_flags(kvfl + (lane & 31), tag);
            const srd_t sk1 = make_srd(ws + WS_KV + (size_t)(b * 32 + h) * 64 * 4096, 64 * 4096);
#define LDKV(t, f) BF(__builtin_amdgcn_raw_buffer_load_b128(sk1, lane * 16 + (f) * 1024, (t) * 4096, 16))
            f32x16 negm[2];
#pragma unroll
            for (int a = 0; a < 2; ++a) {
                float m = -__builtin_inff();
                for (int t = 0; t < 64; ++t) {
                    const bf16x8 k0 = LDKV(t, 0), k1 = LDKV(t, 1);
                    f32x16 z;
#pragma unroll
                    for (int i = 0; i < 16; ++i) z[i] = 0.f;
                    z = MFMA32(k0, qf[a][0], z); z = MFMA32(k1, qf[a][1], z);
#pragma unroll
                    for (int i = 0; i < 16; ++i) m = fmaxf(m, z[i]);
                }
                m = fmaxf(m, __shfl_xor(m, 32));
#pragma unroll
                for (int i = 0; i < 16; ++i) { negm[a][i] = -m; o[a][i] = 0.f; }
                lacc[a] = (f32x4){0.f, 0.f, 0.f, 0.f};
            }
            for (int t = 0; t < 64; ++t) {
                const bf16x8 k0 = LDKV(t, 0), k1 = LDKV(t, 1), v0 = LDKV(t, 2), v1 = LDKV(t, 3);
#pragma unroll
                for (int a = 0; a < 2; ++a) {
                    f32x16 z = MFMA32(k0, qf[a][0], negm[a]); z = MFMA32(k1, qf[a][1], z);
                    u32x8 P;
#pragma unroll
                    for (int i = 0; i < 8; ++i) P[i] = cvtpk(ex2(z[2 * i]), ex2(z[2 * i + 1]));
                    o[a] = MFMA32(v0, PLO(P), o[a]); o[a] = MFMA32(v1, PHI(P), o[a]); lacc[a] = SMF(sel, P, lacc[a], selidx);
                }
            }
            l[0] = rowsum(0); l[1] = rowsum(1);
        }
        const srd_t sow = make_srd(ws + WS_O + (size_t)(b * 64 + mb * 8 + wm * 2) * 65536, 2 * 65536);
#pragma unroll
        for (int a = 0; a < 2; ++a) {
            const float rl = __builtin_amdgcn_rcpf(l[a]);
            __builtin_amdgcn_raw_buffer_store_b128(pack8(o[a], 0, rl), sow, lane * 16 + a * 65536 + h * 2048, 0, 16);
            __builtin_amdgcn_raw_buffer_store_b128(pack8(o[a], 1, rl), sow, lane * 16 + a * 65536 + h * 2048 + 1024, 0, 16);
        }
    }
    GAS u64* const ofl = flags + (FL_O / 8) + b * 1024;
    WAITV(0); if (lane == 0) st_agent(ofl + (mb * 4 + wm) * 32 + h, tag);
    STAMP_MID(4)

    {
        const int wt = wm, kh = wn;
        unsigned seen;
        SIGNAL(); lds_await(0, cntp, 64);
        for (int i = 0; i < 6; ++i) { dwu(3 + i, wo_hs(i), 0); dwu(3 + i, wo_hs(i), 1); }
        spin_flags(ofl + (tm * 2 + (wt >> 1)) * 32 + (lane & 15) * 2 + kh, tag);
        STAMP_MID(5)
        const srd_t sor = make_srd(ws + WS_O + (size_t)(b * 64 + tm * 4 + wt) * 65536 + kh * 2048, 65536 - 2048);
        bf16x8 of[8][2], wf[3][4];
#define P3_LO(u) do { of[(u) & 7][0] = BF(__builtin_amdgcn_raw_buffer_load_b128(sor, lane * 16, (u) * 4096, 16)); \
                      of[(u) & 7][1] = BF(__builtin_amdgcn_raw_buffer_load_b128(sor, lane * 16, (u) * 4096 + 1024, 16)); } while (0)
        P3_LO(0); P3_LO(1); P3_LO(2); P3_LO(3); P3_LO(4); P3_LO(5); P3_LO(6); P3_LO(7);
        SIGNAL();
        unsigned wn3[4];
#pragma unroll
        for (int n = 0; n < 4; ++n) wn3[n] = (4 * (g4 >> 1) + q4) * 256 + ((n ^ (kh << 1) ^ q4) << 6) + (g4 & 1) * 32 + p4 * 8;
        f32x16 acc[4];
#pragma unroll
        for (int n = 0; n < 4; ++n)
#pragma unroll
            for (int i = 0; i < 16; ++i) acc[n][i] = 0.f;
        auto uslot = [&](int u) { return u < 3 ? wo_rs(u) : u < 9 ? wo_hs(u - 3) : u < 12 ? wo_rs(u - 9) : wo_hs(u - 12); };
#define P3_RW(kk, n) do { const LAS char* w_ = lds + uslot((kk) >> 1) + ((kk) & 1) * 4096 + wn3[n]; wf[(kk) % 3][n] = cat8(ldstr(w_), ldstr(w_ + 2048)); } while (0)
#define P3_KS(kk, M0, M1) do { \
            acc[0] = MFMA32(wf[(kk) % 3][0], of[((kk) >> 1) & 7][(kk) & 1], acc[0]); if ((kk) + 2 < 32) P3_RW((kk) + 2, 0); SBAR(); \
            acc[1] = MFMA32(wf[(kk) % 3][1], of[((kk) >> 1) & 7][(kk) & 1], acc[1]); if ((kk) + 2 < 32) P3_RW((kk) + 2, 1); M0; SBAR(); \
            acc[2] = MFMA32(wf[(kk) % 3][2], of[((kk) >> 1) & 7][(kk) & 1], acc[2]); if ((kk) + 2 < 32) P3_RW((kk) + 2, 2); SBAR(); \
            acc[3] = MFMA32(wf[(kk) % 3][3], of[((kk) >> 1) & 7][(kk) & 1], acc[3]); if ((kk) + 2 < 32) P3_RW((kk) + 2, 3); M1; SBAR(); \
        } while (0)
#define P3_UNIT(u, M0) do { P3_KS(2 * (u), M0, NOP_); P3_KS(2 * (u) + 1, NOP_, if ((u) + 8 < 16) P3_LO((u) + 8)); } while (0)
#define DWU(u, slot) do { dwu(u, slot, 0); dwu(u, slot, 1); } while (0)
        P3_RW(0, 0); P3_RW(0, 1); P3_RW(0, 2); P3_RW(0, 3); P3_RW(1, 0); P3_RW(1, 1); P3_RW(1, 2); P3_RW(1, 3); SBAR();
        P3_UNIT(0, NOP_);
        P3_UNIT(1, PEEK);
        lds_await(seen, cntp, 68);
        SIGNAL();
        P3_UNIT(2, PEEK);
        lds_await(seen, cntp, 72); DWU(9, wo_rs(0)); DWU(10, wo_rs(1)); DWU(11, wo_rs(2));
        P3_UNIT(3, NOP_);
        P3_UNIT(4, NOP_);
        SIGNAL();
        P3_UNIT(5, PEEK);
        lds_await(seen, cntp, 76); DWU(12, wo_hs(0)); DWU(13, wo_hs(1)); DWU(14, wo_hs(2));
        P3_UNIT(6, NOP_);
        WAITV(14); SIGNAL();
        P3_UNIT(7, PEEK);
        lds_await(seen, cntp, 80);
        SIGNAL();
        P3_UNIT(8, PEEK);
        lds_await(seen, cntp, 84); DWU(15, wo_hs(3));
        P3_UNIT(9, NOP_);
        WAITV(6); SIGNAL();
        P3_UNIT(10, PEEK);
        lds_await(seen, cntp, 88);
        P3_UNIT(11, NOP_);
        P3_UNIT(12, NOP_);
        WAITV(0); SIGNAL();
        P3_UNIT(13, PEEK);
        lds_await(seen, cntp, 92);
        P3_UNIT(14, NOP_);
        P3_UNIT(15, NOP_);
        STAMP_MID(6)
        LBAR();
        if (tid == 0 && (xcd & 3) == 0 && j == 0) st_agent(flags + b * 32, tag);
        LAS char* mine = lds + L_S0 + wid * 8192 + lane * 16;
        const LAS char* theirs = lds + L_S0 + (wid ^ 4) * 8192 + lane * 16;
#pragma unroll
        for (int n2 = 0; n2 < 2; ++n2)
#pragma unroll
            for (int qd = 0; qd < 4; ++qd)
                *(LAS f32x4*)(mine + n2 * 4096 + qd * 1024) = (f32x4){acc[2 + n2][4 * qd], acc[2 + n2][4 * qd + 1], acc[2 + n2][4 * qd + 2], acc[2 + n2][4 * qd + 3]};
        LBAR();
        const int token = tm * 128 + wt * 32 + r32;
        const srd_t sout = make_srd(out, 2048 * 2048 * 2);
#pragma unroll
        for (int n2 = 0; n2 < 2; ++n2) {
            f32x16 v = acc[n2];
#pragma unroll
            for (int qd = 0; qd < 4; ++qd) {
                const f32x4 t = *(const LAS f32x4*)(theirs + n2 * 4096 + qd * 1024);
                v[4 * qd] += t[0]; v[4 * qd + 1] += t[1]; v[4 * qd + 2] += t[2]; v[4 * qd + 3] += t[3];
            }
            const unsigned oo = (unsigned)token * 4096u + (unsigned)(b * 1024 + tn * 128 + (2 * kh + n2) * 32) * 2u + hh * 16;
#pragma unroll
            for (int gp = 0; gp < 2; ++gp) {
                const unsigned a0 = cvtpk(v[8 * gp], v[8 * gp + 1]), a1 = cvtpk(v[8 * gp + 2], v[8 * gp + 3]);
                const unsigned b0 = cvtpk(v[8 * gp + 4], v[8 * gp + 5]), b1 = cvtpk(v[8 * gp + 6], v[8 * gp + 7]);
                auto r0 = __builtin_amdgcn_permlane32_swap(a0, b0, false, false);
                auto r1 = __builtin_amdgcn_permlane32_swap(a1, b1, false, false);
                const u32x4 w = {r0[0], r1[0], r0[1], r1[1]};
                __builtin_amdgcn_raw_buffer_store_b128(w, sout, oo + gp * 32, 0, OUTAUX);
            }
        }
    }
    STAMP_END(ws)
}

extern "C" void kernel_launch(void* const* d_in, const int* in_sizes, int n_in, void* d_out, int out_size, void* d_ws, size_t ws_size, hipStream_t stream) {
    (void)hipFuncSetAttribute((const void*)mha_fused, hipFuncAttributeMaxDynamicSharedMemorySize, L_END);
    mha_fused<<<256, 512, L_END, stream>>>((const uint16_t*)d_in[0], (const uint16_t*)d_in[1], (const uint16_t*)d_in[2], (const uint16_t*)d_in[3], (const uint16_t*)d_in[4],
                                           (const uint16_t*)d_in[5], (char*)d_ws, (uint16_t*)d_out);
}
```

```cpp
#define PFD 4
#define PFPRO 1
#define P1FINE
#include <hip/hip_runtime.h>
#include <stdint.h>

typedef __bf16 bf16x8 __attribute__((ext_vector_type(8)));
typedef __bf16 bf16x16 __attribute__((ext_vector_type(16)));
typedef __bf16 bf16x2 __attribute__((ext_vector_type(2)));
typedef short s16x4 __attribute__((ext_vector_type(4)));
typedef short s16x8 __attribute__((ext_vector_type(8)));
typedef float f32x16 __attribute__((ext_vector_type(16)));
typedef float f32x4 __attribute__((ext_vector_type(4)));
typedef float f32x2 __attribute__((ext_vector_type(2)));
typedef unsigned u32x4 __attribute__((ext_vector_type(4)));
typedef unsigned u32x8 __attribute__((ext_vector_type(8)));
typedef unsigned long long u64;
#define LAS __attribute__((address_space(3)))
#define GAS __attribute__((address_space(1)))
#define DI __device__ __forceinline__
#define MFMA32(a, b, c) __builtin_amdgcn_mfma_f32_32x32x16_bf16((a), (b), (c), 0, 0, 0)
#define SMF(a, b, c, idx) __builtin_amdgcn_smfmac_f32_16x16x64_bf16((a), __builtin_bit_cast(bf16x16, b), (c), (idx), 0, 0)
#define PLO(P) __builtin_bit_cast(bf16x8, __builtin_shufflevector(P, P, 0, 1, 2, 3))
#define PHI(P) __builtin_bit_cast(bf16x8, __builtin_shufflevector(P, P, 4, 5, 6, 7))
#define SBAR() __builtin_amdgcn_sched_barrier(0)
#define BF(x) __builtin_bit_cast(bf16x8, x)
#define NOP_ (void)0
typedef __amdgpu_buffer_rsrc_t srd_t;

#define OUTAUX 0
#define MAPSEL 1
constexpr float QSCALE = 0.17677669529663687f * 1.4426950408889634f;
constexpr unsigned WS_KV = 0;
constexpr unsigned WS_O = 16u << 20;
constexpr unsigned WS_FL = 24u << 20;
constexpr unsigned FL_KV = 4096;
constexpr unsigned FL_O = FL_KV + 16384;
constexpr int L_S0 = 32768, L_S1 = 65536, L_S2 = 0, L_R0 = 98304, L_R1 = 131072, L_END = 163840, L_CNT = L_END - 64;
constexpr int P1_XS = 32768, P1_WS = 24576, P1_WOFF = 98304;

DI unsigned rfl(unsigned x) { return __builtin_amdgcn_readfirstlane(x); }
DI srd_t make_srd(const void* p, unsigned bytes) { return __builtin_amdgcn_make_buffer_rsrc((void*)p, (short)0, (int)bytes, 0x00020000); }
DI void dma16(unsigned ldsdst, unsigned voff, srd_t srd, unsigned soff) {
    asm volatile("s_mov_b32 m0, %0\n\ts_nop 0\n\tbuffer_load_dwordx4 %1, %2, %3 offen lds" :: "s"(ldsdst), "v"(voff), "s"(srd), "s"(soff) : "memory");
}
DI void dma16c(unsigned ldsdst, unsigned voff, srd_t srd, unsigned soff) {
    asm volatile("s_mov_b32 m0, %0\n\ts_nop 0\n\tbuffer_load_dwordx4 %1, %2, %3 offen sc1 lds" :: "s"(ldsdst), "v"(voff), "s"(srd), "s"(soff) : "memory");
}
template <int I> DI void dma4c(unsigned ldsdst, unsigned voff, srd_t srd, unsigned soff) {
    if constexpr (I == 0) asm volatile("s_mov_b32 m0, %0\n\ts_nop 0\n\tbuffer_load_dwordx4 %1, %2, %3 offen sc1 lds" :: "s"(ldsdst), "v"(voff), "s"(srd), "s"(soff) : "memory");
    else asm volatile("buffer_load_dwordx4 %0, %1, %2 offen offset:%3 sc1 lds" :: "v"(voff), "s"(srd), "s"(soff), "i"(I * 1024) : "memory");
}
DI unsigned cvtpk(float lo, float hi) { f32x2 v = {lo, hi}; bf16x2 b = __builtin_convertvector(v, bf16x2); return __builtin_bit_cast(unsigned, b); }
DI bf16x8 ldsfrag(const LAS char* p) { return *(const LAS bf16x8*)p; }
typedef short v4i16_t __attribute__((ext_vector_type(4)));
DI s16x4 ldstr(const LAS char* p) { return __builtin_amdgcn_ds_read_tr16_b64_v4i16((LAS v4i16_t*)p); }
DI bf16x8 cat8(s16x4 lo, s16x4 hi) { s16x8 r = __builtin_shufflevector(lo, hi, 0, 1, 2, 3, 4, 5, 6, 7); return __builtin_bit_cast(bf16x8, r); }
DI u32x4 pack8(const f32x16& x, int s, float mul) {
    u32x4 p; p[0] = cvtpk(x[8 * s] * mul, x[8 * s + 1] * mul); p[1] = cvtpk(x[8 * s + 2] * mul, x[8 * s + 3] * mul);
    p[2] = cvtpk(x[8 * s + 4] * mul, x[8 * s + 5] * mul); p[3] = cvtpk(x[8 * s + 6] * mul, x[8 * s + 7] * mul); return p; }
DI u32x4 pack8n(const f32x16& x, int s) {
    u32x4 p; p[0] = cvtpk(x[8 * s], x[8 * s + 1]); p[1] = cvtpk(x[8 * s + 2], x[8 * s + 3]);
    p[2] = cvtpk(x[8 * s + 4], x[8 * s + 5]); p[3] = cvtpk(x[8 * s + 6], x[8 * s + 7]); return p; }
#define WAITBAR(vm) asm volatile("s_waitcnt vmcnt(" #vm ") lgkmcnt(0)\n\ts_barrier" ::: "memory")
#define LBAR() asm volatile("s_waitcnt lgkmcnt(0)\n\ts_barrier" ::: "memory")
#define WAITV(vm) asm volatile("s_waitcnt vmcnt(" #vm ")" ::: "memory")
DI float ex2(float x) { return __builtin_amdgcn_exp2f(x); }
DI u64 mix64(u64 z) { z += 0x9E3779B97F4A7C15ull; z = (z ^ (z >> 30)) * 0xBF58476D1CE4E5B9ull; z = (z ^ (z >> 27)) * 0x94D049BB133111EBull; return z ^ (z >> 31); }
DI u64 ld_agent(const GAS u64* p) { return __hip_atomic_load(p, __ATOMIC_RELAXED, __HIP_MEMORY_SCOPE_AGENT); }
DI void st_agent(GAS u64* p, u64 v) { __hip_atomic_store(p, v, __ATOMIC_RELAXED, __HIP_MEMORY_SCOPE_AGENT); }
DI void spin_flags(const GAS u64* f, u64 tag) {
    const u64 t0 = __builtin_amdgcn_s_memrealtime();
    u64 v0 = ld_agent(f);
    for (;;) {
        __builtin_amdgcn_s_sleep(6);
        const u64 v1 = ld_agent(f);
        if (__all(v0 == tag)) break;
        __builtin_amdgcn_s_sleep(6);
        v0 = ld_agent(f);
        if (__all(v1 == tag)) break;
        if (__builtin_amdgcn_s_memrealtime() - t0 > 20000000ull) break;
    }
    asm volatile("" ::: "memory");
}
DI void lds_signal(unsigned addr) {
    unsigned one = 1;
    asm volatile("s_waitcnt lgkmcnt(0)\n\ts_mov_b64 exec, 1\n\tds_add_u32 %0, %1\n\ts_mov_b64 exec, -1" :: "v"(addr), "v"(one) : "memory");
}
DI unsigned lds_peek(const LAS char* p) { return *(const volatile LAS unsigned*)p; }
DI void lds_await(unsigned seen, const LAS char* p, unsigned need) {
    if (rfl(seen) < need) {
        _Pragma("nounroll") for (int it = 0; it < (1 << 22); ++it) { if (rfl(lds_peek(p)) >= need) break; __builtin_amdgcn_s_sleep(0); }
    }
    asm volatile("" ::: "memory");
}

#define STAMP_BEGIN
#define STAMP_MID(i)
#define STAMP_END(ws)

__global__ __launch_bounds__(512, 2) void mha_fused(const uint16_t* xA, const uint16_t* wA, const uint16_t* woA, const uint16_t* xB, const uint16_t* wB, const uint16_t* woB,
                                                    char* ws, uint16_t* out) {
    extern __shared__ __attribute__((aligned(16))) char lds_[];
    LAS char* lds = (LAS char*)lds_;
    STAMP_BEGIN
    const int tid = threadIdx.x, lane = tid & 63, wid = rfl(tid >> 6);
    const int bid = blockIdx.x, xcd = bid & 7, j = bid >> 3;
    const int b = xcd >> 2;
    const int mb = (xcd & 1) * 4 + (j & 3), hp = ((xcd >> 1) & 1) * 8 + (j >> 2);
    const int wm = wid & 3, wn = wid >> 2, h = hp * 2 + wn;
    const unsigned lds0 = (unsigned)(uintptr_t)lds_;
    const int r32 = lane & 31, hh = lane >> 5, g4 = lane >> 4, q4 = (lane >> 2) & 3, p4 = lane & 3;
    GAS u64* const flags = (GAS u64*)(ws + WS_FL);
    u64 epoch = ld_agent(flags + b * 32);
    if (tid < 16) *(LAS unsigned*)(lds + L_CNT + tid * 4) = 0u;

    bf16x8 qf[2][2];
    u32x4 kvp[2][4];
    const srd_t skvw = make_srd(ws + WS_KV + (size_t)(b * 32 + h) * 64 * 4096 + (size_t)(mb * 8 + wm * 2) * 4096, 2 * 4096);
    {
        const srd_t sx = make_srd(b ? xB : xA, 2048 * 1024 * 2), sw = make_srd(b ? wB : wA, 1024 * 3072 * 2);
        const unsigned vx0 = (lane >> 3) * 2048 + (((lane & 7) ^ (lane >> 4)) << 4);
        const unsigned vx1 = (lane >> 3) * 2048 + (((lane & 7) ^ (4 + (lane >> 4))) << 4);
        const unsigned vw = (lane >> 3) * 6144 + (((lane & 7) ^ (((lane >> 4) & 1) << 2) ^ ((wid & 1) << 1)) << 4);
        const unsigned sx0 = (mb * 256 + wid * 32) * 2048, sw0 = wid * 8 * 6144 + hp * 128;
        auto dx = [&](int kt, int i) { dma16(lds0 + (kt % 3) * P1_XS + wid * 4096 + i * 1024, (i & 1) ? vx1 : vx0, sx, sx0 + i * 8 * 2048 + kt * 128); };
        auto dw = [&](int kt, int i) { dma16(lds0 + P1_WOFF + (kt & 1) * P1_WS + wid * 1024 + i * 8192, vw, sw, sw0 + i * 2048 + kt * 64 * 6144); };
        for (int i = 0; i < 3; ++i) dw(0, i);
        for (int i = 0; i < 4; ++i) dx(0, i);
        for (int i = 0; i < 3; ++i) dw(1, i);
        for (int i = 0; i < 4; ++i) dx(1, i);
        for (int i = 0; i < 4; ++i) dx(2, i);
        const char* pfp; unsigned pfstep;
        {
            const int l = lane < 10 ? lane : 9;
            const int xrow = (xcd & 1) * 1024 + j * 32 + wid * 4 + (l & 3), wl = wid * 6 + (l - 4);
            const size_t woff = (size_t)(j * 2 + wl / 24) * 6144 + ((wl % 24) >> 3) * 2048 + (((xcd >> 1) & 1) * 8 + (wl & 7)) * 128;
            pfp = l < 4 ? (const char*)(b ? xB : xA) + (size_t)xrow * 2048 : (const char*)(b ? wB : wA) + woff;
            pfstep = l < 4 ? 128u : 64u * 6144u;
        }
        const unsigned pfdst = lds0 + 147456 + wid * 1024;
        auto pf = [&](int kt) {
            const char* g = pfp + (size_t)kt * pfstep;
            asm volatile("s_mov_b32 m0, %0\n\ts_nop 0\n\tglobal_load_lds_dword %1, off" :: "s"(pfdst), "v"(g) : "memory"); };
        if (PFD) for (int i = 0; i < PFPRO; ++i) pf(3 + i);
#define NPRO PFPRO
        const int r16 = lane & 15;
        unsigned xa[2], wb[2];
#pragma unroll
        for (int s = 0; s < 2; ++s) xa[s] = (wm * 64 + r16) * 128 + (((4 * s + g4) ^ (r16 >> 1)) << 4);
#pragma unroll
        for (int c = 0; c < 2; ++c) wb[c] = P1_WOFF + (8 * g4 + q4) * 128 + ((wn ^ (q4 >> 1)) << 6) + ((c ^ (g4 & 1)) << 5) + p4 * 8;
        f32x4 acc[4][6];
#pragma unroll
        for (int a = 0; a < 4; ++a)
#pragma unroll
            for (int c = 0; c < 6; ++c) acc[a][c] = (f32x4){0.f, 0.f, 0.f, 0.f};
        bf16x8 xf[2][4][2], wf[3][2];
#define MFMA16(a, b, c) __builtin_amdgcn_mfma_f32_16x16x32_bf16((a), (b), (c), 0, 0, 0)
#define P1_RX(kt, tb, s) xf[(kt) & 1][tb][s] = ldsfrag(lds + ((kt) % 3) * P1_XS + xa[s] + (tb) * 2048)
#define P1_RW(kt, fb, s) do { const LAS char* wp_ = lds + wb[(fb) & 1] + ((kt) & 1) * P1_WS + ((fb) >> 1) * 8192 + (s) * 4096; wf[(fb) % 3][s] = cat8(ldstr(wp_), ldstr(wp_ + 512)); } while (0)
#define P1_MM(kt, tb, fb) do { if ((fb) < 4) { acc[tb][fb] = MFMA16(wf[(fb) % 3][0], xf[(kt) & 1][tb][0], acc[tb][fb]); acc[tb][fb] = MFMA16(wf[(fb) % 3][1], xf[(kt) & 1][tb][1], acc[tb][fb]); } \
                               else { acc[tb][fb] = MFMA16(xf[(kt) & 1][tb][0], wf[(fb) % 3][0], acc[tb][fb]); acc[tb][fb] = MFMA16(xf[(kt) & 1][tb][1], wf[(fb) % 3][1], acc[tb][fb]); } } while (0)
        s16x4 wlo[3][2], whi[3][2];
#define P1_WL(kt, fb, s) wlo[(fb) % 3][s] = ldstr(lds + wb[(fb) & 1] + ((kt) & 1) * P1_WS + ((fb) >> 1) * 8192 + (s) * 4096)
#define P1_WH(kt, fb, s) whi[(fb) % 3][s] = ldstr(lds + wb[(fb) & 1] + ((kt) & 1) * P1_WS + ((fb) >> 1) * 8192 + (s) * 4096 + 512)
#define P1_M1(kt, tb, fb, s) do { const bf16x8 w_ = cat8(wlo[(fb) % 3][s], whi[(fb) % 3][s]); if ((fb) < 4) acc[tb][fb] = MFMA16(w_, xf[(kt) & 1][tb][s], acc[tb][fb]); else acc[tb][fb] = MFMA16(xf[(kt) & 1][tb][s], w_, acc[tb][fb]); } while (0)
#define P1_FB8(kt, fb, R0, R1, R2, R3, R4, R5, R6, R7) do { \
            P1_M1(kt, 0, fb, 0); R0; SBAR(); P1_M1(kt, 1, fb, 0); R1; SBAR(); P1_M1(kt, 2, fb, 0); R2; SBAR(); P1_M1(kt, 3, fb, 0); R3; SBAR(); \
            P1_M1(kt, 0, fb, 1); R4; SBAR(); P1_M1(kt, 1, fb, 1); R5; SBAR(); P1_M1(kt, 2, fb, 1); R6; SBAR(); P1_M1(kt, 3, fb, 1); R7; SBAR(); } while (0)
#define P1_FB(kt, fb, R0, R1, R2, R3) do { \
            P1_MM(kt, 0, fb); R0; SBAR(); P1_MM(kt, 1, fb); R1; SBAR(); P1_MM(kt, 2, fb); R2; SBAR(); P1_MM(kt, 3, fb); R3; SBAR(); } while (0)
        asm volatile("s_waitcnt vmcnt(%0) lgkmcnt(0)\n\ts_barrier" :: "n"(11 + NPRO) : "memory");
        STAMP_MID(0)
        P1_RX(0, 0, 0); P1_WL(0, 0, 0); P1_WH(0, 0, 0); P1_RX(0, 1, 0); P1_RX(0, 2, 0); P1_RX(0, 3, 0); P1_WL(0, 0, 1); P1_WH(0, 0, 1);
        P1_RX(0, 0, 1); P1_RX(0, 1, 1); P1_RX(0, 2, 1); P1_RX(0, 3, 1); P1_WL(0, 1, 0); P1_WH(0, 1, 0); P1_WL(0, 1, 1); P1_WH(0, 1, 1); SBAR();
#pragma unroll
        for (int t = 0; t < 16; ++t) {
            const bool xs = (t >= 1 && t + 2 < 16);
            P1_FB8(t, 0, P1_WL(t, 2, 0), NOP_, P1_WH(t, 2, 0), NOP_, P1_WL(t, 2, 1), if (xs) dx(t + 2, 0), P1_WH(t, 2, 1), NOP_);
            P1_FB8(t, 1, P1_WL(t, 3, 0), NOP_, P1_WH(t, 3, 0), NOP_, P1_WL(t, 3, 1), if (xs) dx(t + 2, 1), P1_WH(t, 3, 1), NOP_);
            P1_FB8(t, 2, P1_WL(t, 4, 0), NOP_, P1_WH(t, 4, 0), NOP_, P1_WL(t, 4, 1), if (xs) dx(t + 2, 2), P1_WH(t, 4, 1), NOP_);
            P1_FB8(t, 3, P1_WL(t, 5, 0), NOP_, P1_WH(t, 5, 0), NOP_, P1_WL(t, 5, 1), if (xs) dx(t + 2, 3), P1_WH(t, 5, 1), NOP_);
            if (t < 15) {
                { const int npf = t == 0 ? NPRO : (PFD && t - 1 + PFD < 16 ? 1 : 0);
                  const int nw = (t < 14 ? 4 : 0) + (t < 14 ? npf : 0);
                  if (nw == 0) WAITBAR(0); else if (nw == 4) WAITBAR(4); else if (nw == 5) WAITBAR(5); else asm volatile("s_waitcnt vmcnt(%0) lgkmcnt(0)\n\ts_barrier" :: "n"(4 + NPRO) : "memory"); }
                const bool wsch = (t + 2 < 16);
                P1_FB8(t, 4, P1_RX(t + 1, 0, 0), P1_WL(t + 1, 0, 0), P1_WH(t + 1, 0, 0), P1_RX(t + 1, 1, 0), P1_RX(t + 1, 2, 0), P1_RX(t + 1, 3, 0), P1_WL(t + 1, 0, 1), { P1_WH(t + 1, 0, 1); if (wsch) dw(t + 2, 0); });
                P1_FB8(t, 5, P1_RX(t + 1, 0, 1), P1_RX(t + 1, 1, 1), P1_RX(t + 1, 2, 1), { P1_RX(t + 1, 3, 1); if (wsch) dw(t + 2, 1); }, P1_WL(t + 1, 1, 0), { P1_WH(t + 1, 1, 0); if (wsch) dw(t + 2, 2); }, P1_WL(t + 1, 1, 1), { P1_WH(t + 1, 1, 1); if (PFD && t + PFD < 16) pf(t + PFD); });
            } else {
                P1_FB8(t, 4, NOP_, NOP_, NOP_, NOP_, NOP_, NOP_, NOP_, NOP_);
                P1_FB8(t, 5, NOP_, NOP_, NOP_, NOP_, NOP_, NOP_, NOP_, NOP_);
            }
        }
        STAMP_MID(1)
        auto swap16 = [](unsigned& x, unsigned& y) { auto r = __builtin_amdgcn_permlane16_swap(x, y, false, false); x = r[0]; y = r[1]; };
        auto swap32 = [](unsigned& x, unsigned& y) { auto r = __builtin_amdgcn_permlane32_swap(x, y, false, false); x = r[0]; y = r[1]; };
        auto qkfrag = [&](const f32x4& t0, const f32x4& t1, float mul) {
            unsigned a0 = cvtpk(t0[0] * mul, t0[1] * mul), a1 = cvtpk(t0[2] * mul, t0[3] * mul), b0 = cvtpk(t1[0] * mul, t1[1] * mul), b1 = cvtpk(t1[2] * mul, t1[3] * mul);
            swap16(a0, b0); swap16(a1, b1);
            return (u32x4){a0, a1, b0, b1}; };
        auto vfrag = [&](const f32x4& x0, const f32x4& x1) {
            unsigned a0 = cvtpk(x0[0], x0[1]), a1 = cvtpk(x0[2], x0[3]), b0 = cvtpk(x1[0], x1[1]), b1 = cvtpk(x1[2], x1[3]);
            swap16(a0, b0); swap16(a1, b1); swap32(a0, b0); swap32(a1, b1);
            return (u32x4){a0, a1, b0, b1}; };
#pragma unroll
        for (int a = 0; a < 2; ++a)
#pragma unroll
            for (int s = 0; s < 2; ++s) {
                kvp[a][s] = qkfrag(acc[2 * a][2 + s], acc[2 * a + 1][2 + s], 1.0f); kvp[a][2 + s] = vfrag(acc[2 * a + s][4], acc[2 * a + s][5]);
                __builtin_amdgcn_raw_buffer_store_b128(kvp[a][s], skvw, lane * 16 + a * 4096 + s * 1024, 0, 16);
                __builtin_amdgcn_raw_buffer_store_b128(kvp[a][2 + s], skvw, lane * 16 + a * 4096 + 2048 + s * 1024, 0, 16);
            }
#define P1_QF(a, s) BF(qkfrag(acc[2 * (a)][s], acc[2 * (a) + 1][s], QSCALE))
#pragma unroll
        for (int a = 0; a < 2; ++a) {
            const int tl = wm * 2 + a;
            LAS char* lp = lds + L_S0 + (tl >> 2) * 32768 + wn * 16384 + (tl & 3) * 4096 + lane * 16;
#pragma unroll
            for (int f = 0; f < 4; ++f) *(LAS u32x4*)(lp + f * 1024) = kvp[a][f];
        }
        SBAR();
#pragma unroll
        for (int a = 0; a < 2; ++a)
#pragma unroll
            for (int s = 0; s < 2; ++s) qf[a][s] = P1_QF(a, s);
    }
    asm volatile("" : "+v"(epoch));
    const u64 tag = mix64(((u64)rfl((unsigned)(epoch >> 32)) << 32) | rfl((unsigned)epoch));
    STAMP_MID(2)

    const srd_t skv = make_srd(ws + WS_KV + (size_t)(b * 32 + hp * 2) * 64 * 4096, 2 * 64 * 4096);
    const srd_t swo = make_srd(b ? woB : woA, 1024 * 1024 * 2);
    const int tm = (xcd & 3) * 4 + (j >> 3), tn = j & 7;
    const unsigned vl = lane * 16;
    const unsigned vwo = (lane >> 4) * 2048 + (((lane & 15) ^ (((lane >> 4) & 3) << 2)) << 4);
    auto chunk_off = [&](int c) { const int i = (c - 2) >> 1; const int blk = c < 2 ? mb : (i < mb ? i : i + 1); return (unsigned)((wn * 64 + blk * 8 + (c & 1) * 4 + wm) * 4096); };
    const unsigned ldw = lds0 + wid * 4096;
#define DKV(coff, slotoff, i) dma4c<i>(ldw + (slotoff), vl, skv, coff)
    const unsigned wo0 = (wn * 32 + wm * 8) * 2048 + tn * 256;
    auto dwu = [&](int u, unsigned slotaddr, int p) { dma16(lds0 + slotaddr + wm * 2048 + p * 1024, vwo, swo, wo0 + (u * 64 + p * 4) * 2048); };
    auto wo_rs = [&](int k) { return (unsigned)(L_R0 + wn * 24576 + k * 8192); };
    auto wo_hs = [&](int k) { return (unsigned)((k >> 1 == 0 ? L_S0 : k >> 1 == 1 ? L_S1 : L_S2) + wn * 16384 + (k & 1) * 8192); };
    bf16x8 sel; int selidx;
    { const int i16 = lane & 15; const short lo = (i16 < 2) ? (short)0x3F80 : (short)0, hi = (i16 == 2 || i16 == 3) ? (short)0x3F80 : (short)0;
      s16x8 t = {lo, lo, lo, lo, hi, hi, hi, hi}; sel = __builtin_bit_cast(bf16x8, t); selidx = (i16 & 1) ? 0xEEEE : 0x4444; }
    const LAS char* kvb = lds + wn * 16384 + lane * 16;
    GAS u64* const kvfl = flags + (FL_KV / 8) + (b * 32 + h) * 32;
    const GAS u64* const kvmine = kvfl + ((lane >> 1) & 7) * 4 + (lane & 1) * 2 + (wm >> 1);
    const LAS char* cntp = lds + L_CNT + wn * 16;
    const unsigned cnta = lds0 + L_CNT + wn * 16;
    f32x16 o[2]; f32x4 lacc[2];
    {
        f32x16 negm[2], S[2];
        bf16x8 kf[2][2], vf[2][2];
        u32x8 P;
        const f32x16 zero16 = {0.f,0.f,0.f,0.f,0.f,0.f,0.f,0.f,0.f,0.f,0.f,0.f,0.f,0.f,0.f,0.f};
        kf[0][0] = BF(kvp[0][0]); kf[0][1] = BF(kvp[0][1]); vf[0][0] = BF(kvp[0][2]); vf[0][1] = BF(kvp[0][3]);
#pragma unroll
        for (int a = 0; a < 2; ++a) {
            f32x16 z;
#pragma unroll
            for (int i = 0; i < 16; ++i) z[i] = 0.f;
            z = MFMA32(kf[0][0], qf[a][0], z); z = MFMA32(kf[0][1], qf[a][1], z);
            float m = z[0];
#pragma unroll
            for (int i = 1; i < 16; ++i) m = fmaxf(m, z[i]);
            m = fmaxf(m, __shfl_xor(m, 32));
#pragma unroll
            for (int i = 0; i < 16; ++i) { negm[a][i] = -m; o[a][i] = 0.f; }
            lacc[a] = (f32x4){0.f, 0.f, 0.f, 0.f};
        }
        S[0] = MFMA32(kf[0][0], qf[0][0], negm[0]); S[0] = MFMA32(kf[0][1], qf[0][1], S[0]);
        P = (u32x8){0u, 0u, 0u, 0u, 0u, 0u, 0u, 0u};
        bf16x8 vprev1 = vf[0][1];
        unsigned seen;
#define EXPI(X, i) X[i] = ex2(X[i])
#define PVM(O, V, PP) O = MFMA32(V, PP, O)
#define PKA(L, X, i) NOP_
#define NEGSEL(N) N
#define UNIT(SX, SN, KA0, KA1, QB0, QB1, NEG, DOQK, OPREV, LPREV, VPREV1, OCUR, LCUR, VCUR0, H0, H1, H2, H3) do { \
            if (DOQK) SN = MFMA32(KA0, QB0, NEGSEL(NEG)); H0; SBAR(); \
            EXPI(SX, 0); EXPI(SX, 1); EXPI(SX, 2); SBAR(); \
            LPREV = SMF(sel, P, LPREV, selidx); SBAR(); \
            EXPI(SX, 3); P[0] = cvtpk(SX[0], SX[1]); P[1] = cvtpk(SX[2], SX[3]); PKA(LCUR, SX, 0); PKA(LCUR, SX, 2); SBAR(); \
            PVM(OPREV, VPREV1, PHI(P)); H1; SBAR(); \
            EXPI(SX, 4); EXPI(SX, 5); EXPI(SX, 6); EXPI(SX, 7); P[2] = cvtpk(SX[4], SX[5]); P[3] = cvtpk(SX[6], SX[7]); PKA(LCUR, SX, 4); PKA(LCUR, SX, 6); SBAR(); \
            if (DOQK) SN = MFMA32(KA1, QB1, SN); H2; SBAR(); \
            EXPI(SX, 8); EXPI(SX, 9); EXPI(SX, 10); EXPI(SX, 11); P[4] = cvtpk(SX[8], SX[9]); P[5] = cvtpk(SX[10], SX[11]); PKA(LCUR, SX, 8); PKA(LCUR, SX, 10); SBAR(); \
            PVM(OCUR, VCUR0, PLO(P)); H3; SBAR(); \
            EXPI(SX, 12); EXPI(SX, 13); EXPI(SX, 14); EXPI(SX, 15); P[6] = cvtpk(SX[12], SX[13]); P[7] = cvtpk(SX[14], SX[15]); PKA(LCUR, SX, 12); PKA(LCUR, SX, 14); SBAR(); \
        } while (0)
#define TILEG(cur, nxt, LK, LV, LOAD, A2, A3, B0, B1, B2, B3) do { \
            UNIT(S[0], S[1], kf[cur][0], kf[cur][1], qf[1][0], qf[1][1], negm[1], true, o[1], lacc[1], vprev1, o[0], lacc[0], vf[cur][0], LK, LV, A2, A3); \
            UNIT(S[1], S[0], kf[nxt][0], kf[nxt][1], qf[0][0], qf[0][1], negm[0], LOAD, o[0], lacc[0], vf[cur][1], o[1], lacc[1], vf[cur][0], B0, B1, B2, B3); \
            vprev1 = vf[cur][1]; \
        } while (0)
#define TILEX(cur, nxt, rdp, LOAD, A2, A3, B0, B1, B2, B3) TILEG(cur, nxt, \
            if (LOAD) { kf[nxt][0] = ldsfrag(rdp); kf[nxt][1] = ldsfrag((rdp) + 1024); }, if (LOAD) { vf[nxt][0] = ldsfrag((rdp) + 2048); vf[nxt][1] = ldsfrag((rdp) + 3072); }, \
            LOAD, A2, A3, B0, B1, B2, B3)
#define TILE(cur, nxt, rdp, PK) TILEX(cur, nxt, rdp, true, NOP_, NOP_, NOP_, PK, NOP_, NOP_)
#define TILED(cur, nxt, rdp, coff, slot) TILEX(cur, nxt, rdp, true, DKV(coff, slot, 0), DKV(coff, slot, 1), NOP_, NOP_, DKV(coff, slot, 2), DKV(coff, slot, 3))
#define PEEK seen = lds_peek(cntp)
#define SIGNAL() lds_signal(cnta)
#define FLIP 56
#define BALANCE(m) do { if (wn == 0 && (m) == FLIP) __builtin_amdgcn_s_setprio(0); } while (0)
        if (wn == 0) __builtin_amdgcn_s_setprio(2); else __builtin_amdgcn_s_setprio(1);
        auto own = [&](int p) {
            const int lo = wm < 2;
            const int off = lo ? (p < 4 ? L_S0 + ((1 - wm) * 2 + p - 2) * 4096 : L_S1 + (p - 4) * 4096) : (p < 6 ? L_S0 + (p - 2) * 4096 : L_S1 + ((3 - wm) * 2 + p - 6) * 4096);
            return kvb + off; };
        TILEG(0, 1, { kf[1][0] = BF(kvp[1][0]); kf[1][1] = BF(kvp[1][1]); }, { vf[1][0] = BF(kvp[1][2]); vf[1][1] = BF(kvp[1][3]); }, true, NOP_, NOP_, NOP_, NOP_, NOP_, NOP_);
        LBAR();
        TILE(1, 0, own(2), NOP_);
        TILE(0, 1, own(3), NOP_);
        STAMP_MID(8)
        WAITV(0); if (lane == 0) st_agent(kvfl + mb * 4 + wm, tag);
        STAMP_MID(9)
        TILE(1, 0, own(4), NOP_);
        u64 pollv = ld_agent(kvmine);
        TILE(0, 1, own(5), NOP_);
        STAMP_MID(10)
        if (!__all(pollv == tag)) spin_flags(kvmine, tag);
        STAMP_MID(11)
        SIGNAL();
        unsigned cn = chunk_off(2);
        TILEX(1, 0, own(6), true, DKV(cn, L_S2, 0), DKV(cn, L_S2, 1), NOP_, PEEK, DKV(cn, L_S2, 2), DKV(cn, L_S2, 3));
        lds_await(seen, cntp, 4);
        cn = chunk_off(3);
        TILED(0, 1, own(7), cn, L_S0);
        WAITV(4); SIGNAL(); lds_await(0, cntp, 8);
        TILE(1, 0, kvb + L_S2, NOP_);
        cn = chunk_off(4);
        TILED(0, 1, kvb + L_S2 + 4096, cn, L_S1);
        WAITV(4); SIGNAL();
        TILE(1, 0, kvb + L_S2 + 8192, NOP_);
        TILE(0, 1, kvb + L_S2 + 12288, PEEK);
        lds_await(seen, cntp, 12);
        WAITV(0); SIGNAL();
        TILE(1, 0, kvb + L_S0, PEEK);
        STAMP_MID(7)
        int sc = L_S0, sn = L_S1, sp = L_S2;
        for (int m = 3; m <= 13; ++m) {
            const LAS char* cp = kvb + sc;
            cn = chunk_off(m + 2);
            lds_await(seen, cntp, 4 * (m + 1));
            BALANCE(4 * m);
            TILED(0, 1, cp + 4096, cn, sp);
            TILE(1, 0, cp + 8192, NOP_);
            TILE(0, 1, cp + 12288, NOP_);
            WAITV(0); SIGNAL();
            TILE(1, 0, kvb + sn, PEEK);
            const int t_ = sc; sc = sn; sn = sp; sp = t_;
        }
        lds_await(seen, cntp, 60);
        BALANCE(56);
        TILEX(0, 1, kvb + L_S2 + 4096, true, dwu(0, wo_rs(0), 0), dwu(0, wo_rs(0), 1), NOP_, NOP_, NOP_, NOP_);
        BALANCE(57);
        TILEX(1, 0, kvb + L_S2 + 8192, true, dwu(1, wo_rs(1), 0), dwu(1, wo_rs(1), 1), NOP_, NOP_, NOP_, NOP_);
        BALANCE(58);
        TILEX(0, 1, kvb + L_S2 + 12288, true, dwu(2, wo_rs(2), 0), dwu(2, wo_rs(2), 1), NOP_, NOP_, NOP_, NOP_);
        BALANCE(59);
        TILE(1, 0, kvb + L_S0, NOP_);
        BALANCE(60);
        TILE(0, 1, kvb + L_S0 + 4096, NOP_);
        BALANCE(61);
        TILE(1, 0, kvb + L_S0 + 8192, NOP_);
        BALANCE(62);
        TILE(0, 1, kvb + L_S0 + 12288, NOP_);
        TILEX(1, 0, kvb, false, NOP_, NOP_, NOP_, NOP_, NOP_, NOP_);
        lacc[1] = SMF(sel, P, lacc[1], selidx); o[1] = MFMA32(vprev1, PHI(P), o[1]);
    }
#define P3PRIO 0
    if (wn == 0) __builtin_amdgcn_s_setprio(P3PRIO);
    STAMP_MID(3)
    {
        float l[2];
        auto rowsum = [&](int a) { const float t0 = __shfl(lacc[a][0] + lacc[a][1], lane & 15), t1 = __shfl(lacc[a][2] + lacc[a][3], lane & 15); return (lane & 16) ? t1 : t0; };
        bool bad = false;
#pragma unroll
        for (int a = 0; a < 2; ++a) {
            l[a] = rowsum(a);
            float z = l[a] * 0.f, z1 = 0.f;
#pragma unroll
            for (int i = 0; i < 16; i += 2) { z = __builtin_fmaf(o[a][i], 0.f, z); z1 = __builtin_fmaf(o[a][i + 1], 0.f, z1); }
            bad |= !(l[a] < 1.0e18f) || !(z + z1 == 0.f);
        }
        if (__any(bad)) {
            spin_flags(kvfl + (lane & 31), tag);
            const srd_t sk1 = make_srd(ws + WS_KV + (size_t)(b * 32 + h) * 64 * 4096, 64 * 4096);
#define LDKV(t, f) BF(__builtin_amdgcn_raw_buffer_load_b128(sk1, lane * 16 + (f) * 1024, (t) * 4096, 16))
            f32x16 negm[2];
#pragma unroll
            for (int a = 0; a < 2; ++a) {
                float m = -__builtin_inff();
                for (int t = 0; t < 64; ++t) {
                    const bf16x8 k0 = LDKV(t, 0), k1 = LDKV(t, 1);
                    f32x16 z;
#pragma unroll
                    for (int i = 0; i < 16; ++i) z[i] = 0.f;
                    z = MFMA32(k0, qf[a][0], z); z = MFMA32(k1, qf[a][1], z);
#pragma unroll
                    for (int i = 0; i < 16; ++i) m = fmaxf(m, z[i]);
                }
                m = fmaxf(m, __shfl_xor(m, 32));
#pragma unroll
                for (int i = 0; i < 16; ++i) { negm[a][i] = -m; o[a][i] = 0.f; }
                lacc[a] = (f32x4){0.f, 0.f, 0.f, 0.f};
            }
            for (int t = 0; t < 64; ++t) {
                const bf16x8 k0 = LDKV(t, 0), k1 = LDKV(t, 1), v0 = LDKV(t, 2), v1 = LDKV(t, 3);
#pragma unroll
                for (int a = 0; a < 2; ++a) {
                    f32x16 z = MFMA32(k0, qf[a][0], negm[a]); z = MFMA32(k1, qf[a][1], z);
                    u32x8 P;
#pragma unroll
                    for (int i = 0; i < 8; ++i) P[i] = cvtpk(ex2(z[2 * i]), ex2(z[2 * i + 1]));
                    o[a] = MFMA32(v0, PLO(P), o[a]); o[a] = MFMA32(v1, PHI(P), o[a]); lacc[a] = SMF(sel, P, lacc[a], selidx);
                }
            }
            l[0] = rowsum(0); l[1] = rowsum(1);
        }
        const srd_t sow = make_srd(ws + WS_O + (size_t)(b * 64 + mb * 8 + wm * 2) * 65536, 2 * 65536);
#pragma unroll
        for (int a = 0; a < 2; ++a) {
            const float rl = __builtin_amdgcn_rcpf(l[a]);
            __builtin_amdgcn_raw_buffer_store_b128(pack8(o[a], 0, rl), sow, lane * 16 + a * 65536 + h * 2048, 0, 16);
            __builtin_amdgcn_raw_buffer_store_b128(pack8(o[a], 1, rl), sow, lane * 16 + a * 65536 + h * 2048 + 1024, 0, 16);
        }
    }
    GAS u64* const ofl = flags + (FL_O / 8) + b * 1024;
    WAITV(0); if (lane == 0) st_agent(ofl + (mb * 4 + wm) * 32 + h, tag);
    STAMP_MID(4)

    {
        const int wt = wm, kh = wn;
        unsigned seen;
        SIGNAL(); lds_await(0, cntp, 64);
        for (int i = 0; i < 6; ++i) { dwu(3 + i, wo_hs(i), 0); dwu(3 + i, wo_hs(i), 1); }
        spin_flags(ofl + (tm * 2 + (wt >> 1)) * 32 + (lane & 15) * 2 + kh, tag);
        STAMP_MID(5)
        const srd_t sor = make_srd(ws + WS_O + (size_t)(b * 64 + tm * 4 + wt) * 65536 + kh * 2048, 65536 - 2048);
        bf16x8 of[8][2], wf[3][4];
#define P3_LO(u) do { of[(u) & 7][0] = BF(__builtin_amdgcn_raw_buffer_load_b128(sor, lane * 16, (u) * 4096, 16)); \
                      of[(u) & 7][1] = BF(__builtin_amdgcn_raw_buffer_load_b128(sor, lane * 16, (u) * 4096 + 1024, 16)); } while (0)
        P3_LO(0); P3_LO(1); P3_LO(2); P3_LO(3); P3_LO(4); P3_LO(5); P3_LO(6); P3_LO(7);
        SIGNAL();
        unsigned wn3[4];
#pragma unroll
        for (int n = 0; n < 4; ++n) wn3[n] = (4 * (g4 >> 1) + q4) * 256 + ((n ^ (kh << 1) ^ q4) << 6) + (g4 & 1) * 32 + p4 * 8;
        f32x16 acc[4];
#pragma unroll
        for (int n = 0; n < 4; ++n)
#pragma unroll
            for (int i = 0; i < 16; ++i) acc[n][i] = 0.f;
        auto uslot = [&](int u) { return u < 3 ? wo_rs(u) : u < 9 ? wo_hs(u - 3) : u < 12 ? wo_rs(u - 9) : wo_hs(u - 12); };
#define P3_RW(kk, n) do { const LAS char* w_ = lds + uslot((kk) >> 1) + ((kk) & 1) * 4096 + wn3[n]; wf[(kk) % 3][n] = cat8(ldstr(w_), ldstr(w_ + 2048)); } while (0)
#define P3_KS(kk, M0, M1) do { \
            acc[0] = MFMA32(wf[(kk) % 3][0], of[((kk) >> 1) & 7][(kk) & 1], acc[0]); if ((kk) + 2 < 32) P3_RW((kk) + 2, 0); SBAR(); \
            acc[1] = MFMA32(wf[(kk) % 3][1], of[((kk) >> 1) & 7][(kk) & 1], acc[1]); if ((kk) + 2 < 32) P3_RW((kk) + 2, 1); M0; SBAR(); \
            acc[2] = MFMA32(wf[(kk) % 3][2], of[((kk) >> 1) & 7][(kk) & 1], acc[2]); if ((kk) + 2 < 32) P3_RW((kk) + 2, 2); SBAR(); \
            acc[3] = MFMA32(wf[(kk) % 3][3], of[((kk) >> 1) & 7][(kk) & 1], acc[3]); if ((kk) + 2 < 32) P3_RW((kk) + 2, 3); M1; SBAR(); \
        } while (0)
#define P3_UNIT(u, M0) do { P3_KS(2 * (u), M0, NOP_); P3_KS(2 * (u) + 1, NOP_, if ((u) + 8 < 16) P3_LO((u) + 8)); } while (0)
#define DWU(u, slot) do { dwu(u, slot, 0); dwu(u, slot, 1); } while (0)
        P3_RW(0, 0); P3_RW(0, 1); P3_RW(0, 2); P3_RW(0, 3); P3_RW(1, 0); P3_RW(1, 1); P3_RW(1, 2); P3_RW(1, 3); SBAR();
        P3_UNIT(0, NOP_);
        P3_UNIT(1, PEEK);
        lds_await(seen, cntp, 68);
        SIGNAL();
        P3_UNIT(2, PEEK);
        lds_await(seen, cntp, 72); DWU(9, wo_rs(0)); DWU(10, wo_rs(1)); DWU(11, wo_rs(2));
        P3_UNIT(3, NOP_);
        P3_UNIT(4, NOP_);
        SIGNAL();
        P3_UNIT(5, PEEK);
        lds_await(seen, cntp, 76); DWU(12, wo_hs(0)); DWU(13, wo_hs(1)); DWU(14, wo_hs(2));
        P3_UNIT(6, NOP_);
        WAITV(14); SIGNAL();
        P3_UNIT(7, PEEK);
        lds_await(seen, cntp, 80);
        SIGNAL();
        P3_UNIT(8, PEEK);
        lds_await(seen, cntp, 84); DWU(15, wo_hs(3));
        P3_UNIT(9, NOP_);
        WAITV(6); SIGNAL();
        P3_UNIT(10, PEEK);
        lds_await(seen, cntp, 88);
        P3_UNIT(11, NOP_);
        P3_UNIT(12, NOP_);
        WAITV(0); SIGNAL();
        P3_UNIT(13, PEEK);
        lds_await(seen, cntp, 92);
        P3_UNIT(14, NOP_);
        P3_UNIT(15, NOP_);
        STAMP_MID(6)
        LBAR();
        if (tid == 0 && (xcd & 3) == 0 && j == 0) st_agent(flags + b * 32, tag);
        LAS char* mine = lds + L_S0 + wid * 8192 + lane * 16;
        const LAS char* theirs = lds + L_S0 + (wid ^ 4) * 8192 + lane * 16;
#pragma unroll
        for (int n2 = 0; n2 < 2; ++n2)
#pragma unroll
            for (int qd = 0; qd < 4; ++qd)
                *(LAS f32x4*)(mine + n2 * 4096 + qd * 1024) = (f32x4){acc[2 + n2][4 * qd], acc[2 + n2][4 * qd + 1], acc[2 + n2][4 * qd + 2], acc[2 + n2][4 * qd + 3]};
        LBAR();
        const int token = tm * 128 + wt * 32 + r32;
        const srd_t sout = make_srd(out, 2048 * 2048 * 2);
#pragma unroll
        for (int n2 = 0; n2 < 2; ++n2) {
            f32x16 v = acc[n2];
#pragma unroll
            for (int qd = 0; qd < 4; ++qd) {
                const f32x4 t = *(const LAS f32x4*)(theirs + n2 * 4096 + qd * 1024);
                v[4 * qd] += t[0]; v[4 * qd + 1] += t[1]; v[4 * qd + 2] += t[2]; v[4 * qd + 3] += t[3];
            }
            const unsigned oo = (unsigned)token * 4096u + (unsigned)(b * 1024 + tn * 128 + (2 * kh + n2) * 32) * 2u + hh * 16;
#pragma unroll
            for (int gp = 0; gp < 2; ++gp) {
                const unsigned a0 = cvtpk(v[8 * gp], v[8 * gp + 1]), a1 = cvtpk(v[8 * gp + 2], v[8 * gp + 3]);
                const unsigned b0 = cvtpk(v[8 * gp + 4], v[8 * gp + 5]), b1 = cvtpk(v[8 * gp + 6], v[8 * gp + 7]);
                auto r0 = __builtin_amdgcn_permlane32_swap(a0, b0, false, false);
                auto r1 = __builtin_amdgcn_permlane32_swap(a1, b1, false, false);
                const u32x4 w = {r0[0], r1[0], r0[1], r1[1]};
                __builtin_amdgcn_raw_buffer_store_b128(w, sout, oo + gp * 32, 0, OUTAUX);
            }
        }
    }
    STAMP_END(ws)
}

extern "C" void kernel_launch(void* const* d_in, const int* in_sizes, int n_in, void* d_out, int out_size, void* d_ws, size_t ws_size, hipStream_t stream) {
    (void)hipFuncSetAttribute((const void*)mha_fused, hipFuncAttributeMaxDynamicSharedMemorySize, L_END);
    mha_fused<<<256, 512, L_END, stream>>>((const uint16_t*)d_in[0], (const uint16_t*)d_in[1], (const uint16_t*)d_in[2], (const uint16_t*)d_in[3], (const uint16_t*)d_in[4],
                                           (const uint16_t*)d_in[5], (char*)d_ws, (uint16_t*)d_out);
}
```

```cpp
#define OUTAUX 16
#define PFD 4
#define PFPRO 1
#define P1FINE
#include <hip/hip_runtime.h>
#include <stdint.h>

typedef __bf16 bf16x8 __attribute__((ext_vector_type(8)));
typedef __bf16 bf16x16 __attribute__((ext_vector_type(16)));
typedef __bf16 bf16x2 __attribute__((ext_vector_type(2)));
typedef short s16x4 __attribute__((ext_vector_type(4)));
typedef short s16x8 __attribute__((ext_vector_type(8)));
typedef float f32x16 __attribute__((ext_vector_type(16)));
typedef float f32x4 __attribute__((ext_vector_type(4)));
typedef float f32x2 __attribute__((ext_vector_type(2)));
typedef unsigned u32x4 __attribute__((ext_vector_type(4)));
typedef unsigned u32x8 __attribute__((ext_vector_type(8)));
typedef unsigned long long u64;
#define LAS __attribute__((address_space(3)))
#define GAS __attribute__((address_space(1)))
#define DI __device__ __forceinline__
#define MFMA32(a, b, c) __builtin_amdgcn_mfma_f32_32x32x16_bf16((a), (b), (c), 0, 0, 0)
#define SMF(a, b, c, idx) __builtin_amdgcn_smfmac_f32_16x16x64_bf16((a), __builtin_bit_cast(bf16x16, b), (c), (idx), 0, 0)
#define PLO(P) __builtin_bit_cast(bf16x8, __builtin_shufflevector(P, P, 0, 1, 2, 3))
#define PHI(P) __builtin_bit_cast(bf16x8, __builtin_shufflevector(P, P, 4, 5, 6, 7))
#define SBAR() __builtin_amdgcn_sched_barrier(0)
#define BF(x) __builtin_bit_cast(bf16x8, x)
#define NOP_ (void)0
typedef __amdgpu_buffer_rsrc_t srd_t;

#define MAPSEL 1
constexpr float QSCALE = 0.17677669529663687f * 1.4426950408889634f;
constexpr unsigned WS_KV = 0;
constexpr unsigned WS_O = 16u << 20;
constexpr unsigned WS_FL = 24u << 20;
constexpr unsigned FL_KV = 4096;
constexpr unsigned FL_O = FL_KV + 16384;
constexpr int L_S0 = 32768, L_S1 = 65536, L_S2 = 0, L_R0 = 98304, L_R1 = 131072, L_END = 163840, L_CNT = L_END - 64;
constexpr int P1_XS = 32768, P1_WS = 24576, P1_WOFF = 98304;

DI unsigned rfl(unsigned x) { return __builtin_amdgcn_readfirstlane(x); }
DI srd_t make_srd(const void* p, unsigned bytes) { return __builtin_amdgcn_make_buffer_rsrc((void*)p, (short)0, (int)bytes, 0x00020000); }
DI void dma16(unsigned ldsdst, unsigned voff, srd_t srd, unsigned soff) {
    asm volatile("s_mov_b32 m0, %0\n\ts_nop 0\n\tbuffer_load_dwordx4 %1, %2, %3 offen lds" :: "s"(ldsdst), "v"(voff), "s"(srd), "s"(soff) : "memory");
}
DI void dma16c(unsigned ldsdst, unsigned voff, srd_t srd, unsigned soff) {
    asm volatile("s_mov_b32 m0, %0\n\ts_nop 0\n\tbuffer_load_dwordx4 %1, %2, %3 offen sc1 lds" :: "s"(ldsdst), "v"(voff), "s"(srd), "s"(soff) : "memory");
}
template <int I> DI void dma4c(unsigned ldsdst, unsigned voff, srd_t srd, unsigned soff) {
    if constexpr (I == 0) asm volatile("s_mov_b32 m0, %0\n\ts_nop 0\n\tbuffer_load_dwordx4 %1, %2, %3 offen sc1 lds" :: "s"(ldsdst), "v"(voff), "s"(srd), "s"(soff) : "memory");
    else asm volatile("buffer_load_dwordx4 %0, %1, %2 offen offset:%3 sc1 lds" :: "v"(voff), "s"(srd), "s"(soff), "i"(I * 1024) : "memory");
}
DI unsigned cvtpk(float lo, float hi) { f32x2 v = {lo, hi}; bf16x2 b = __builtin_convertvector(v, bf16x2); return __builtin_bit_cast(unsigned, b); }
DI bf16x8 ldsfrag(const LAS char* p) { return *(const LAS bf16x8*)p; }
typedef short v4i16_t __attribute__((ext_vector_type(4)));
DI s16x4 ldstr(const LAS char* p) { return __builtin_amdgcn_ds_read_tr16_b64_v4i16((LAS v4i16_t*)p); }
DI bf16x8 cat8(s16x4 lo, s16x4 hi) { s16x8 r = __builtin_shufflevector(lo, hi, 0, 1, 2, 3, 4, 5, 6, 7); return __builtin_bit_cast(bf16x8, r); }
DI u32x4 pack8(const f32x16& x, int s, float mul) {
    u32x4 p; p[0] = cvtpk(x[8 * s] * mul, x[8 * s + 1] * mul); p[1] = cvtpk(x[8 * s + 2] * mul, x[8 * s + 3] * mul);
    p[2] = cvtpk(x[8 * s + 4] * mul, x[8 * s + 5] * mul); p[3] = cvtpk(x[8 * s + 6] * mul, x[8 * s + 7] * mul); return p; }
DI u32x4 pack8n(const f32x16& x, int s) {
    u32x4 p; p[0] = cvtpk(x[8 * s], x[8 * s + 1]); p[1] = cvtpk(x[8 * s + 2], x[8 * s + 3]);
    p[2] = cvtpk(x[8 * s + 4], x[8 * s + 5]); p[3] = cvtpk(x[8 * s + 6], x[8 * s + 7]); return p; }
#define WAITBAR(vm) asm volatile("s_waitcnt vmcnt(" #vm ") lgkmcnt(0)\n\ts_barrier" ::: "memory")
#define LBAR() asm volatile("s_waitcnt lgkmcnt(0)\n\ts_barrier" ::: "memory")
#define WAITV(vm) asm volatile("s_waitcnt vmcnt(" #vm ")" ::: "memory")
DI float ex2(float x) { return __builtin_amdgcn_exp2f(x); }
DI u64 mix64(u64 z) { z += 0x9E3779B97F4A7C15ull; z = (z ^ (z >> 30)) * 0xBF58476D1CE4E5B9ull; z = (z ^ (z >> 27)) * 0x94D049BB133111EBull; return z ^ (z >> 31); }
DI u64 ld_agent(const GAS u64* p) { return __hip_atomic_load(p, __ATOMIC_RELAXED, __HIP_MEMORY_SCOPE_AGENT); }
DI void st_agent(GAS u64* p, u64 v) { __hip_atomic_store(p, v, __ATOMIC_RELAXED, __HIP_MEMORY_SCOPE_AGENT); }
DI void spin_flags(const GAS u64* f, u64 tag) {
    const u64 t0 = __builtin_amdgcn_s_memrealtime();
    u64 v0 = ld_agent(f);
    for (;;) {
        __builtin_amdgcn_s_sleep(6);
        const u64 v1 = ld_agent(f);
        if (__all(v0 == tag)) break;
        __builtin_amdgcn_s_sleep(6);
        v0 = ld_agent(f);
        if (__all(v1 == tag)) break;
        if (__builtin_amdgcn_s_memrealtime() - t0 > 20000000ull) break;
    }
    asm volatile("" ::: "memory");
}
DI void lds_signal(unsigned addr) {
    unsigned one = 1;
    asm volatile("s_waitcnt lgkmcnt(0)\n\ts_mov_b64 exec, 1\n\tds_add_u32 %0, %1\n\ts_mov_b64 exec, -1" :: "v"(addr), "v"(one) : "memory");
}
DI unsigned lds_peek(const LAS char* p) { return *(const volatile LAS unsigned*)p; }
DI void lds_await(unsigned seen, const LAS char* p, unsigned need) {
    if (rfl(seen) < need) {
        _Pragma("nounroll") for (int it = 0; it < (1 << 22); ++it) { if (rfl(lds_peek(p)) >= need) break; __builtin_amdgcn_s_sleep(0); }
    }
    asm volatile("" ::: "memory");
}

#define STAMP_BEGIN
#define STAMP_MID(i)
#define STAMP_END(ws)

__global__ __launch_bounds__(512, 2) void mha_fused(const uint16_t* xA, const uint16_t* wA, const uint16_t* woA, const uint16_t* xB, const uint16_t* wB, const uint16_t* woB,
                                                    char* ws, uint16_t* out) {
    extern __shared__ __attribute__((aligned(16))) char lds_[];
    LAS char* lds = (LAS char*)lds_;
    STAMP_BEGIN
    const int tid = threadIdx.x, lane = tid & 63, wid = rfl(tid >> 6);
    const int bid = blockIdx.x, xcd = bid & 7, j = bid >> 3;
    const int b = xcd >> 2;
    const int mb = (xcd & 1) * 4 + (j & 3), hp = ((xcd >> 1) & 1) * 8 + (j >> 2);
    const int wm = wid & 3, wn = wid >> 2, h = hp * 2 + wn;
    const unsigned lds0 = (unsigned)(uintptr_t)lds_;
    const int r32 = lane & 31, hh = lane >> 5, g4 = lane >> 4, q4 = (lane >> 2) & 3, p4 = lane & 3;
    GAS u64* const flags = (GAS u64*)(ws + WS_FL);
    u64 epoch = ld_agent(flags + b * 32);
    if (tid < 16) *(LAS unsigned*)(lds + L_CNT + tid * 4) = 0u;

    bf16x8 qf[2][2];
    u32x4 kvp[2][4];
    const srd_t skvw = make_srd(ws + WS_KV + (size_t)(b * 32 + h) * 64 * 4096 + (size_t)(mb * 8 + wm * 2) * 4096, 2 * 4096);
    {
        const srd_t sx = make_srd(b ? xB : xA, 2048 * 1024 * 2), sw = make_srd(b ? wB : wA, 1024 * 3072 * 2);
        const unsigned vx0 = (lane >> 3) * 2048 + (((lane & 7) ^ (lane >> 4)) << 4);
        const unsigned vx1 = (lane >> 3) * 2048 + (((lane & 7) ^ (4 + (lane >> 4))) << 4);
        const unsigned vw = (lane >> 3) * 6144 + (((lane & 7) ^ (((lane >> 4) & 1) << 2) ^ ((wid & 1) << 1)) << 4);
        const unsigned sx0 = (mb * 256 + wid * 32) * 2048, sw0 = wid * 8 * 6144 + hp * 128;
        auto dx = [&](int kt, int i) { dma16(lds0 + (kt % 3) * P1_XS + wid * 4096 + i * 1024, (i & 1) ? vx1 : vx0, sx, sx0 + i * 8 * 2048 + kt * 128); };
        auto dw = [&](int kt, int i) { dma16(lds0 + P1_WOFF + (kt & 1) * P1_WS + wid * 1024 + i * 8192, vw, sw, sw0 + i * 2048 + kt * 64 * 6144); };
        for (int i = 0; i < 3; ++i) dw(0, i);
        for (int i = 0; i < 4; ++i) dx(0, i);
        for (int i = 0; i < 3; ++i) dw(1, i);
        for (int i = 0; i < 4; ++i) dx(1, i);
        for (int i = 0; i < 4; ++i) dx(2, i);
        const char* pfp; unsigned pfstep;
        {
            const int l = lane < 10 ? lane : 9;
            const int xrow = (xcd & 1) * 1024 + j * 32 + wid * 4 + (l & 3), wl = wid * 6 + (l - 4);
            const size_t woff = (size_t)(j * 2 + wl / 24) * 6144 + ((wl % 24) >> 3) * 2048 + (((xcd >> 1) & 1) * 8 + (wl & 7)) * 128;
            pfp = l < 4 ? (const char*)(b ? xB : xA) + (size_t)xrow * 2048 : (const char*)(b ? wB : wA) + woff;
            pfstep = l < 4 ? 128u : 64u * 6144u;
        }
        const unsigned pfdst = lds0 + 147456 + wid * 1024;
        auto pf = [&](int kt) {
            const char* g = pfp + (size_t)kt * pfstep;
            asm volatile("s_mov_b32 m0, %0\n\ts_nop 0\n\tglobal_load_lds_dword %1, off" :: "s"(pfdst), "v"(g) : "memory"); };
        if (PFD) for (int i = 0; i < PFPRO; ++i) pf(3 + i);
#define NPRO PFPRO
        const int r16 = lane & 15;
        unsigned xa[2], wb[2];
#pragma unroll
        for (int s = 0; s < 2; ++s) xa[s] = (wm * 64 + r16) * 128 + (((4 * s + g4) ^ (r16 >> 1)) << 4);
#pragma unroll
        for (int c = 0; c < 2; ++c) wb[c] = P1_WOFF + (8 * g4 + q4) * 128 + ((wn ^ (q4 >> 1)) << 6) + ((c ^ (g4 & 1)) << 5) + p4 * 8;
        f32x4 acc[4][6];
#pragma unroll
        for (int a = 0; a < 4; ++a)
#pragma unroll
            for (int c = 0; c < 6; ++c) acc[a][c] = (f32x4){0.f, 0.f, 0.f, 0.f};
        bf16x8 xf[2][4][2], wf[3][2];
#define MFMA16(a, b, c) __builtin_amdgcn_mfma_f32_16x16x32_bf16((a), (b), (c), 0, 0, 0)
#define P1_RX(kt, tb, s) xf[(kt) & 1][tb][s] = ldsfrag(lds + ((kt) % 3) * P1_XS + xa[s] + (tb) * 2048)
#define P1_RW(kt, fb, s) do { const LAS char* wp_ = lds + wb[(fb) & 1] + ((kt) & 1) * P1_WS + ((fb) >> 1) * 8192 + (s) * 4096; wf[(fb) % 3][s] = cat8(ldstr(wp_), ldstr(wp_ + 512)); } while (0)
#define P1_MM(kt, tb, fb) do { if ((fb) < 4) { acc[tb][fb] = MFMA16(wf[(fb) % 3][0], xf[(kt) & 1][tb][0], acc[tb][fb]); acc[tb][fb] = MFMA16(wf[(fb) % 3][1], xf[(kt) & 1][tb][1], acc[tb][fb]); } \
                               else { acc[tb][fb] = MFMA16(xf[(kt) & 1][tb][0], wf[(fb) % 3][0], acc[tb][fb]); acc[tb][fb] = MFMA16(xf[(kt) & 1][tb][1], wf[(fb) % 3][1], acc[tb][fb]); } } while (0)
        s16x4 wlo[3][2], whi[3][2];
#define P1_WL(kt, fb, s) wlo[(fb) % 3][s] = ldstr(lds + wb[(fb) & 1] + ((kt) & 1) * P1_WS + ((fb) >> 1) * 8192 + (s) * 4096)
#define P1_WH(kt, fb, s) whi[(fb) % 3][s] = ldstr(lds + wb[(fb) & 1] + ((kt) & 1) * P1_WS + ((fb) >> 1) * 8192 + (s) * 4096 + 512)
#define P1_M1(kt, tb, fb, s) do { const bf16x8 w_ = cat8(wlo[(fb) % 3][s], whi[(fb) % 3][s]); if ((fb) < 4) acc[tb][fb] = MFMA16(w_, xf[(kt) & 1][tb][s], acc[tb][fb]); else acc[tb][fb] = MFMA16(xf[(kt) & 1][tb][s], w_, acc[tb][fb]); } while (0)
#define P1_FB8(kt, fb, R0, R1, R2, R3, R4, R5, R6, R7) do { \
            P1_M1(kt, 0, fb, 0); R0; SBAR(); P1_M1(kt, 1, fb, 0); R1; SBAR(); P1_M1(kt, 2, fb, 0); R2; SBAR(); P1_M1(kt, 3, fb, 0); R3; SBAR(); \
            P1_M1(kt, 0, fb, 1); R4; SBAR(); P1_M1(kt, 1, fb, 1); R5; SBAR(); P1_M1(kt, 2, fb, 1); R6; SBAR(); P1_M1(kt, 3, fb, 1); R7; SBAR(); } while (0)
#define P1_FB(kt, fb, R0, R1, R2, R3) do { \
            P1_MM(kt, 0, fb); R0; SBAR(); P1_MM(kt, 1, fb); R1; SBAR(); P1_MM(kt, 2, fb); R2; SBAR(); P1_MM(kt, 3, fb); R3; SBAR(); } while (0)
        asm volatile("s_waitcnt vmcnt(%0) lgkmcnt(0)\n\ts_barrier" :: "n"(11 + NPRO) : "memory");
        STAMP_MID(0)
        P1_RX(0, 0, 0); P1_WL(0, 0, 0); P1_WH(0, 0, 0); P1_RX(0, 1, 0); P1_RX(0, 2, 0); P1_RX(0, 3, 0); P1_WL(0, 0, 1); P1_WH(0, 0, 1);
        P1_RX(0, 0, 1); P1_RX(0, 1, 1); P1_RX(0, 2, 1); P1_RX(0, 3, 1); P1_WL(0, 1, 0); P1_WH(0, 1, 0); P1_WL(0, 1, 1); P1_WH(0, 1, 1); SBAR();
#pragma unroll
        for (int t = 0; t < 16; ++t) {
            const bool xs = (t >= 1 && t + 2 < 16);
            P1_FB8(t, 0, P1_WL(t, 2, 0), NOP_, P1_WH(t, 2, 0), NOP_, P1_WL(t, 2, 1), if (xs) dx(t + 2, 0), P1_WH(t, 2, 1), NOP_);
            P1_FB8(t, 1, P1_WL(t, 3, 0), NOP_, P1_WH(t, 3, 0), NOP_, P1_WL(t, 3, 1), if (xs) dx(t + 2, 1), P1_WH(t, 3, 1), NOP_);
            P1_FB8(t, 2, P1_WL(t, 4, 0), NOP_, P1_WH(t, 4, 0), NOP_, P1_WL(t, 4, 1), if (xs) dx(t + 2, 2), P1_WH(t, 4, 1), NOP_);
            P1_FB8(t, 3, P1_WL(t, 5, 0), NOP_, P1_WH(t, 5, 0), NOP_, P1_WL(t, 5, 1), if (xs) dx(t + 2, 3), P1_WH(t, 5, 1), NOP_);
            if (t < 15) {
                { const int npf = t == 0 ? NPRO : (PFD && t - 1 + PFD < 16 ? 1 : 0);
                  const int nw = (t < 14 ? 4 : 0) + (t < 14 ? npf : 0);
                  if (nw == 0) WAITBAR(0); else if (nw == 4) WAITBAR(4); else if (nw == 5) WAITBAR(5); else asm volatile("s_waitcnt vmcnt(%0) lgkmcnt(0)\n\ts_barrier" :: "n"(4 + NPRO) : "memory"); }
                const bool wsch = (t + 2 < 16);
                P1_FB8(t, 4, P1_RX(t + 1, 0, 0), P1_WL(t + 1, 0, 0), P1_WH(t + 1, 0, 0), P1_RX(t + 1, 1, 0), P1_RX(t + 1, 2, 0), P1_RX(t + 1, 3, 0), P1_WL(t + 1, 0, 1), { P1_WH(t + 1, 0, 1); if (wsch) dw(t + 2, 0); });
                P1_FB8(t, 5, P1_RX(t + 1, 0, 1), P1_RX(t + 1, 1, 1), P1_RX(t + 1, 2, 1), { P1_RX(t + 1, 3, 1); if (wsch) dw(t + 2, 1); }, P1_WL(t + 1, 1, 0), { P1_WH(t + 1, 1, 0); if (wsch) dw(t + 2, 2); }, P1_WL(t + 1, 1, 1), { P1_WH(t + 1, 1, 1); if (PFD && t + PFD < 16) pf(t + PFD); });
            } else {
                P1_FB8(t, 4, NOP_, NOP_, NOP_, NOP_, NOP_, NOP_, NOP_, NOP_);
                P1_FB8(t, 5, NOP_, NOP_, NOP_, NOP_, NOP_, NOP_, NOP_, NOP_);
            }
        }
        lds_signal(lds0 + L_CNT + 32);
        STAMP_MID(1)
        auto swap16 = [](unsigned& x, unsigned& y) { auto r = __builtin_amdgcn_permlane16_swap(x, y, false, false); x = r[0]; y = r[1]; };
        auto swap32 = [](unsigned& x, unsigned& y) { auto r = __builtin_amdgcn_permlane32_swap(x, y, false, false); x = r[0]; y = r[1]; };
        auto qkfrag = [&](const f32x4& t0, const f32x4& t1, float mul) {
            unsigned a0 = cvtpk(t0[0] * mul, t0[1] * mul), a1 = cvtpk(t0[2] * mul, t0[3] * mul), b0 = cvtpk(t1[0] * mul, t1[1] * mul), b1 = cvtpk(t1[2] * mul, t1[3] * mul);
            swap16(a0, b0); swap16(a1, b1);
            return (u32x4){a0, a1, b0, b1}; };
        auto vfrag = [&](const f32x4& x0, const f32x4& x1) {
            unsigned a0 = cvtpk(x0[0], x0[1]), a1 = cvtpk(x0[2], x0[3]), b0 = cvtpk(x1[0], x1[1]), b1 = cvtpk(x1[2], x1[3]);
            swap16(a0, b0); swap16(a1, b1); swap32(a0, b0); swap32(a1, b1);
            return (u32x4){a0, a1, b0, b1}; };
#pragma unroll
        for (int a = 0; a < 2; ++a)
#pragma unroll
            for (int s = 0; s < 2; ++s) {
                kvp[a][s] = qkfrag(acc[2 * a][2 + s], acc[2 * a + 1][2 + s], 1.0f); kvp[a][2 + s] = vfrag(acc[2 * a + s][4], acc[2 * a + s][5]);
                __builtin_amdgcn_raw_buffer_store_b128(kvp[a][s], skvw, lane * 16 + a * 4096 + s * 1024, 0, 16);
                __builtin_amdgcn_raw_buffer_store_b128(kvp[a][2 + s], skvw, lane * 16 + a * 4096 + 2048 + s * 1024, 0, 16);
            }
#define P1_QF(a, s) BF(qkfrag(acc[2 * (a)][s], acc[2 * (a) + 1][s], QSCALE))
#pragma unroll
        for (int a = 0; a < 2; ++a) {
            const int tl = wm * 2 + a;
            LAS char* lp = lds + L_S0 + (tl >> 2) * 32768 + wn * 16384 + (tl & 3) * 4096 + lane * 16;
#pragma unroll
            for (int f = 0; f < 4; ++f) *(LAS u32x4*)(lp + f * 1024) = kvp[a][f];
        }
        SBAR();
#pragma unroll
        for (int a = 0; a < 2; ++a)
#pragma unroll
            for (int s = 0; s < 2; ++s) qf[a][s] = P1_QF(a, s);
    }
    asm volatile("" : "+v"(epoch));
    const u64 tag = mix64(((u64)rfl((unsigned)(epoch >> 32)) << 32) | rfl((unsigned)epoch));
    STAMP_MID(2)

    const srd_t skv = make_srd(ws + WS_KV + (size_t)(b * 32 + hp * 2) * 64 * 4096, 2 * 64 * 4096);
    const srd_t swo = make_srd(b ? woB : woA, 1024 * 1024 * 2);
    const int tm = (xcd & 3) * 4 + (j >> 3), tn = j & 7;
    const unsigned vl = lane * 16;
    const unsigned vwo = (lane >> 4) * 2048 + (((lane & 15) ^ (((lane >> 4) & 3) << 2)) << 4);
    auto chunk_off = [&](int c) { const int i = (c - 2) >> 1; const int blk = c < 2 ? mb : (i < mb ? i : i + 1); return (unsigned)((wn * 64 + blk * 8 + (c & 1) * 4 + wm) * 4096); };
    const unsigned ldw = lds0 + wid * 4096;
#define DKV(coff, slotoff, i) dma4c<i>(ldw + (slotoff), vl, skv, coff)
    const unsigned wo0 = (wn * 32 + wm * 8) * 2048 + tn * 256;
    auto dwu = [&](int u, unsigned slotaddr, int p) { dma16(lds0 + slotaddr + wm * 2048 + p * 1024, vwo, swo, wo0 + (u * 64 + p * 4) * 2048); };
    auto wo_rs = [&](int k) { return (unsigned)(L_R0 + wn * 24576 + k * 8192); };
    auto wo_hs = [&](int k) { return (unsigned)((k >> 1 == 0 ? L_S0 : k >> 1 == 1 ? L_S1 : L_S2) + wn * 16384 + (k & 1) * 8192); };
    bf16x8 sel; int selidx;
    { const int i16 = lane & 15; const short lo = (i16 < 2) ? (short)0x3F80 : (short)0, hi = (i16 == 2 || i16 == 3) ? (short)0x3F80 : (short)0;
      s16x8 t = {lo, lo, lo, lo, hi, hi, hi, hi}; sel = __builtin_bit_cast(bf16x8, t); selidx = (i16 & 1) ? 0xEEEE : 0x4444; }
    const LAS char* kvb = lds + wn * 16384 + lane * 16;
    GAS u64* const kvfl = flags + (FL_KV / 8) + (b * 32 + h) * 32;
    const GAS u64* const kvmine = kvfl + ((lane >> 1) & 7) * 4 + (lane & 1) * 2 + (wm >> 1);
    const LAS char* cntp = lds + L_CNT + wn * 16;
    const unsigned cnta = lds0 + L_CNT + wn * 16;
    f32x16 o[2]; f32x4 lacc[2];
    {
        f32x16 negm[2], S[2];
        bf16x8 kf[2][2], vf[2][2];
        u32x8 P;
        const f32x16 zero16 = {0.f,0.f,0.f,0.f,0.f,0.f,0.f,0.f,0.f,0.f,0.f,0.f,0.f,0.f,0.f,0.f};
        kf[0][0] = BF(kvp[0][0]); kf[0][1] = BF(kvp[0][1]); vf[0][0] = BF(kvp[0][2]); vf[0][1] = BF(kvp[0][3]);
#pragma unroll
        for (int a = 0; a < 2; ++a) {
            f32x16 z;
#pragma unroll
            for (int i = 0; i < 16; ++i) z[i] = 0.f;
            z = MFMA32(kf[0][0], qf[a][0], z); z = MFMA32(kf[0][1], qf[a][1], z);
            float m = z[0];
#pragma unroll
            for (int i = 1; i < 16; ++i) m = fmaxf(m, z[i]);
            m = fmaxf(m, __shfl_xor(m, 32));
#pragma unroll
            for (int i = 0; i < 16; ++i) { negm[a][i] = -m; o[a][i] = 0.f; }
            lacc[a] = (f32x4){0.f, 0.f, 0.f, 0.f};
        }
        S[0] = MFMA32(kf[0][0], qf[0][0], negm[0]); S[0] = MFMA32(kf[0][1], qf[0][1], S[0]);
        P = (u32x8){0u, 0u, 0u, 0u, 0u, 0u, 0u, 0u};
        bf16x8 vprev1 = vf[0][1];
        unsigned seen;
#define EXPI(X, i) X[i] = ex2(X[i])
#define PVM(O, V, PP) O = MFMA32(V, PP, O)
#define PKA(L, X, i) NOP_
#define NEGSEL(N) N
#define UNIT(SX, SN, KA0, KA1, QB0, QB1, NEG, DOQK, OPREV, LPREV, VPREV1, OCUR, LCUR, VCUR0, H0, H1, H2, H3) do { \
            if (DOQK) SN = MFMA32(KA0, QB0, NEGSEL(NEG)); H0; SBAR(); \
            EXPI(SX, 0); EXPI(SX, 1); EXPI(SX, 2); SBAR(); \
            LPREV = SMF(sel, P, LPREV, selidx); SBAR(); \
            EXPI(SX, 3); P[0] = cvtpk(SX[0], SX[1]); P[1] = cvtpk(SX[2], SX[3]); PKA(LCUR, SX, 0); PKA(LCUR, SX, 2); SBAR(); \
            PVM(OPREV, VPREV1, PHI(P)); H1; SBAR(); \
            EXPI(SX, 4); EXPI(SX, 5); EXPI(SX, 6); EXPI(SX, 7); P[2] = cvtpk(SX[4], SX[5]); P[3] = cvtpk(SX[6], SX[7]); PKA(LCUR, SX, 4); PKA(LCUR, SX, 6); SBAR(); \
            if (DOQK) SN = MFMA32(KA1, QB1, SN); H2; SBAR(); \
            EXPI(SX, 8); EXPI(SX, 9); EXPI(SX, 10); EXPI(SX, 11); P[4] = cvtpk(SX[8], SX[9]); P[5] = cvtpk(SX[10], SX[11]); PKA(LCUR, SX, 8); PKA(LCUR, SX, 10); SBAR(); \
            PVM(OCUR, VCUR0, PLO(P)); H3; SBAR(); \
            EXPI(SX, 12); EXPI(SX, 13); EXPI(SX, 14); EXPI(SX, 15); P[6] = cvtpk(SX[12], SX[13]); P[7] = cvtpk(SX[14], SX[15]); PKA(LCUR, SX, 12); PKA(LCUR, SX, 14); SBAR(); \
        } while (0)
#define TILEG(cur, nxt, LK, LV, LOAD, A2, A3, B0, B1, B2, B3) do { \
            UNIT(S[0], S[1], kf[cur][0], kf[cur][1], qf[1][0], qf[1][1], negm[1], true, o[1], lacc[1], vprev1, o[0], lacc[0], vf[cur][0], LK, LV, A2, A3); \
            UNIT(S[1], S[0], kf[nxt][0], kf[nxt][1], qf[0][0], qf[0][1], negm[0], LOAD, o[0], lacc[0], vf[cur][1], o[1], lacc[1], vf[cur][0], B0, B1, B2, B3); \
            vprev1 = vf[cur][1]; \
        } while (0)
#define TILEX(cur, nxt, rdp, LOAD, A2, A3, B0, B1, B2, B3) TILEG(cur, nxt, \
            if (LOAD) { kf[nxt][0] = ldsfrag(rdp); kf[nxt][1] = ldsfrag((rdp) + 1024); }, if (LOAD) { vf[nxt][0] = ldsfrag((rdp) + 2048); vf[nxt][1] = ldsfrag((rdp) + 3072); }, \
            LOAD, A2, A3, B0, B1, B2, B3)
#define TILE(cur, nxt, rdp, PK) TILEX(cur, nxt, rdp, true, NOP_, NOP_, NOP_, PK, NOP_, NOP_)
#define TILED(cur, nxt, rdp, coff, slot) TILEX(cur, nxt, rdp, true, DKV(coff, slot, 0), DKV(coff, slot, 1), NOP_, NOP_, DKV(coff, slot, 2), DKV(coff, slot, 3))
#define PEEK seen = lds_peek(cntp)
#define SIGNAL() lds_signal(cnta)
#define FLIP 56
#define BALANCE(m) do { if (wn == 0 && (m) == FLIP) __builtin_amdgcn_s_setprio(0); } while (0)
        if (wn == 0) __builtin_amdgcn_s_setprio(2); else __builtin_amdgcn_s_setprio(1);
        auto own = [&](int p) {
            const int lo = wm < 2;
            const int off = lo ? (p < 4 ? L_S0 + ((1 - wm) * 2 + p - 2) * 4096 : L_S1 + (p - 4) * 4096) : (p < 6 ? L_S0 + (p - 2) * 4096 : L_S1 + ((3 - wm) * 2 + p - 6) * 4096);
            return kvb + off; };
        TILEG(0, 1, { kf[1][0] = BF(kvp[1][0]); kf[1][1] = BF(kvp[1][1]); }, { vf[1][0] = BF(kvp[1][2]); vf[1][1] = BF(kvp[1][3]); }, true, NOP_, NOP_, NOP_, NOP_, NOP_, NOP_);
        lds_signal(cnta + 4); lds_await(0, cntp + 4, 4);
        TILE(1, 0, own(2), NOP_);
        TILE(0, 1, own(3), NOP_);
        STAMP_MID(8)
        WAITV(0); if (lane == 0) st_agent(kvfl + mb * 4 + wm, tag);
        STAMP_MID(9)
        TILE(1, 0, own(4), NOP_);
        u64 pollv = ld_agent(kvmine);
        TILE(0, 1, own(5), NOP_);
        STAMP_MID(10)
        if (!__all(pollv == tag)) spin_flags(kvmine, tag);
        STAMP_MID(11)
        SIGNAL();
        lds_await(0, lds + L_CNT + 32, 8);
        unsigned cn = chunk_off(2);
        TILEX(1, 0, own(6), true, DKV(cn, L_S2, 0), DKV(cn, L_S2, 1), NOP_, PEEK, DKV(cn, L_S2, 2), DKV(cn, L_S2, 3));
        lds_await(seen, cntp, 4);
        cn = chunk_off(3);
        TILED(0, 1, own(7), cn, L_S0);
        WAITV(4); SIGNAL(); lds_await(0, cntp, 8);
        TILE(1, 0, kvb + L_S2, NOP_);
        cn = chunk_off(4);
        TILED(0, 1, kvb + L_S2 + 4096, cn, L_S1);
        WAITV(4); SIGNAL();
        TILE(1, 0, kvb + L_S2 + 8192, NOP_);
        TILE(0, 1, kvb + L_S2 + 12288, PEEK);
        lds_await(seen, cntp, 12);
        WAITV(0); SIGNAL();
        TILE(1, 0, kvb + L_S0, PEEK);
        STAMP_MID(7)
        int sc = L_S0, sn = L_S1, sp = L_S2;
        for (int m = 3; m <= 13; ++m) {
            const LAS char* cp = kvb + sc;
            cn = chunk_off(m + 2);
            lds_await(seen, cntp, 4 * (m + 1));
            BALANCE(4 * m);
            TILED(0, 1, cp + 4096, cn, sp);
            TILE(1, 0, cp + 8192, NOP_);
            TILE(0, 1, cp + 12288, NOP_);
            WAITV(0); SIGNAL();
            TILE(1, 0, kvb + sn, PEEK);
            const int t_ = sc; sc = sn; sn = sp; sp = t_;
        }
        lds_await(seen, cntp, 60);
        BALANCE(56);
        TILEX(0, 1, kvb + L_S2 + 4096, true, dwu(0, wo_rs(0), 0), dwu(0, wo_rs(0), 1), NOP_, NOP_, NOP_, NOP_);
        BALANCE(57);
        TILEX(1, 0, kvb + L_S2 + 8192, true, dwu(1, wo_rs(1), 0), dwu(1, wo_rs(1), 1), NOP_, NOP_, NOP_, NOP_);
        BALANCE(58);
        TILEX(0, 1, kvb + L_S2 + 12288, true, dwu(2, wo_rs(2), 0), dwu(2, wo_rs(2), 1), NOP_, NOP_, NOP_, NOP_);
        BALANCE(59);
        TILE(1, 0, kvb + L_S0, NOP_);
        BALANCE(60);
        TILE(0, 1, kvb + L_S0 + 4096, NOP_);
        BALANCE(61);
        TILE(1, 0, kvb + L_S0 + 8192, NOP_);
        BALANCE(62);
        TILE(0, 1, kvb + L_S0 + 12288, NOP_);
        TILEX(1, 0, kvb, false, NOP_, NOP_, NOP_, NOP_, NOP_, NOP_);
        lacc[1] = SMF(sel, P, lacc[1], selidx); o[1] = MFMA32(vprev1, PHI(P), o[1]);
    }
#define P3PRIO 0
    if (wn == 0) __builtin_amdgcn_s_setprio(P3PRIO);
    STAMP_MID(3)
    {
        float l[2];
        auto rowsum = [&](int a) { const float t0 = __shfl(lacc[a][0] + lacc[a][1], lane & 15), t1 = __shfl(lacc[a][2] + lacc[a][3], lane & 15); return (lane & 16) ? t1 : t0; };
        bool bad = false;
#pragma unroll
        for (int a = 0; a < 2; ++a) {
            l[a] = rowsum(a);
            float z = l[a] * 0.f, z1 = 0.f;
#pragma unroll
            for (int i = 0; i < 16; i += 2) { z = __builtin_fmaf(o[a][i], 0.f, z); z1 = __builtin_fmaf(o[a][i + 1], 0.f, z1); }
            bad |= !(l[a] < 1.0e18f) || !(z + z1 == 0.f);
        }
        if (__any(bad)) {
            spin_flags(kvfl + (lane & 31), tag);
            const srd_t sk1 = make_srd(ws + WS_KV + (size_t)(b * 32 + h) * 64 * 4096, 64 * 4096);
#define LDKV(t, f) BF(__builtin_amdgcn_raw_buffer_load_b128(sk1, lane * 16 + (f) * 1024, (t) * 4096, 16))
            f32x16 negm[2];
#pragma unroll
            for (int a = 0; a < 2; ++a) {
                float m = -__builtin_inff();
                for (int t = 0; t < 64; ++t) {
                    const bf16x8 k0 = LDKV(t, 0), k1 = LDKV(t, 1);
                    f32x16 z;
#pragma unroll
                    for (int i = 0; i < 16; ++i) z[i] = 0.f;
                    z = MFMA32(k0, qf[a][0], z); z = MFMA32(k1, qf[a][1], z);
#pragma unroll
                    for (int i = 0; i < 16; ++i) m = fmaxf(m, z[i]);
                }
                m = fmaxf(m, __shfl_xor(m, 32));
#pragma unroll
                for (int i = 0; i < 16; ++i) { negm[a][i] = -m; o[a][i] = 0.f; }
                lacc[a] = (f32x4){0.f, 0.f, 0.f, 0.f};
            }
            for (int t = 0; t < 64; ++t) {
                const bf16x8 k0 = LDKV(t, 0), k1 = LDKV(t, 1), v0 = LDKV(t, 2), v1 = LDKV(t, 3);
#pragma unroll
                for (int a = 0; a < 2; ++a) {
                    f32x16 z = MFMA32(k0, qf[a][0], negm[a]); z = MFMA32(k1, qf[a][1], z);
                    u32x8 P;
#pragma unroll
                    for (int i = 0; i < 8; ++i) P[i] = cvtpk(ex2(z[2 * i]), ex2(z[2 * i + 1]));
                    o[a] = MFMA32(v0, PLO(P), o[a]); o[a] = MFMA32(v1, PHI(P), o[a]); lacc[a] = SMF(sel, P, lacc[a], selidx);
                }
            }
            l[0] = rowsum(0); l[1] = rowsum(1);
        }
        const srd_t sow = make_srd(ws + WS_O + (size_t)(b * 64 + mb * 8 + wm * 2) * 65536, 2 * 65536);
#pragma unroll
        for (int a = 0; a < 2; ++a) {
            const float rl = __builtin_amdgcn_rcpf(l[a]);
            __builtin_amdgcn_raw_buffer_store_b128(pack8(o[a], 0, rl), sow, lane * 16 + a * 65536 + h * 2048, 0, 16);
            __builtin_amdgcn_raw_buffer_store_b128(pack8(o[a], 1, rl), sow, lane * 16 + a * 65536 + h * 2048 + 1024, 0, 16);
        }
    }
    GAS u64* const ofl = flags + (FL_O / 8) + b * 1024;
    WAITV(0); if (lane == 0) st_agent(ofl + (mb * 4 + wm) * 32 + h, tag);
    STAMP_MID(4)

    {
        const int wt = wm, kh = wn;
        unsigned seen;
        SIGNAL(); lds_await(0, cntp, 64);
        for (int i = 0; i < 6; ++i) { dwu(3 + i, wo_hs(i), 0); dwu(3 + i, wo_hs(i), 1); }
        spin_flags(ofl + (tm * 2 + (wt >> 1)) * 32 + (lane & 15) * 2 + kh, tag);
        STAMP_MID(5)
        const srd_t sor = make_srd(ws + WS_O + (size_t)(b * 64 + tm * 4 + wt) * 65536 + kh * 2048, 65536 - 2048);
        bf16x8 of[8][2], wf[3][4];
#define P3_LO(u) do { of[(u) & 7][0] = BF(__builtin_amdgcn_raw_buffer_load_b128(sor, lane * 16, (u) * 4096, 16)); \
                      of[(u) & 7][1] = BF(__builtin_amdgcn_raw_buffer_load_b128(sor, lane * 16, (u) * 4096 + 1024, 16)); } while (0)
        P3_LO(0); P3_LO(1); P3_LO(2); P3_LO(3); P3_LO(4); P3_LO(5); P3_LO(6); P3_LO(7);
        SIGNAL();
        unsigned wn3[4];
#pragma unroll
        for (int n = 0; n < 4; ++n) wn3[n] = (4 * (g4 >> 1) + q4) * 256 + ((n ^ (kh << 1) ^ q4) << 6) + (g4 & 1) * 32 + p4 * 8;
        f32x16 acc[4];
#pragma unroll
        for (int n = 0; n < 4; ++n)
#pragma unroll
            for (int i = 0; i < 16; ++i) acc[n][i] = 0.f;
        auto uslot = [&](int u) { return u < 3 ? wo_rs(u) : u < 9 ? wo_hs(u - 3) : u < 12 ? wo_rs(u - 9) : wo_hs(u - 12); };
#define P3_RW(kk, n) do { const LAS char* w_ = lds + uslot((kk) >> 1) + ((kk) & 1) * 4096 + wn3[n]; wf[(kk) % 3][n] = cat8(ldstr(w_), ldstr(w_ + 2048)); } while (0)
#define P3_KS(kk, M0, M1) do { \
            acc[0] = MFMA32(wf[(kk) % 3][0], of[((kk) >> 1) & 7][(kk) & 1], acc[0]); if ((kk) + 2 < 32) P3_RW((kk) + 2, 0); SBAR(); \
            acc[1] = MFMA32(wf[(kk) % 3][1], of[((kk) >> 1) & 7][(kk) & 1], acc[1]); if ((kk) + 2 < 32) P3_RW((kk) + 2, 1); M0; SBAR(); \
            acc[2] = MFMA32(wf[(kk) % 3][2], of[((kk) >> 1) & 7][(kk) & 1], acc[2]); if ((kk) + 2 < 32) P3_RW((kk) + 2, 2); SBAR(); \
            acc[3] = MFMA32(wf[(kk) % 3][3], of[((kk) >> 1) & 7][(kk) & 1], acc[3]); if ((kk) + 2 < 32) P3_RW((kk) + 2, 3); M1; SBAR(); \
        } while (0)
#define P3_UNIT(u, M0) do { P3_KS(2 * (u), M0, NOP_); P3_KS(2 * (u) + 1, NOP_, if ((u) + 8 < 16) P3_LO((u) + 8)); } while (0)
#define DWU(u, slot) do { dwu(u, slot, 0); dwu(u, slot, 1); } while (0)
        P3_RW(0, 0); P3_RW(0, 1); P3_RW(0, 2); P3_RW(0, 3); P3_RW(1, 0); P3_RW(1, 1); P3_RW(1, 2); P3_RW(1, 3); SBAR();
        P3_UNIT(0, NOP_);
        P3_UNIT(1, PEEK);
        lds_await(seen, cntp, 68);
        SIGNAL();
        P3_UNIT(2, PEEK);
        lds_await(seen, cntp, 72); DWU(9, wo_rs(0)); DWU(10, wo_rs(1)); DWU(11, wo_rs(2));
        P3_UNIT(3, NOP_);
        P3_UNIT(4, NOP_);
        SIGNAL();
        P3_UNIT(5, PEEK);
        lds_await(seen, cntp, 76); DWU(12, wo_hs(0)); DWU(13, wo_hs(1)); DWU(14, wo_hs(2));
        P3_UNIT(6, NOP_);
        WAITV(14); SIGNAL();
        P3_UNIT(7, PEEK);
        lds_await(seen, cntp, 80);
        SIGNAL();
        P3_UNIT(8, PEEK);
        lds_await(seen, cntp, 84); DWU(15, wo_hs(3));
        P3_UNIT(9, NOP_);
        WAITV(6); SIGNAL();
        P3_UNIT(10, PEEK);
        lds_await(seen, cntp, 88);
        P3_UNIT(11, NOP_);
        P3_UNIT(12, NOP_);
        WAITV(0); SIGNAL();
        P3_UNIT(13, PEEK);
        lds_await(seen, cntp, 92);
        P3_UNIT(14, NOP_);
        P3_UNIT(15, NOP_);
        STAMP_MID(6)
        const LAS char* cnt0 = lds + L_CNT;
        if (kh == 0) {
            SIGNAL(); lds_await(0, cntp, 96);
            LAS char* mine = lds + wo_hs(0) + lane * 16;
            LAS char* dst = wm < 3 ? lds + (wm == 0 ? L_S0 : wm == 1 ? L_S1 : L_S2) + lane * 16 : lds + L_R0 + lane * 16;
            (void)mine;
#pragma unroll
            for (int n = 0; n < 4; ++n)
#pragma unroll
                for (int qd = 0; qd < 4; ++qd)
                    *(LAS f32x4*)(dst + n * 4096 + qd * 1024) = (f32x4){acc[n][4 * qd], acc[n][4 * qd + 1], acc[n][4 * qd + 2], acc[n][4 * qd + 3]};
            SIGNAL();
        } else {
            lds_await(0, cnt0, 100);
            if (tid == 256 && (xcd & 3) == 0 && j == 0) st_agent(flags + b * 32, tag);
            const LAS char* src = wm < 3 ? lds + (wm == 0 ? L_S0 : wm == 1 ? L_S1 : L_S2) + lane * 16 : lds + L_R0 + lane * 16;
            const srd_t sout = make_srd(out, 2048 * 2048 * 2);
            LAS char* stg = lds + (wm < 3 ? wo_rs(wm) : wo_hs(0));
#pragma unroll
            for (int n = 0; n < 4; ++n) {
                f32x16 v = acc[n];
#pragma unroll
                for (int qd = 0; qd < 4; ++qd) {
                    const f32x4 t = *(const LAS f32x4*)(src + (n ^ 2) * 4096 + qd * 1024);
                    v[4 * qd] += t[0]; v[4 * qd + 1] += t[1]; v[4 * qd + 2] += t[2]; v[4 * qd + 3] += t[3];
                }
#pragma unroll
                for (int gp = 0; gp < 2; ++gp) {
                    const unsigned a0 = cvtpk(v[8 * gp], v[8 * gp + 1]), a1 = cvtpk(v[8 * gp + 2], v[8 * gp + 3]);
                    const unsigned b0 = cvtpk(v[8 * gp + 4], v[8 * gp + 5]), b1 = cvtpk(v[8 * gp + 6], v[8 * gp + 7]);
                    auto r0 = __builtin_amdgcn_permlane32_swap(a0, b0, false, false);
                    auto r1 = __builtin_amdgcn_permlane32_swap(a1, b1, false, false);
                    const u32x4 w = {r0[0], r1[0], r0[1], r1[1]};
                    *(LAS u32x4*)(stg + r32 * 256 + ((((n ^ 2) * 4 + gp * 2 + hh) ^ (r32 & 15)) << 4)) = w;
                }
            }
            const unsigned oo = (unsigned)(tm * 128 + wt * 32 + g4) * 4096u + (unsigned)(b * 1024 + tn * 128) * 2u + (lane & 15) * 16;
#pragma unroll
            for (int i = 0; i < 8; ++i) {
                const int row = 4 * i + g4;
                const u32x4 w = *(const LAS u32x4*)(stg + row * 256 + (((lane & 15) ^ (row & 15)) << 4));
                __builtin_amdgcn_raw_buffer_store_b128(w, sout, oo + i * 4 * 4096, 0, OUTAUX);
            }
        }
    }
    STAMP_END(ws)
}

extern "C" void kernel_launch(void* const* d_in, const int* in_sizes, int n_in, void* d_out, int out_size, void* d_ws, size_t ws_size, hipStream_t stream) {
    (void)hipFuncSetAttribute((const void*)mha_fused, hipFuncAttributeMaxDynamicSharedMemorySize, L_END);
    mha_fused<<<256, 512, L_END, stream>>>((const uint16_t*)d_in[0], (const uint16_t*)d_in[1], (const uint16_t*)d_in[2], (const uint16_t*)d_in[3], (const uint16_t*)d_in[4],
                                           (const uint16_t*)d_in[5], (char*)d_ws, (uint16_t*)d_out);
}
```

```cpp
#define OUTAUX 16
#define P1FINE
#include <hip/hip_runtime.h>
#include <stdint.h>

typedef __bf16 bf16x8 __attribute__((ext_vector_type(8)));
typedef __bf16 bf16x16 __attribute__((ext_vector_type(16)));
typedef __bf16 bf16x2 __attribute__((ext_vector_type(2)));
typedef short s16x4 __attribute__((ext_vector_type(4)));
typedef short s16x8 __attribute__((ext_vector_type(8)));
typedef float f32x16 __attribute__((ext_vector_type(16)));
typedef float f32x4 __attribute__((ext_vector_type(4)));
typedef float f32x2 __attribute__((ext_vector_type(2)));
typedef unsigned u32x4 __attribute__((ext_vector_type(4)));
typedef unsigned u32x8 __attribute__((ext_vector_type(8)));
typedef unsigned long long u64;
#define LAS __attribute__((address_space(3)))
#define GAS __attribute__((address_space(1)))
#define DI __device__ __forceinline__
#define MFMA32(a, b, c) __builtin_amdgcn_mfma_f32_32x32x16_bf16((a), (b), (c), 0, 0, 0)
#define SMF(a, b, c, idx) __builtin_amdgcn_smfmac_f32_16x16x64_bf16((a), __builtin_bit_cast(bf16x16, b), (c), (idx), 0, 0)
#define PLO(P) __builtin_bit_cast(bf16x8, __builtin_shufflevector(P, P, 0, 1, 2, 3))
#define PHI(P) __builtin_bit_cast(bf16x8, __builtin_shufflevector(P, P, 4, 5, 6, 7))
#define SBAR() __builtin_amdgcn_sched_barrier(0)
#define BF(x) __builtin_bit_cast(bf16x8, x)
#define NOP_ (void)0
typedef __amdgpu_buffer_rsrc_t srd_t;

#define MAPSEL 1
constexpr float QSCALE = 0.17677669529663687f * 1.4426950408889634f;
constexpr unsigned WS_KV = 0;
constexpr unsigned WS_O = 16u << 20;
constexpr unsigned WS_FL = 24u << 20;
constexpr unsigned FL_KV = 4096;
constexpr unsigned FL_O = FL_KV + 16384;
constexpr int L_S0 = 32768, L_S1 = 65536, L_S2 = 0, L_R0 = 98304, L_R1 = 131072, L_END = 163840, L_CNT = L_END - 64;
constexpr int P1_XS = 32768, P1_WS = 24576, P1_WOFF = 98304;

DI unsigned rfl(unsigned x) { return __builtin_amdgcn_readfirstlane(x); }
DI srd_t make_srd(const void* p, unsigned bytes) { return __builtin_amdgcn_make_buffer_rsrc((void*)p, (short)0, (int)bytes, 0x00020000); }
DI void dma16(unsigned ldsdst, unsigned voff, srd_t srd, unsigned soff) {
    asm volatile("s_mov_b32 m0, %0\n\ts_nop 0\n\tbuffer_load_dwordx4 %1, %2, %3 offen lds" :: "s"(ldsdst), "v"(voff), "s"(srd), "s"(soff) : "memory");
}
DI void dma16c(unsigned ldsdst, unsigned voff, srd_t srd, unsigned soff) {
    asm volatile("s_mov_b32 m0, %0\n\ts_nop 0\n\tbuffer_load_dwordx4 %1, %2, %3 offen sc1 lds" :: "s"(ldsdst), "v"(voff), "s"(srd), "s"(soff) : "memory");
}
template <int I> DI void dma4c(unsigned ldsdst, unsigned voff, srd_t srd, unsigned soff) {
    if constexpr (I == 0) asm volatile("s_mov_b32 m0, %0\n\ts_nop 0\n\tbuffer_load_dwordx4 %1, %2, %3 offen sc1 lds" :: "s"(ldsdst), "v"(voff), "s"(srd), "s"(soff) : "memory");
    else asm volatile("buffer_load_dwordx4 %0, %1, %2 offen offset:%3 sc1 lds" :: "v"(voff), "s"(srd), "s"(soff), "i"(I * 1024) : "memory");
}
DI unsigned cvtpk(float lo, float hi) { f32x2 v = {lo, hi}; bf16x2 b = __builtin_convertvector(v, bf16x2); return __builtin_bit_cast(unsigned, b); }
DI bf16x8 ldsfrag(const LAS char* p) { return *(const LAS bf16x8*)p; }
typedef short v4i16_t __attribute__((ext_vector_type(4)));
DI s16x4 ldstr(const LAS char* p) { return __builtin_amdgcn_ds_read_tr16_b64_v4i16((LAS v4i16_t*)p); }
DI bf16x8 cat8(s16x4 lo, s16x4 hi) { s16x8 r = __builtin_shufflevector(lo, hi, 0, 1, 2, 3, 4, 5, 6, 7); return __builtin_bit_cast(bf16x8, r); }
DI u32x4 pack8(const f32x16& x, int s, float mul) {
    u32x4 p; p[0] = cvtpk(x[8 * s] * mul, x[8 * s + 1] * mul); p[1] = cvtpk(x[8 * s + 2] * mul, x[8 * s + 3] * mul);
    p[2] = cvtpk(x[8 * s + 4] * mul, x[8 * s + 5] * mul); p[3] = cvtpk(x[8 * s + 6] * mul, x[8 * s + 7] * mul); return p; }
DI u32x4 pack8n(const f32x16& x, int s) {
    u32x4 p; p[0] = cvtpk(x[8 * s], x[8 * s + 1]); p[1] = cvtpk(x[8 * s + 2], x[8 * s + 3]);
    p[2] = cvtpk(x[8 * s + 4], x[8 * s + 5]); p[3] = cvtpk(x[8 * s + 6], x[8 * s + 7]); return p; }
#define WAITBAR(vm) asm volatile("s_waitcnt vmcnt(" #vm ") lgkmcnt(0)\n\ts_barrier" ::: "memory")
#define LBAR() asm volatile("s_waitcnt lgkmcnt(0)\n\ts_barrier" ::: "memory")
#define WAITV(vm) asm volatile("s_waitcnt vmcnt(" #vm ")" ::: "memory")
DI float ex2(float x) { return __builtin_amdgcn_exp2f(x); }
DI u64 mix64(u64 z) { z += 0x9E3779B97F4A7C15ull; z = (z ^ (z >> 30)) * 0xBF58476D1CE4E5B9ull; z = (z ^ (z >> 27)) * 0x94D049BB133111EBull; return z ^ (z >> 31); }
DI u64 ld_agent(const GAS u64* p) { return __hip_atomic_load(p, __ATOMIC_RELAXED, __HIP_MEMORY_SCOPE_AGENT); }
DI void st_agent(GAS u64* p, u64 v) { __hip_atomic_store(p, v, __ATOMIC_RELAXED, __HIP_MEMORY_SCOPE_AGENT); }
DI void spin_flags(const GAS u64* f, u64 tag) {
    const u64 t0 = __builtin_amdgcn_s_memrealtime();
    u64 v0 = ld_agent(f);
    for (;;) {
        __builtin_amdgcn_s_sleep(6);
        const u64 v1 = ld_agent(f);
        if (__all(v0 == tag)) break;
        __builtin_amdgcn_s_sleep(6);
        v0 = ld_agent(f);
        if (__all(v1 == tag)) break;
        if (__builtin_amdgcn_s_memrealtime() - t0 > 20000000ull) break;
    }
    asm volatile("" ::: "memory");
}
DI void lds_signal(unsigned addr) {
    unsigned one = 1;
    asm volatile("s_waitcnt lgkmcnt(0)\n\ts_mov_b64 exec, 1\n\tds_add_u32 %0, %1\n\ts_mov_b64 exec, -1" :: "v"(addr), "v"(one) : "memory");
}
DI unsigned lds_peek(const LAS char* p) { return *(const volatile LAS unsigned*)p; }
DI void lds_await(unsigned seen, const LAS char* p, unsigned need) {
    if (rfl(seen) < need) {
        _Pragma("nounroll") for (int it = 0; it < (1 << 22); ++it) { if (rfl(lds_peek(p)) >= need) break; __builtin_amdgcn_s_sleep(0); }
    }
    asm volatile("" ::: "memory");
}

#define STAMP_BEGIN
#define STAMP_MID(i)
#define STAMP_END(ws)

__global__ __launch_bounds__(512, 2) void mha_fused(const uint16_t* xA, const uint16_t* wA, const uint16_t* woA, const uint16_t* xB, const uint16_t* wB, const uint16_t* woB,
                                                    char* ws, uint16_t* out) {
    extern __shared__ __attribute__((aligned(16))) char lds_[];
    LAS char* lds = (LAS char*)lds_;
    STAMP_BEGIN
    const int tid = threadIdx.x, lane = tid & 63, wid = rfl(tid >> 6);
    const int bid = blockIdx.x, xcd = bid & 7, j = bid >> 3;
    const int b = xcd >> 2;
    const int mb = (xcd & 1) * 4 + (j & 3), hp = ((xcd >> 1) & 1) * 8 + (j >> 2);
    const int wm = wid & 3, wn = wid >> 2, h = hp * 2 + wn;
    const unsigned lds0 = (unsigned)(uintptr_t)lds_;
    const int r32 = lane & 31, hh = lane >> 5, g4 = lane >> 4, q4 = (lane >> 2) & 3, p4 = lane & 3;
    GAS u64* const flags = (GAS u64*)(ws + WS_FL);
    u64 epoch;
    if (tid < 16) *(LAS unsigned*)(lds + L_CNT + tid * 4) = 0u;

    bf16x8 qf[2][2];
    u32x4 kvp[2][4];
    const srd_t skvw = make_srd(ws + WS_KV + (size_t)(b * 32 + h) * 64 * 4096 + (size_t)(mb * 8 + wm * 2) * 4096, 2 * 4096);
    {
        const srd_t sx = make_srd(b ? xB : xA, 2048 * 1024 * 2), sw = make_srd(b ? wB : wA, 1024 * 3072 * 2);
        const unsigned vx0 = (lane >> 3) * 2048 + (((lane & 7) ^ (lane >> 4)) << 4);
        const unsigned vx1 = (lane >> 3) * 2048 + (((lane & 7) ^ (4 + (lane >> 4))) << 4);
        const unsigned vw = (lane >> 3) * 6144 + (((lane & 7) ^ (((lane >> 4) & 1) << 2) ^ ((wid & 1) << 1)) << 4);
        const unsigned sx0 = (mb * 256 + wid * 32) * 2048, sw0 = wid * 8 * 6144 + hp * 128;
        auto dx = [&](int kt, int i) { dma16(lds0 + (kt % 3) * P1_XS + wid * 4096 + i * 1024, (i & 1) ? vx1 : vx0, sx, sx0 + i * 8 * 2048 + kt * 128); };
        auto dw = [&](int kt, int i) { dma16(lds0 + P1_WOFF + (kt & 1) * P1_WS + wid * 1024 + i * 8192, vw, sw, sw0 + i * 2048 + kt * 64 * 6144); };
        for (int i = 0; i < 3; ++i) dw(0, i);
        for (int i = 0; i < 4; ++i) dx(0, i);
        for (int i = 0; i < 3; ++i) dw(1, i);
        for (int i = 0; i < 4; ++i) dx(1, i);
        for (int i = 0; i < 4; ++i) dx(2, i);
#define PFD 4
#define PFPRO 1
        const char* pfp; unsigned pfstep;
        {
            const int l = lane < 10 ? lane : 9;
            const int xrow = (xcd & 1) * 1024 + j * 32 + wid * 4 + (l & 3), wl = wid * 6 + (l - 4);
            const size_t woff = (size_t)(j * 2 + wl / 24) * 6144 + ((wl % 24) >> 3) * 2048 + (((xcd >> 1) & 1) * 8 + (wl & 7)) * 128;
            pfp = l < 4 ? (const char*)(b ? xB : xA) + (size_t)xrow * 2048 : (const char*)(b ? wB : wA) + woff;
            pfstep = l < 4 ? 128u : 64u * 6144u;
        }
        const unsigned pfdst = lds0 + 147456 + wid * 1024;
        auto pf = [&](int kt) {
            const char* g = pfp + (size_t)kt * pfstep;
            asm volatile("s_mov_b32 m0, %0\n\ts_nop 0\n\tglobal_load_lds_dword %1, off" :: "s"(pfdst), "v"(g) : "memory"); };
        if (PFD) for (int i = 0; i < PFPRO; ++i) pf(3 + i);
#define NPRO (PFPRO + 1)
        epoch = ld_agent(flags + b * 32);
        const int r16 = lane & 15;
        unsigned xa[2], wb[2];
#pragma unroll
        for (int s = 0; s < 2; ++s) xa[s] = (wm * 64 + r16) * 128 + (((4 * s + g4) ^ (r16 >> 1)) << 4);
#pragma unroll
        for (int c = 0; c < 2; ++c) wb[c] = P1_WOFF + (8 * g4 + q4) * 128 + ((wn ^ (q4 >> 1)) << 6) + ((c ^ (g4 & 1)) << 5) + p4 * 8;
        f32x4 acc[4][6];
#pragma unroll
        for (int a = 0; a < 4; ++a)
#pragma unroll
            for (int c = 0; c < 6; ++c) acc[a][c] = (f32x4){0.f, 0.f, 0.f, 0.f};
        bf16x8 xf[2][4][2], wf[3][2];
#define MFMA16(a, b, c) __builtin_amdgcn_mfma_f32_16x16x32_bf16((a), (b), (c), 0, 0, 0)
#define P1_RX(kt, tb, s) xf[(kt) & 1][tb][s] = ldsfrag(lds + ((kt) % 3) * P1_XS + xa[s] + (tb) * 2048)
#define P1_RW(kt, fb, s) do { const LAS char* wp_ = lds + wb[(fb) & 1] + ((kt) & 1) * P1_WS + ((fb) >> 1) * 8192 + (s) * 4096; wf[(fb) % 3][s] = cat8(ldstr(wp_), ldstr(wp_ + 512)); } while (0)
#define P1_MM(kt, tb, fb) do { if ((fb) < 4) { acc[tb][fb] = MFMA16(wf[(fb) % 3][0], xf[(kt) & 1][tb][0], acc[tb][fb]); acc[tb][fb] = MFMA16(wf[(fb) % 3][1], xf[(kt) & 1][tb][1], acc[tb][fb]); } \
                               else { acc[tb][fb] = MFMA16(xf[(kt) & 1][tb][0], wf[(fb) % 3][0], acc[tb][fb]); acc[tb][fb] = MFMA16(xf[(kt) & 1][tb][1], wf[(fb) % 3][1], acc[tb][fb]); } } while (0)
        s16x4 wlo[3][2], whi[3][2];
#define P1_WL(kt, fb, s) wlo[(fb) % 3][s] = ldstr(lds + wb[(fb) & 1] + ((kt) & 1) * P1_WS + ((fb) >> 1) * 8192 + (s) * 4096)
#define P1_WH(kt, fb, s) whi[(fb) % 3][s] = ldstr(lds + wb[(fb) & 1] + ((kt) & 1) * P1_WS + ((fb) >> 1) * 8192 + (s) * 4096 + 512)
#define P1_M1(kt, tb, fb, s) do { const bf16x8 w_ = cat8(wlo[(fb) % 3][s], whi[(fb) % 3][s]); if ((fb) < 4) acc[tb][fb] = MFMA16(w_, xf[(kt) & 1][tb][s], acc[tb][fb]); else acc[tb][fb] = MFMA16(xf[(kt) & 1][tb][s], w_, acc[tb][fb]); } while (0)
#define P1_FB8(kt, fb, R0, R1, R2, R3, R4, R5, R6, R7) do { \
            P1_M1(kt, 0, fb, 0); R0; SBAR(); P1_M1(kt, 1, fb, 0); R1; SBAR(); P1_M1(kt, 2, fb, 0); R2; SBAR(); P1_M1(kt, 3, fb, 0); R3; SBAR(); \
            P1_M1(kt, 0, fb, 1); R4; SBAR(); P1_M1(kt, 1, fb, 1); R5; SBAR(); P1_M1(kt, 2, fb, 1); R6; SBAR(); P1_M1(kt, 3, fb, 1); R7; SBAR(); } while (0)
#define P1_FB(kt, fb, R0, R1, R2, R3) do { \
            P1_MM(kt, 0, fb); R0; SBAR(); P1_MM(kt, 1, fb); R1; SBAR(); P1_MM(kt, 2, fb); R2; SBAR(); P1_MM(kt, 3, fb); R3; SBAR(); } while (0)
        asm volatile("s_waitcnt vmcnt(%0) lgkmcnt(0)\n\ts_barrier" :: "n"(11 + NPRO) : "memory");
        STAMP_MID(0)
        P1_RX(0, 0, 0); P1_WL(0, 0, 0); P1_WH(0, 0, 0); P1_RX(0, 1, 0); P1_RX(0, 2, 0); P1_RX(0, 3, 0); P1_WL(0, 0, 1); P1_WH(0, 0, 1);
        P1_RX(0, 0, 1); P1_RX(0, 1, 1); P1_RX(0, 2, 1); P1_RX(0, 3, 1); P1_WL(0, 1, 0); P1_WH(0, 1, 0); P1_WL(0, 1, 1); P1_WH(0, 1, 1); SBAR();
#pragma unroll
        for (int t = 0; t < 16; ++t) {
            const bool xs = (t >= 1 && t + 2 < 16);
            P1_FB8(t, 0, P1_WL(t, 2, 0), NOP_, P1_WH(t, 2, 0), NOP_, P1_WL(t, 2, 1), if (xs) dx(t + 2, 0), P1_WH(t, 2, 1), NOP_);
            P1_FB8(t, 1, P1_WL(t, 3, 0), NOP_, P1_WH(t, 3, 0), NOP_, P1_WL(t, 3, 1), if (xs) dx(t + 2, 1), P1_WH(t, 3, 1), NOP_);
            P1_FB8(t, 2, P1_WL(t, 4, 0), NOP_, P1_WH(t, 4, 0), NOP_, P1_WL(t, 4, 1), if (xs) dx(t + 2, 2), P1_WH(t, 4, 1), NOP_);
            P1_FB8(t, 3, P1_WL(t, 5, 0), NOP_, P1_WH(t, 5, 0), NOP_, P1_WL(t, 5, 1), if (xs) dx(t + 2, 3), P1_WH(t, 5, 1), NOP_);
            if (t < 15) {
                { const int npf = t == 0 ? NPRO : (PFD && t - 1 + PFD < 16 ? 1 : 0);
                  const int nw = (t < 14 ? 4 : 0) + (t < 14 ? npf : 0);
                  if (nw == 0) WAITBAR(0); else if (nw == 4) WAITBAR(4); else if (nw == 5) WAITBAR(5); else asm volatile("s_waitcnt vmcnt(%0) lgkmcnt(0)\n\ts_barrier" :: "n"(4 + NPRO) : "memory"); }
                const bool wsch = (t + 2 < 16);
                P1_FB8(t, 4, P1_RX(t + 1, 0, 0), P1_WL(t + 1, 0, 0), P1_WH(t + 1, 0, 0), P1_RX(t + 1, 1, 0), P1_RX(t + 1, 2, 0), P1_RX(t + 1, 3, 0), P1_WL(t + 1, 0, 1), { P1_WH(t + 1, 0, 1); if (wsch) dw(t + 2, 0); });
                P1_FB8(t, 5, P1_RX(t + 1, 0, 1), P1_RX(t + 1, 1, 1), P1_RX(t + 1, 2, 1), { P1_RX(t + 1, 3, 1); if (wsch) dw(t + 2, 1); }, P1_WL(t + 1, 1, 0), { P1_WH(t + 1, 1, 0); if (wsch) dw(t + 2, 2); }, P1_WL(t + 1, 1, 1), { P1_WH(t + 1, 1, 1); if (PFD && t + PFD < 16) pf(t + PFD); });
            } else {
                P1_FB8(t, 4, NOP_, NOP_, NOP_, NOP_, NOP_, NOP_, NOP_, NOP_);
                P1_FB8(t, 5, NOP_, NOP_, NOP_, NOP_, NOP_, NOP_, NOP_, NOP_);
            }
        }
        lds_signal(lds0 + L_CNT + 32);
        STAMP_MID(1)
        auto swap16 = [](unsigned& x, unsigned& y) { auto r = __builtin_amdgcn_permlane16_swap(x, y, false, false); x = r[0]; y = r[1]; };
        auto swap32 = [](unsigned& x, unsigned& y) { auto r = __builtin_amdgcn_permlane32_swap(x, y, false, false); x = r[0]; y = r[1]; };
        auto qkfrag = [&](const f32x4& t0, const f32x4& t1, float mul) {
            unsigned a0 = cvtpk(t0[0] * mul, t0[1] * mul), a1 = cvtpk(t0[2] * mul, t0[3] * mul), b0 = cvtpk(t1[0] * mul, t1[1] * mul), b1 = cvtpk(t1[2] * mul, t1[3] * mul);
            swap16(a0, b0); swap16(a1, b1);
            return (u32x4){a0, a1, b0, b1}; };
        auto vfrag = [&](const f32x4& x0, const f32x4& x1) {
            unsigned a0 = cvtpk(x0[0], x0[1]), a1 = cvtpk(x0[2], x0[3]), b0 = cvtpk(x1[0], x1[1]), b1 = cvtpk(x1[2], x1[3]);
            swap16(a0, b0); swap16(a1, b1); swap32(a0, b0); swap32(a1, b1);
            return (u32x4){a0, a1, b0, b1}; };
#pragma unroll
        for (int a = 0; a < 2; ++a)
#pragma unroll
            for (int s = 0; s < 2; ++s) {
                kvp[a][s] = qkfrag(acc[2 * a][2 + s], acc[2 * a + 1][2 + s], 1.0f); kvp[a][2 + s] = vfrag(acc[2 * a + s][4], acc[2 * a + s][5]);
                __builtin_amdgcn_raw_buffer_store_b128(kvp[a][s], skvw, lane * 16 + a * 4096 + s * 1024, 0, 16);
                __builtin_amdgcn_raw_buffer_store_b128(kvp[a][2 + s], skvw, lane * 16 + a * 4096 + 2048 + s * 1024, 0, 16);
            }
#define P1_QF(a, s) BF(qkfrag(acc[2 * (a)][s], acc[2 * (a) + 1][s], QSCALE))
#pragma unroll
        for (int a = 0; a < 2; ++a) {
            const int tl = wm * 2 + a;
            LAS char* lp = lds + L_S0 + (tl >> 2) * 32768 + wn * 16384 + (tl & 3) * 4096 + lane * 16;
#pragma unroll
            for (int f = 0; f < 4; ++f) *(LAS u32x4*)(lp + f * 1024) = kvp[a][f];
        }
        SBAR();
#pragma unroll
        for (int a = 0; a < 2; ++a)
#pragma unroll
            for (int s = 0; s < 2; ++s) qf[a][s] = P1_QF(a, s);
    }
    asm volatile("" : "+v"(epoch));
    const u64 tag = mix64(((u64)rfl((unsigned)(epoch >> 32)) << 32) | rfl((unsigned)epoch));
    STAMP_MID(2)

    const srd_t skv = make_srd(ws + WS_KV + (size_t)(b * 32 + hp * 2) * 64 * 4096, 2 * 64 * 4096);
    const srd_t swo = make_srd(b ? woB : woA, 1024 * 1024 * 2);
    const int tm = (xcd & 3) * 4 + (j >> 3), tn = j & 7;
    const unsigned vl = lane * 16;
    const unsigned vwo = (lane >> 4) * 2048 + (((lane & 15) ^ (((lane >> 4) & 3) << 2)) << 4);
    auto chunk_off = [&](int c) { const int i = (c - 2) >> 1; const int blk = c < 2 ? mb : (i < mb ? i : i + 1); return (unsigned)((wn * 64 + blk * 8 + (c & 1) * 4 + wm) * 4096); };
    const unsigned ldw = lds0 + wid * 4096;
#define DKV(coff, slotoff, i) dma4c<i>(ldw + (slotoff), vl, skv, coff)
    const unsigned wo0 = (wn * 32 + wm * 8) * 2048 + tn * 256;
    auto dwu = [&](int u, unsigned slotaddr, int p) { dma16(lds0 + slotaddr + wm * 2048 + p * 1024, vwo, swo, wo0 + (u * 64 + p * 4) * 2048); };
    auto wo_rs = [&](int k) { return (unsigned)(L_R0 + wn * 24576 + k * 8192); };
    auto wo_hs = [&](int k) { return (unsigned)((k >> 1 == 0 ? L_S0 : k >> 1 == 1 ? L_S1 : L_S2) + wn * 16384 + (k & 1) * 8192); };
    bf16x8 sel; int selidx;
    { const int i16 = lane & 15; const short lo = (i16 < 2) ? (short)0x3F80 : (short)0, hi = (i16 == 2 || i16 == 3) ? (short)0x3F80 : (short)0;
      s16x8 t = {lo, lo, lo, lo, hi, hi, hi, hi}; sel = __builtin_bit_cast(bf16x8, t); selidx = (i16 & 1) ? 0xEEEE : 0x4444; }
    const LAS char* kvb = lds + wn * 16384 + lane * 16;
    GAS u64* const kvfl = flags + (FL_KV / 8) + (b * 32 + h) * 32;
    const GAS u64* const kvmine = kvfl + ((lane >> 1) & 7) * 4 + (lane & 1) * 2 + (wm >> 1);
    const LAS char* cntp = lds + L_CNT + wn * 16;
    const unsigned cnta = lds0 + L_CNT + wn * 16;
    f32x16 o[2]; f32x4 lacc[2];
    {
        f32x16 negm[2], S[2];
        bf16x8 kf[2][2], vf[2][2];
        u32x8 P;
        const f32x16 zero16 = {0.f,0.f,0.f,0.f,0.f,0.f,0.f,0.f,0.f,0.f,0.f,0.f,0.f,0.f,0.f,0.f};
        kf[0][0] = BF(kvp[0][0]); kf[0][1] = BF(kvp[0][1]); vf[0][0] = BF(kvp[0][2]); vf[0][1] = BF(kvp[0][3]);
#pragma unroll
        for (int a = 0; a < 2; ++a) {
            f32x16 z;
#pragma unroll
            for (int i = 0; i < 16; ++i) z[i] = 0.f;
            z = MFMA32(kf[0][0], qf[a][0], z); z = MFMA32(kf[0][1], qf[a][1], z);
            float m = z[0];
#pragma unroll
            for (int i = 1; i < 16; ++i) m = fmaxf(m, z[i]);
            { auto r_ = __builtin_amdgcn_permlane32_swap(__float_as_uint(m), __float_as_uint(m), false, false); m = fmaxf(__uint_as_float(r_[0]), __uint_as_float(r_[1])); }
#pragma unroll
            for (int i = 0; i < 16; ++i) { negm[a][i] = -m; o[a][i] = 0.f; }
            lacc[a] = (f32x4){0.f, 0.f, 0.f, 0.f};
        }
        S[0] = MFMA32(kf[0][0], qf[0][0], negm[0]); S[0] = MFMA32(kf[0][1], qf[0][1], S[0]);
        P = (u32x8){0u, 0u, 0u, 0u, 0u, 0u, 0u, 0u};
        bf16x8 vprev1 = vf[0][1];
        unsigned seen;
#define EXPI(X, i) X[i] = ex2(X[i])
#define PVM(O, V, PP) O = MFMA32(V, PP, O)
#define PKA(L, X, i) NOP_
#define NEGSEL(N) N
#define UNIT(SX, SN, KA0, KA1, QB0, QB1, NEG, DOQK, OPREV, LPREV, VPREV1, OCUR, LCUR, VCUR0, H0, H1, H2, H3) do { \
            if (DOQK) SN = MFMA32(KA0, QB0, NEGSEL(NEG)); H0; SBAR(); \
            EXPI(SX, 0); EXPI(SX, 1); EXPI(SX, 2); SBAR(); \
            LPREV = SMF(sel, P, LPREV, selidx); SBAR(); \
            EXPI(SX, 3); P[0] = cvtpk(SX[0], SX[1]); P[1] = cvtpk(SX[2], SX[3]); PKA(LCUR, SX, 0); PKA(LCUR, SX, 2); SBAR(); \
            PVM(OPREV, VPREV1, PHI(P)); H1; SBAR(); \
            EXPI(SX, 4); EXPI(SX, 5); EXPI(SX, 6); EXPI(SX, 7); P[2] = cvtpk(SX[4], SX[5]); P[3] = cvtpk(SX[6], SX[7]); PKA(LCUR, SX, 4); PKA(LCUR, SX, 6); SBAR(); \
            if (DOQK) SN = MFMA32(KA1, QB1, SN); H2; SBAR(); \
            EXPI(SX, 8); EXPI(SX, 9); EXPI(SX, 10); EXPI(SX, 11); P[4] = cvtpk(SX[8], SX[9]); P[5] = cvtpk(SX[10], SX[11]); PKA(LCUR, SX, 8); PKA(LCUR, SX, 10); SBAR(); \
            PVM(OCUR, VCUR0, PLO(P)); H3; SBAR(); \
            EXPI(SX, 12); EXPI(SX, 13); EXPI(SX, 14); EXPI(SX, 15); P[6] = cvtpk(SX[12], SX[13]); P[7] = cvtpk(SX[14], SX[15]); PKA(LCUR, SX, 12); PKA(LCUR, SX, 14); SBAR(); \
        } while (0)
#define TILEG(cur, nxt, LK, LV, LOAD, A2, A3, B0, B1, B2, B3) do { \
            UNIT(S[0], S[1], kf[cur][0], kf[cur][1], qf[1][0], qf[1][1], negm[1], true, o[1], lacc[1], vprev1, o[0], lacc[0], vf[cur][0], LK, LV, A2, A3); \
            UNIT(S[1], S[0], kf[nxt][0], kf[nxt][1], qf[0][0], qf[0][1], negm[0], LOAD, o[0], lacc[0], vf[cur][1], o[1], lacc[1], vf[cur][0], B0, B1, B2, B3); \
            vprev1 = vf[cur][1]; \
        } while (0)
#define TILEX(cur, nxt, rdp, LOAD, A2, A3, B0, B1, B2, B3) TILEG(cur, nxt, \
            if (LOAD) { kf[nxt][0] = ldsfrag(rdp); kf[nxt][1] = ldsfrag((rdp) + 1024); }, if (LOAD) { vf[nxt][0] = ldsfrag((rdp) + 2048); vf[nxt][1] = ldsfrag((rdp) + 3072); }, \
            LOAD, A2, A3, B0, B1, B2, B3)
#define TILE(cur, nxt, rdp, PK) TILEX(cur, nxt, rdp, true, NOP_, NOP_, NOP_, PK, NOP_, NOP_)
#define TILED(cur, nxt, rdp, coff, slot) TILEX(cur, nxt, rdp, true, DKV(coff, slot, 0), DKV(coff, slot, 1), NOP_, NOP_, DKV(coff, slot, 2), DKV(coff, slot, 3))
#define PEEK seen = lds_peek(cntp)
#define SIGNAL() lds_signal(cnta)
#define FLIP 56
#define BALANCE(m) do { if (wn == 0 && (m) == FLIP) __builtin_amdgcn_s_setprio(0); } while (0)
        if (wn == 0) __builtin_amdgcn_s_setprio(2); else __builtin_amdgcn_s_setprio(1);
        auto own = [&](int p) {
            const int lo = wm < 2;
            const int off = lo ? (p < 4 ? L_S0 + ((1 - wm) * 2 + p - 2) * 4096 : L_S1 + (p - 4) * 4096) : (p < 6 ? L_S0 + (p - 2) * 4096 : L_S1 + ((3 - wm) * 2 + p - 6) * 4096);
            return kvb + off; };
        TILEG(0, 1, { kf[1][0] = BF(kvp[1][0]); kf[1][1] = BF(kvp[1][1]); }, { vf[1][0] = BF(kvp[1][2]); vf[1][1] = BF(kvp[1][3]); }, true, NOP_, NOP_, NOP_, NOP_, NOP_, NOP_);
        lds_signal(cnta + 4); lds_await(0, cntp + 4, 4);
        TILE(1, 0, own(2), NOP_);
        TILE(0, 1, own(3), NOP_);
        STAMP_MID(8)
        WAITV(0); if (lane == 0) st_agent(kvfl + mb * 4 + wm, tag);
        STAMP_MID(9)
        TILE(1, 0, own(4), NOP_);
        u64 pollv = ld_agent(kvmine);
        TILE(0, 1, own(5), NOP_);
        STAMP_MID(10)
        if (!__all(pollv == tag)) spin_flags(kvmine, tag);
        STAMP_MID(11)
        SIGNAL();
        lds_await(0, lds + L_CNT + 32, 8);
        unsigned cn = chunk_off(2);
        TILEX(1, 0, own(6), true, DKV(cn, L_S2, 0), DKV(cn, L_S2, 1), NOP_, PEEK, DKV(cn, L_S2, 2), DKV(cn, L_S2, 3));
        lds_await(seen, cntp, 4);
        cn = chunk_off(3);
        TILED(0, 1, own(7), cn, L_S0);
        WAITV(4); SIGNAL(); lds_await(0, cntp, 8);
        TILE(1, 0, kvb + L_S2, NOP_);
        cn = chunk_off(4);
        TILED(0, 1, kvb + L_S2 + 4096, cn, L_S1);
        WAITV(4); SIGNAL();
        TILE(1, 0, kvb + L_S2 + 8192, NOP_);
        TILE(0, 1, kvb + L_S2 + 12288, PEEK);
        lds_await(seen, cntp, 12);
        WAITV(0); SIGNAL();
        TILE(1, 0, kvb + L_S0, PEEK);
        STAMP_MID(7)
        int sc = L_S0, sn = L_S1, sp = L_S2;
        for (int m = 3; m <= 13; ++m) {
            const LAS char* cp = kvb + sc;
            cn = chunk_off(m + 2);
            lds_await(seen, cntp, 4 * (m + 1));
            BALANCE(4 * m);
            TILED(0, 1, cp + 4096, cn, sp);
            TILE(1, 0, cp + 8192, NOP_);
            TILE(0, 1, cp + 12288, NOP_);
            WAITV(0); SIGNAL();
            TILE(1, 0, kvb + sn, PEEK);
            const int t_ = sc; sc = sn; sn = sp; sp = t_;
        }
        lds_await(seen, cntp, 60);
        BALANCE(56);
        TILEX(0, 1, kvb + L_S2 + 4096, true, dwu(0, wo_rs(0), 0), dwu(0, wo_rs(0), 1), NOP_, NOP_, NOP_, NOP_);
        BALANCE(57);
        TILEX(1, 0, kvb + L_S2 + 8192, true, dwu(1, wo_rs(1), 0), dwu(1, wo_rs(1), 1), NOP_, NOP_, NOP_, NOP_);
        BALANCE(58);
        TILEX(0, 1, kvb + L_S2 + 12288, true, dwu(2, wo_rs(2), 0), dwu(2, wo_rs(2), 1), NOP_, NOP_, NOP_, NOP_);
        BALANCE(59);
        TILE(1, 0, kvb + L_S0, NOP_);
        BALANCE(60);
        TILE(0, 1, kvb + L_S0 + 4096, NOP_);
        BALANCE(61);
        TILE(1, 0, kvb + L_S0 + 8192, NOP_);
        BALANCE(62);
        TILE(0, 1, kvb + L_S0 + 12288, NOP_);
        TILEX(1, 0, kvb, false, NOP_, NOP_, NOP_, NOP_, NOP_, NOP_);
        lacc[1] = SMF(sel, P, lacc[1], selidx); o[1] = MFMA32(vprev1, PHI(P), o[1]);
    }
#define P3PRIO 0
    if (wn == 0) __builtin_amdgcn_s_setprio(P3PRIO);
    STAMP_MID(3)
    {
        float l[2];
        auto rowsum = [&](int a) { const float t0 = __shfl(lacc[a][0] + lacc[a][1], lane & 15), t1 = __shfl(lacc[a][2] + lacc[a][3], lane & 15); return (lane & 16) ? t1 : t0; };
        bool bad = false;
#pragma unroll
        for (int a = 0; a < 2; ++a) {
            l[a] = rowsum(a);
            float z = l[a] * 0.f, z1 = 0.f;
#pragma unroll
            for (int i = 0; i < 16; i += 2) { z = __builtin_fmaf(o[a][i], 0.f, z); z1 = __builtin_fmaf(o[a][i + 1], 0.f, z1); }
            bad |= !(l[a] < 1.0e18f) || !(z + z1 == 0.f);
        }
        if (__any(bad)) {
            spin_flags(kvfl + (lane & 31), tag);
            const srd_t sk1 = make_srd(ws + WS_KV + (size_t)(b * 32 + h) * 64 * 4096, 64 * 4096);
#define LDKV(t, f) BF(__builtin_amdgcn_raw_buffer_load_b128(sk1, lane * 16 + (f) * 1024, (t) * 4096, 16))
            f32x16 negm[2];
#pragma unroll
            for (int a = 0; a < 2; ++a) {
                float m = -__builtin_inff();
                for (int t = 0; t < 64; ++t) {
                    const bf16x8 k0 = LDKV(t, 0), k1 = LDKV(t, 1);
                    f32x16 z;
#pragma unroll
                    for (int i = 0; i < 16; ++i) z[i] = 0.f;
                    z = MFMA32(k0, qf[a][0], z); z = MFMA32(k1, qf[a][1], z);
#pragma unroll
                    for (int i = 0; i < 16; ++i) m = fmaxf(m, z[i]);
                }
                m = fmaxf(m, __shfl_xor(m, 32));
#pragma unroll
                for (int i = 0; i < 16; ++i) { negm[a][i] = -m; o[a][i] = 0.f; }
                lacc[a] = (f32x4){0.f, 0.f, 0.f, 0.f};
            }
            for (int t = 0; t < 64; ++t) {
                const bf16x8 k0 = LDKV(t, 0), k1 = LDKV(t, 1), v0 = LDKV(t, 2), v1 = LDKV(t, 3);
#pragma unroll
                for (int a = 0; a < 2; ++a) {
                    f32x16 z = MFMA32(k0, qf[a][0], negm[a]); z = MFMA32(k1, qf[a][1], z);
                    u32x8 P;
#pragma unroll
                    for (int i = 0; i < 8; ++i) P[i] = cvtpk(ex2(z[2 * i]), ex2(z[2 * i + 1]));
                    o[a] = MFMA32(v0, PLO(P), o[a]); o[a] = MFMA32(v1, PHI(P), o[a]); lacc[a] = SMF(sel, P, lacc[a], selidx);
                }
            }
            l[0] = rowsum(0); l[1] = rowsum(1);
        }
        const srd_t sow = make_srd(ws + WS_O + (size_t)(b * 64 + mb * 8 + wm * 2) * 65536, 2 * 65536);
#pragma unroll
        for (int a = 0; a < 2; ++a) {
            const float rl = __builtin_amdgcn_rcpf(l[a]);
            __builtin_amdgcn_raw_buffer_store_b128(pack8(o[a], 0, rl), sow, lane * 16 + a * 65536 + h * 2048, 0, 16);
            __builtin_amdgcn_raw_buffer_store_b128(pack8(o[a], 1, rl), sow, lane * 16 + a * 65536 + h * 2048 + 1024, 0, 16);
        }
    }
    GAS u64* const ofl = flags + (FL_O / 8) + b * 1024;
    WAITV(0); if (lane == 0) st_agent(ofl + (mb * 4 + wm) * 32 + h, tag);
    STAMP_MID(4)

    {
        const int wt = wm, kh = wn;
        unsigned seen;
        SIGNAL(); lds_await(0, cntp, 64);
        for (int i = 0; i < 6; ++i) { dwu(3 + i, wo_hs(i), 0); dwu(3 + i, wo_hs(i), 1); }
        spin_flags(ofl + (tm * 2 + (wt >> 1)) * 32 + (lane & 15) * 2 + kh, tag);
        STAMP_MID(5)
        const srd_t sor = make_srd(ws + WS_O + (size_t)(b * 64 + tm * 4 + wt) * 65536 + kh * 2048, 65536 - 2048);
        bf16x8 of[8][2], wf[3][4];
#define P3_LO(u) do { of[(u) & 7][0] = BF(__builtin_amdgcn_raw_buffer_load_b128(sor, lane * 16, (u) * 4096, 16)); \
                      of[(u) & 7][1] = BF(__builtin_amdgcn_raw_buffer_load_b128(sor, lane * 16, (u) * 4096 + 1024, 16)); } while (0)
        P3_LO(0); P3_LO(1); P3_LO(2); P3_LO(3); P3_LO(4); P3_LO(5); P3_LO(6); P3_LO(7);
        SIGNAL();
        unsigned wn3[4];
#pragma unroll
        for (int n = 0; n < 4; ++n) wn3[n] = (4 * (g4 >> 1) + q4) * 256 + ((n ^ (kh << 1) ^ q4) << 6) + (g4 & 1) * 32 + p4 * 8;
        f32x16 acc[4];
#pragma unroll
        for (int n = 0; n < 4; ++n)
#pragma unroll
            for (int i = 0; i < 16; ++i) acc[n][i] = 0.f;
        auto uslot = [&](int u) { return u < 3 ? wo_rs(u) : u < 9 ? wo_hs(u - 3) : u < 12 ? wo_rs(u - 9) : wo_hs(u - 12); };
#define P3_RW(kk, n) do { const LAS char* w_ = lds + uslot((kk) >> 1) + ((kk) & 1) * 4096 + wn3[n]; wf[(kk) % 3][n] = cat8(ldstr(w_), ldstr(w_ + 2048)); } while (0)
#define P3_KS(kk, M0, M1) do { \
            acc[0] = MFMA32(wf[(kk) % 3][0], of[((kk) >> 1) & 7][(kk) & 1], acc[0]); if ((kk) + 2 < 32) P3_RW((kk) + 2, 0); SBAR(); \
            acc[1] = MFMA32(wf[(kk) % 3][1], of[((kk) >> 1) & 7][(kk) & 1], acc[1]); if ((kk) + 2 < 32) P3_RW((kk) + 2, 1); M0; SBAR(); \
            acc[2] = MFMA32(wf[(kk) % 3][2], of[((kk) >> 1) & 7][(kk) & 1], acc[2]); if ((kk) + 2 < 32) P3_RW((kk) + 2, 2); SBAR(); \
            acc[3] = MFMA32(wf[(kk) % 3][3], of[((kk) >> 1) & 7][(kk) & 1], acc[3]); if ((kk) + 2 < 32) P3_RW((kk) + 2, 3); M1; SBAR(); \
        } while (0)
#define P3_UNIT(u, M0) do { P3_KS(2 * (u), M0, NOP_); P3_KS(2 * (u) + 1, NOP_, if ((u) + 8 < 16) P3_LO((u) + 8)); } while (0)
#define DWU(u, slot) do { dwu(u, slot, 0); dwu(u, slot, 1); } while (0)
        P3_RW(0, 0); P3_RW(0, 1); P3_RW(0, 2); P3_RW(0, 3); P3_RW(1, 0); P3_RW(1, 1); P3_RW(1, 2); P3_RW(1, 3); SBAR();
        P3_UNIT(0, NOP_);
        P3_UNIT(1, PEEK);
        lds_await(seen, cntp, 68);
        SIGNAL();
        P3_UNIT(2, PEEK);
        lds_await(seen, cntp, 72); DWU(9, wo_rs(0)); DWU(10, wo_rs(1)); DWU(11, wo_rs(2));
        P3_UNIT(3, NOP_);
        P3_UNIT(4, NOP_);
        SIGNAL();
        P3_UNIT(5, PEEK);
        lds_await(seen, cntp, 76); DWU(12, wo_hs(0)); DWU(13, wo_hs(1)); DWU(14, wo_hs(2));
        P3_UNIT(6, NOP_);
        WAITV(14); SIGNAL();
        P3_UNIT(7, PEEK);
        lds_await(seen, cntp, 80);
        SIGNAL();
        P3_UNIT(8, PEEK);
        lds_await(seen, cntp, 84); DWU(15, wo_hs(3));
        P3_UNIT(9, NOP_);
        WAITV(6); SIGNAL();
        P3_UNIT(10, PEEK);
        lds_await(seen, cntp, 88);
        P3_UNIT(11, NOP_);
        P3_UNIT(12, NOP_);
        WAITV(0); SIGNAL();
        P3_UNIT(13, PEEK);
        lds_await(seen, cntp, 92);
        P3_UNIT(14, NOP_);
        P3_UNIT(15, NOP_);
        STAMP_MID(6)
        const LAS char* cnt0 = lds + L_CNT;
        if (kh == 0) {
            SIGNAL(); lds_await(0, cntp, 96);
            LAS char* mine = lds + wo_hs(0) + lane * 16;
            LAS char* dst = wm < 3 ? lds + (wm == 0 ? L_S0 : wm == 1 ? L_S1 : L_S2) + lane * 16 : lds + L_R0 + lane * 16;
            (void)mine;
#pragma unroll
            for (int n = 0; n < 4; ++n)
#pragma unroll
                for (int qd = 0; qd < 4; ++qd)
                    *(LAS f32x4*)(dst + n * 4096 + qd * 1024) = (f32x4){acc[n][4 * qd], acc[n][4 * qd + 1], acc[n][4 * qd + 2], acc[n][4 * qd + 3]};
            SIGNAL();
        } else {
            lds_await(0, cnt0, 100);
            if (tid == 256 && (xcd & 3) == 0 && j == 0) st_agent(flags + b * 32, tag);
            const LAS char* src = wm < 3 ? lds + (wm == 0 ? L_S0 : wm == 1 ? L_S1 : L_S2) + lane * 16 : lds + L_R0 + lane * 16;
            const srd_t sout = make_srd(out, 2048 * 2048 * 2);
            LAS char* stg = lds + (wm < 3 ? wo_rs(wm) : wo_hs(0));
#pragma unroll
            for (int n = 0; n < 4; ++n) {
                f32x16 v = acc[n];
#pragma unroll
                for (int qd = 0; qd < 4; ++qd) {
                    const f32x4 t = *(const LAS f32x4*)(src + (n ^ 2) * 4096 + qd * 1024);
                    v[4 * qd] += t[0]; v[4 * qd + 1] += t[1]; v[4 * qd + 2] += t[2]; v[4 * qd + 3] += t[3];
                }
#pragma unroll
                for (int gp = 0; gp < 2; ++gp) {
                    const unsigned a0 = cvtpk(v[8 * gp], v[8 * gp + 1]), a1 = cvtpk(v[8 * gp + 2], v[8 * gp + 3]);
                    const unsigned b0 = cvtpk(v[8 * gp + 4], v[8 * gp + 5]), b1 = cvtpk(v[8 * gp + 6], v[8 * gp + 7]);
                    auto r0 = __builtin_amdgcn_permlane32_swap(a0, b0, false, false);
                    auto r1 = __builtin_amdgcn_permlane32_swap(a1, b1, false, false);
                    const u32x4 w = {r0[0], r1[0], r0[1], r1[1]};
                    *(LAS u32x4*)(stg + r32 * 256 + ((((n ^ 2) * 4 + gp * 2 + hh) ^ (r32 & 15)) << 4)) = w;
                }
            }
            const unsigned oo = (unsigned)(tm * 128 + wt * 32 + g4) * 4096u + (unsigned)(b * 1024 + tn * 128) * 2u + (lane & 15) * 16;
#pragma unroll
            for (int i = 0; i < 8; ++i) {
                const int row = 4 * i + g4;
                const u32x4 w = *(const LAS u32x4*)(stg + row * 256 + (((lane & 15) ^ (row & 15)) << 4));
                __builtin_amdgcn_raw_buffer_store_b128(w, sout, oo + i * 4 * 4096, 0, OUTAUX);
            }
        }
    }
    STAMP_END(ws)
}

extern "C" void kernel_launch(void* const* d_in, const int* in_sizes, int n_in, void* d_out, int out_size, void* d_ws, size_t ws_size, hipStream_t stream) {
    (void)hipFuncSetAttribute((const void*)mha_fused, hipFuncAttributeMaxDynamicSharedMemorySize, L_END);
    mha_fused<<<256, 512, L_END, stream>>>((const uint16_t*)d_in[0], (const uint16_t*)d_in[1], (const uint16_t*)d_in[2], (const uint16_t*)d_in[3], (const uint16_t*)d_in[4],
                                           (const uint16_t*)d_in[5], (char*)d_ws, (uint16_t*)d_out);
}
```

```cpp
#include <hip/hip_runtime.h>
#include <stdint.h>

typedef __bf16 bf16x8 __attribute__((ext_vector_type(8)));
typedef __bf16 bf16x16 __attribute__((ext_vector_type(16)));
typedef __bf16 bf16x2 __attribute__((ext_vector_type(2)));
typedef short s16x4 __attribute__((ext_vector_type(4)));
typedef short s16x8 __attribute__((ext_vector_type(8)));
typedef float f32x16 __attribute__((ext_vector_type(16)));
typedef float f32x4 __attribute__((ext_vector_type(4)));
typedef float f32x2 __attribute__((ext_vector_type(2)));
typedef unsigned u32x4 __attribute__((ext_vector_type(4)));
typedef unsigned u32x8 __attribute__((ext_vector_type(8)));
typedef unsigned long long u64;
#define LAS __attribute__((address_space(3)))
#define GAS __attribute__((address_space(1)))
#define DI __device__ __forceinline__
#define MFMA32(a, b, c) __builtin_amdgcn_mfma_f32_32x32x16_bf16((a), (b), (c), 0, 0, 0)
#define MFMA16(a, b, c) __builtin_amdgcn_mfma_f32_16x16x32_bf16((a), (b), (c), 0, 0, 0)
#define SMF(a, b, c, idx) __builtin_amdgcn_smfmac_f32_16x16x64_bf16((a), __builtin_bit_cast(bf16x16, b), (c), (idx), 0, 0)
#define PLO(P) __builtin_bit_cast(bf16x8, __builtin_shufflevector(P, P, 0, 1, 2, 3))
#define PHI(P) __builtin_bit_cast(bf16x8, __builtin_shufflevector(P, P, 4, 5, 6, 7))
#define SBAR() __builtin_amdgcn_sched_barrier(0)
#define BF(x) __builtin_bit_cast(bf16x8, x)
#define NOP_ (void)0
typedef __amdgpu_buffer_rsrc_t srd_t;

constexpr float QSCALE = 0.17677669529663687f * 1.4426950408889634f;
constexpr unsigned WS_KV = 0;
constexpr unsigned WS_O = 16u << 20;
constexpr unsigned WS_FL = 24u << 20;
constexpr unsigned FL_KV = 4096;
constexpr unsigned FL_O = FL_KV + 16384;
constexpr int L_S0 = 32768, L_S1 = 65536, L_S2 = 0, L_R0 = 98304, L_END = 163840, L_CNT = L_END - 64, L_DUMMY = 147456;
constexpr int P1_XS = 32768, P1_WS = 24576, P1_WOFF = 98304;

DI unsigned rfl(unsigned x) { return __builtin_amdgcn_readfirstlane(x); }
DI srd_t make_srd(const void* p, unsigned bytes) { return __builtin_amdgcn_make_buffer_rsrc((void*)p, (short)0, (int)bytes, 0x00020000); }
DI void dma16(unsigned ldsdst, unsigned voff, srd_t srd, unsigned soff) {
    asm volatile("s_mov_b32 m0, %0\n\ts_nop 0\n\tbuffer_load_dwordx4 %1, %2, %3 offen lds" :: "s"(ldsdst), "v"(voff), "s"(srd), "s"(soff) : "memory");
}
template <int I> DI void dma4c(unsigned ldsdst, unsigned voff, srd_t srd, unsigned soff) {
    if constexpr (I == 0) asm volatile("s_mov_b32 m0, %0\n\ts_nop 0\n\tbuffer_load_dwordx4 %1, %2, %3 offen sc1 lds" :: "s"(ldsdst), "v"(voff), "s"(srd), "s"(soff) : "memory");
    else asm volatile("buffer_load_dwordx4 %0, %1, %2 offen offset:%3 sc1 lds" :: "v"(voff), "s"(srd), "s"(soff), "i"(I * 1024) : "memory");
}
DI unsigned cvtpk(float lo, float hi) { f32x2 v = {lo, hi}; bf16x2 b = __builtin_convertvector(v, bf16x2); return __builtin_bit_cast(unsigned, b); }
DI bf16x8 ldsfrag(const LAS char* p) { return *(const LAS bf16x8*)p; }
typedef short v4i16_t __attribute__((ext_vector_type(4)));
DI s16x4 ldstr(const LAS char* p) { return __builtin_amdgcn_ds_read_tr16_b64_v4i16((LAS v4i16_t*)p); }
DI bf16x8 cat8(s16x4 lo, s16x4 hi) { s16x8 r = __builtin_shufflevector(lo, hi, 0, 1, 2, 3, 4, 5, 6, 7); return __builtin_bit_cast(bf16x8, r); }
DI u32x4 pack8(const f32x16& x, int s, float mul) {
    u32x4 p; p[0] = cvtpk(x[8 * s] * mul, x[8 * s + 1] * mul); p[1] = cvtpk(x[8 * s + 2] * mul, x[8 * s + 3] * mul);
    p[2] = cvtpk(x[8 * s + 4] * mul, x[8 * s + 5] * mul); p[3] = cvtpk(x[8 * s + 6] * mul, x[8 * s + 7] * mul); return p; }
#define WAITBAR(vm) asm volatile("s_waitcnt vmcnt(" #vm ") lgkmcnt(0)\n\ts_barrier" ::: "memory")
#define WAITV(vm) asm volatile("s_waitcnt vmcnt(" #vm ")" ::: "memory")
DI float ex2(float x) { return __builtin_amdgcn_exp2f(x); }
DI float halfmax(float m) { auto r = __builtin_amdgcn_permlane32_swap(__float_as_uint(m), __float_as_uint(m), false, false); return fmaxf(__uint_as_float(r[0]), __uint_as_float(r[1])); }
DI u64 mix64(u64 z) { z += 0x9E3779B97F4A7C15ull; z = (z ^ (z >> 30)) * 0xBF58476D1CE4E5B9ull; z = (z ^ (z >> 27)) * 0x94D049BB133111EBull; return z ^ (z >> 31); }
DI u64 ld_agent(const GAS u64* p) { return __hip_atomic_load(p, __ATOMIC_RELAXED, __HIP_MEMORY_SCOPE_AGENT); }
DI void st_agent(GAS u64* p, u64 v) { __hip_atomic_store(p, v, __ATOMIC_RELAXED, __HIP_MEMORY_SCOPE_AGENT); }
DI void spin_flags(const GAS u64* f, u64 tag) {
    const u64 t0 = __builtin_amdgcn_s_memrealtime();
    u64 v0 = ld_agent(f);
    for (;;) {
        __builtin_amdgcn_s_sleep(6);
        const u64 v1 = ld_agent(f);
        if (__all(v0 == tag)) break;
        __builtin_amdgcn_s_sleep(6);
        v0 = ld_agent(f);
        if (__all(v1 == tag)) break;
        if (__builtin_amdgcn_s_memrealtime() - t0 > 20000000ull) break;
    }
    asm volatile("" ::: "memory");
}
DI void lds_signal(unsigned addr) {
    unsigned one = 1;
    asm volatile("s_waitcnt lgkmcnt(0)\n\ts_mov_b64 exec, 1\n\tds_add_u32 %0, %1\n\ts_mov_b64 exec, -1" :: "v"(addr), "v"(one) : "memory");
}
DI unsigned lds_peek(const LAS char* p) { return *(const volatile LAS unsigned*)p; }
DI void lds_await(unsigned seen, const LAS char* p, unsigned need) {
    if (rfl(seen) < need) {
        _Pragma("nounroll") for (int it = 0; it < (1 << 22); ++it) { if (rfl(lds_peek(p)) >= need) break; __builtin_amdgcn_s_sleep(0); }
    }
    asm volatile("" ::: "memory");
}

constexpr int FLIP = 56;

__global__ __launch_bounds__(512, 2) void mha_fused(const uint16_t* xA, const uint16_t* wA, const uint16_t* woA, const uint16_t* xB, const uint16_t* wB, const uint16_t* woB,
                                                    char* ws, uint16_t* out) {
    extern __shared__ __attribute__((aligned(16))) char lds_[];
    LAS char* lds = (LAS char*)lds_;
    const int tid = threadIdx.x, lane = tid & 63, wid = rfl(tid >> 6);
    const int bid = blockIdx.x, xcd = bid & 7, j = bid >> 3;
    const int b = xcd >> 2;
    const int mb = (xcd & 1) * 4 + (j & 3), hp = ((xcd >> 1) & 1) * 8 + (j >> 2);
    const int wm = wid & 3, wn = wid >> 2, h = hp * 2 + wn;
    const unsigned lds0 = (unsigned)(uintptr_t)lds_;
    const int r32 = lane & 31, hh = lane >> 5, g4 = lane >> 4, q4 = (lane >> 2) & 3, p4 = lane & 3;
    GAS u64* const flags = (GAS u64*)(ws + WS_FL);
    u64 epoch;
    if (tid < 16) *(LAS unsigned*)(lds + L_CNT + tid * 4) = 0u;

    bf16x8 qf[2][2];
    u32x4 kvp[2][4];
    const srd_t skvw = make_srd(ws + WS_KV + (size_t)(b * 32 + h) * 64 * 4096 + (size_t)(mb * 8 + wm * 2) * 4096, 2 * 4096);
    {
        const srd_t sx = make_srd(b ? xB : xA, 2048 * 1024 * 2), sw = make_srd(b ? wB : wA, 1024 * 3072 * 2);
        const unsigned vx0 = (lane >> 3) * 2048 + (((lane & 7) ^ (lane >> 4)) << 4);
        const unsigned vx1 = (lane >> 3) * 2048 + (((lane & 7) ^ (4 + (lane >> 4))) << 4);
        const unsigned vw = (lane >> 3) * 6144 + (((lane & 7) ^ (((lane >> 4) & 1) << 2) ^ ((wid & 1) << 1)) << 4);
        const unsigned sx0 = (mb * 256 + wid * 32) * 2048, sw0 = wid * 8 * 6144 + hp * 128;
        auto dx = [&](int kt, int i) { dma16(lds0 + (kt % 3) * P1_XS + wid * 4096 + i * 1024, (i & 1) ? vx1 : vx0, sx, sx0 + i * 8 * 2048 + kt * 128); };
        auto dw = [&](int kt, int i) { dma16(lds0 + P1_WOFF + (kt & 1) * P1_WS + wid * 1024 + i * 8192, vw, sw, sw0 + i * 2048 + kt * 64 * 6144); };
        for (int i = 0; i < 3; ++i) dw(0, i);
        for (int i = 0; i < 4; ++i) dx(0, i);
        for (int i = 0; i < 3; ++i) dw(1, i);
        for (int i = 0; i < 4; ++i) dx(1, i);
        for (int i = 0; i < 4; ++i) dx(2, i);
        constexpr int PFD = 4;
        const char* pfp; unsigned pfstep;
        {
            const int l = lane < 10 ? lane : 9;
            const int xrow = (xcd & 1) * 1024 + j * 32 + wid * 4 + (l & 3), wl = wid * 6 + (l - 4);
            const size_t woff = (size_t)(j * 2 + wl / 24) * 6144 + ((wl % 24) >> 3) * 2048 + (((xcd >> 1) & 1) * 8 + (wl & 7)) * 128;
            pfp = l < 4 ? (const char*)(b ? xB : xA) + (size_t)xrow * 2048 : (const char*)(b ? wB : wA) + woff;
            pfstep = l < 4 ? 128u : 64u * 6144u;
        }
        const unsigned pfdst = lds0 + L_DUMMY + wid * 1024;
        auto pf = [&](int kt) {
            const char* g = pfp + (size_t)kt * pfstep;
            asm volatile("s_mov_b32 m0, %0\n\ts_nop 0\n\tglobal_load_lds_dword %1, off" :: "s"(pfdst), "v"(g) : "memory"); };
        pf(3);
        epoch = ld_agent(flags + b * 32);
        const int r16 = lane & 15;
        unsigned xa[2], wb[2];
#pragma unroll
        for (int s = 0; s < 2; ++s) xa[s] = (wm * 64 + r16) * 128 + (((4 * s + g4) ^ (r16 >> 1)) << 4);
#pragma unroll
        for (int c = 0; c < 2; ++c) wb[c] = P1_WOFF + (8 * g4 + q4) * 128 + ((wn ^ (q4 >> 1)) << 6) + ((c ^ (g4 & 1)) << 5) + p4 * 8;
        f32x4 acc[4][6];
#pragma unroll
        for (int a = 0; a < 4; ++a)
#pragma unroll
            for (int c = 0; c < 6; ++c) acc[a][c] = (f32x4){0.f, 0.f, 0.f, 0.f};
        bf16x8 xf[2][4][2];
        s16x4 wlo[3][2], whi[3][2];
#define P1_RX(kt, tb, s) xf[(kt) & 1][tb][s] = ldsfrag(lds + ((kt) % 3) * P1_XS + xa[s] + (tb) * 2048)
#define P1_WL(kt, fb, s) wlo[(fb) % 3][s] = ldstr(lds + wb[(fb) & 1] + ((kt) & 1) * P1_WS + ((fb) >> 1) * 8192 + (s) * 4096)
#define P1_WH(kt, fb, s) whi[(fb) % 3][s] = ldstr(lds + wb[(fb) & 1] + ((kt) & 1) * P1_WS + ((fb) >> 1) * 8192 + (s) * 4096 + 512)
#define P1_M1(kt, tb, fb, s) do { const bf16x8 w_ = cat8(wlo[(fb) % 3][s], whi[(fb) % 3][s]); if ((fb) < 4) acc[tb][fb] = MFMA16(w_, xf[(kt) & 1][tb][s], acc[tb][fb]); else acc[tb][fb] = MFMA16(xf[(kt) & 1][tb][s], w_, acc[tb][fb]); } while (0)
#define P1_FB8(kt, fb, R0, R1, R2, R3, R4, R5, R6, R7) do { \
            P1_M1(kt, 0, fb, 0); R0; SBAR(); P1_M1(kt, 1, fb, 0); R1; SBAR(); P1_M1(kt, 2, fb, 0); R2; SBAR(); P1_M1(kt, 3, fb, 0); R3; SBAR(); \
            P1_M1(kt, 0, fb, 1); R4; SBAR(); P1_M1(kt, 1, fb, 1); R5; SBAR(); P1_M1(kt, 2, fb, 1); R6; SBAR(); P1_M1(kt, 3, fb, 1); R7; SBAR(); } while (0)
        WAITBAR(13);
        P1_RX(0, 0, 0); P1_WL(0, 0, 0); P1_WH(0, 0, 0); P1_RX(0, 1, 0); P1_RX(0, 2, 0); P1_RX(0, 3, 0); P1_WL(0, 0, 1); P1_WH(0, 0, 1);
        P1_RX(0, 0, 1); P1_RX(0, 1, 1); P1_RX(0, 2, 1); P1_RX(0, 3, 1); P1_WL(0, 1, 0); P1_WH(0, 1, 0); P1_WL(0, 1, 1); P1_WH(0, 1, 1); SBAR();
#pragma unroll
        for (int t = 0; t < 16; ++t) {
            const bool xs = (t >= 1 && t + 2 < 16);
            P1_FB8(t, 0, P1_WL(t, 2, 0), NOP_, P1_WH(t, 2, 0), NOP_, P1_WL(t, 2, 1), if (xs) dx(t + 2, 0), P1_WH(t, 2, 1), NOP_);
            P1_FB8(t, 1, P1_WL(t, 3, 0), NOP_, P1_WH(t, 3, 0), NOP_, P1_WL(t, 3, 1), if (xs) dx(t + 2, 1), P1_WH(t, 3, 1), NOP_);
            P1_FB8(t, 2, P1_WL(t, 4, 0), NOP_, P1_WH(t, 4, 0), NOP_, P1_WL(t, 4, 1), if (xs) dx(t + 2, 2), P1_WH(t, 4, 1), NOP_);
            P1_FB8(t, 3, P1_WL(t, 5, 0), NOP_, P1_WH(t, 5, 0), NOP_, P1_WL(t, 5, 1), if (xs) dx(t + 2, 3), P1_WH(t, 5, 1), NOP_);
            if (t < 15) {
                if (t == 0) WAITBAR(6); else if (t == 14) WAITBAR(0); else if (t - 1 + PFD < 16) WAITBAR(5); else WAITBAR(4);
                const bool wsch = (t + 2 < 16);
                P1_FB8(t, 4, P1_RX(t + 1, 0, 0), P1_WL(t + 1, 0, 0), P1_WH(t + 1, 0, 0), P1_RX(t + 1, 1, 0), P1_RX(t + 1, 2, 0), P1_RX(t + 1, 3, 0), P1_WL(t + 1, 0, 1), { P1_WH(t + 1, 0, 1); if (wsch) dw(t + 2, 0); });
                P1_FB8(t, 5, P1_RX(t + 1, 0, 1), P1_RX(t + 1, 1, 1), P1_RX(t + 1, 2, 1), { P1_RX(t + 1, 3, 1); if (wsch) dw(t + 2, 1); }, P1_WL(t + 1, 1, 0), { P1_WH(t + 1, 1, 0); if (wsch) dw(t + 2, 2); }, P1_WL(t + 1, 1, 1), { P1_WH(t + 1, 1, 1); if (t + PFD < 16) pf(t + PFD); });
            } else {
                P1_FB8(t, 4, NOP_, NOP_, NOP_, NOP_, NOP_, NOP_, NOP_, NOP_);
                P1_FB8(t, 5, NOP_, NOP_, NOP_, NOP_, NOP_, NOP_, NOP_, NOP_);
            }
        }
        lds_signal(lds0 + L_CNT + 32);
        auto swap16 = [](unsigned& x, unsigned& y) { auto r = __builtin_amdgcn_permlane16_swap(x, y, false, false); x = r[0]; y = r[1]; };
        auto swap32 = [](unsigned& x, unsigned& y) { auto r = __builtin_amdgcn_permlane32_swap(x, y, false, false); x = r[0]; y = r[1]; };
        auto qkfrag = [&](const f32x4& t0, const f32x4& t1, float mul) {
            unsigned a0 = cvtpk(t0[0] * mul, t0[1] * mul), a1 = cvtpk(t0[2] * mul, t0[3] * mul), b0 = cvtpk(t1[0] * mul, t1[1] * mul), b1 = cvtpk(t1[2] * mul, t1[3] * mul);
            swap16(a0, b0); swap16(a1, b1);
            return (u32x4){a0, a1, b0, b1}; };
        auto vfrag = [&](const f32x4& x0, const f32x4& x1) {
            unsigned a0 = cvtpk(x0[0], x0[1]), a1 = cvtpk(x0[2], x0[3]), b0 = cvtpk(x1[0], x1[1]), b1 = cvtpk(x1[2], x1[3]);
            swap16(a0, b0); swap16(a1, b1); swap32(a0, b0); swap32(a1, b1);
            return (u32x4){a0, a1, b0, b1}; };
#pragma unroll
        for (int a = 0; a < 2; ++a)
#pragma unroll
            for (int s = 0; s < 2; ++s) {
                kvp[a][s] = qkfrag(acc[2 * a][2 + s], acc[2 * a + 1][2 + s], 1.0f); kvp[a][2 + s] = vfrag(acc[2 * a + s][4], acc[2 * a + s][5]);
                __builtin_amdgcn_raw_buffer_store_b128(kvp[a][s], skvw, lane * 16 + a * 4096 + s * 1024, 0, 16);
                __builtin_amdgcn_raw_buffer_store_b128(kvp[a][2 + s], skvw, lane * 16 + a * 4096 + 2048 + s * 1024, 0, 16);
            }
#pragma unroll
        for (int a = 0; a < 2; ++a) {
            const int tl = wm * 2 + a;
            LAS char* lp = lds + L_S0 + (tl >> 2) * 32768 + wn * 16384 + (tl & 3) * 4096 + lane * 16;
#pragma unroll
            for (int f = 0; f < 4; ++f) *(LAS u32x4*)(lp + f * 1024) = kvp[a][f];
        }
        SBAR();
#pragma unroll
        for (int a = 0; a < 2; ++a)
#pragma unroll
            for (int s = 0; s < 2; ++s) qf[a][s] = BF(qkfrag(acc[2 * a][s], acc[2 * a + 1][s], QSCALE));
    }
    asm volatile("" : "+v"(epoch));
    const u64 tag = mix64(((u64)rfl((unsigned)(epoch >> 32)) << 32) | rfl((unsigned)epoch));

    const srd_t skv = make_srd(ws + WS_KV + (size_t)(b * 32 + hp * 2) * 64 * 4096, 2 * 64 * 4096);
    const srd_t swo = make_srd(b ? woB : woA, 1024 * 1024 * 2);
    const int tm = (xcd & 3) * 4 + (j >> 3), tn = j & 7;
    const unsigned vl = lane * 16;
    const unsigned vwo = (lane >> 4) * 2048 + (((lane & 15) ^ (((lane >> 4) & 3) << 2)) << 4);
    auto chunk_off = [&](int c) { const int i = (c - 2) >> 1; const int blk = i < mb ? i : i + 1; return (unsigned)((wn * 64 + blk * 8 + (c & 1) * 4 + wm) * 4096); };
    const unsigned ldw = lds0 + wid * 4096;
#define DKV(coff, slotoff, i) dma4c<i>(ldw + (slotoff), vl, skv, coff)
    const unsigned wo0 = wm * 8 * 2048 + tn * 256;
    auto dwu = [&](int head, unsigned slotaddr, int p) { dma16(lds0 + slotaddr + wm * 2048 + p * 1024, vwo, swo, wo0 + (head * 32 + p * 4) * 2048); };
    auto wo_rs = [&](int k) { return (unsigned)(L_R0 + wn * 24576 + k * 8192); };
    auto wo_hs = [&](int k) { return (unsigned)((k >> 1 == 0 ? L_S0 : k >> 1 == 1 ? L_S1 : L_S2) + wn * 16384 + (k & 1) * 8192); };
    bf16x8 sel; int selidx;
    { const int i16 = lane & 15; const short lo = (i16 < 2) ? (short)0x3F80 : (short)0, hi = (i16 == 2 || i16 == 3) ? (short)0x3F80 : (short)0;
      s16x8 t = {lo, lo, lo, lo, hi, hi, hi, hi}; sel = __builtin_bit_cast(bf16x8, t); selidx = (i16 & 1) ? 0xEEEE : 0x4444; }
    const LAS char* kvb = lds + wn * 16384 + lane * 16;
    GAS u64* const kvfl = flags + (FL_KV / 8) + (b * 32 + h) * 32;
    const GAS u64* const kvmine = kvfl + ((lane >> 1) & 7) * 4 + (lane & 1) * 2 + (wm >> 1);
    const LAS char* cntp = lds + L_CNT + wn * 16;
    const unsigned cnta = lds0 + L_CNT + wn * 16;
    f32x16 o[2]; f32x4 lacc[2];
    {
        f32x16 negm[2], S[2];
        bf16x8 kf[2][2], vf[2][2];
        u32x8 P;
        kf[0][0] = BF(kvp[0][0]); kf[0][1] = BF(kvp[0][1]); vf[0][0] = BF(kvp[0][2]); vf[0][1] = BF(kvp[0][3]);
#pragma unroll
        for (int a = 0; a < 2; ++a) {
            f32x16 z;
#pragma unroll
            for (int i = 0; i < 16; ++i) z[i] = 0.f;
            z = MFMA32(kf[0][0], qf[a][0], z); z = MFMA32(kf[0][1], qf[a][1], z);
            float m = z[0];
#pragma unroll
            for (int i = 1; i < 16; ++i) m = fmaxf(m, z[i]);
            m = halfmax(m);
#pragma unroll
            for (int i = 0; i < 16; ++i) { negm[a][i] = -m; o[a][i] = 0.f; }
            lacc[a] = (f32x4){0.f, 0.f, 0.f, 0.f};
        }
        S[0] = MFMA32(kf[0][0], qf[0][0], negm[0]); S[0] = MFMA32(kf[0][1], qf[0][1], S[0]);
        P = (u32x8){0u, 0u, 0u, 0u, 0u, 0u, 0u, 0u};
        bf16x8 vprev1 = vf[0][1];
        unsigned seen;
#define EXPI(X, i) X[i] = ex2(X[i])
#define UNIT(SX, SN, KA0, KA1, QB0, QB1, NEG, DOQK, OPREV, LPREV, VPREV1, OCUR, VCUR0, H0, H1, H2, H3) do { \
            if (DOQK) SN = MFMA32(KA0, QB0, NEG); H0; SBAR(); \
            EXPI(SX, 0); EXPI(SX, 1); EXPI(SX, 2); SBAR(); \
            LPREV = SMF(sel, P, LPREV, selidx); SBAR(); \
            EXPI(SX, 3); P[0] = cvtpk(SX[0], SX[1]); P[1] = cvtpk(SX[2], SX[3]); SBAR(); \
            OPREV = MFMA32(VPREV1, PHI(P), OPREV); H1; SBAR(); \
            EXPI(SX, 4); EXPI(SX, 5); EXPI(SX, 6); EXPI(SX, 7); P[2] = cvtpk(SX[4], SX[5]); P[3] = cvtpk(SX[6], SX[7]); SBAR(); \
            if (DOQK) SN = MFMA32(KA1, QB1, SN); H2; SBAR(); \
            EXPI(SX, 8); EXPI(SX, 9); EXPI(SX, 10); EXPI(SX, 11); P[4] = cvtpk(SX[8], SX[9]); P[5] = cvtpk(SX[10], SX[11]); SBAR(); \
            OCUR = MFMA32(VCUR0, PLO(P), OCUR); H3; SBAR(); \
            EXPI(SX, 12); EXPI(SX, 13); EXPI(SX, 14); EXPI(SX, 15); P[6] = cvtpk(SX[12], SX[13]); P[7] = cvtpk(SX[14], SX[15]); SBAR(); \
        } while (0)
#define TILEG(cur, nxt, LK, LV, LOAD, A2, A3, B0, B1, B2, B3) do { \
            UNIT(S[0], S[1], kf[cur][0], kf[cur][1], qf[1][0], qf[1][1], negm[1], true, o[1], lacc[1], vprev1, o[0], vf[cur][0], LK, LV, A2, A3); \
            UNIT(S[1], S[0], kf[nxt][0], kf[nxt][1], qf[0][0], qf[0][1], negm[0], LOAD, o[0], lacc[0], vf[cur][1], o[1], vf[cur][0], B0, B1, B2, B3); \
            vprev1 = vf[cur][1]; \
        } while (0)
#define TILEX(cur, nxt, rdp, LOAD, A2, A3, B0, B1, B2, B3) TILEG(cur, nxt, \
            if (LOAD) { kf[nxt][0] = ldsfrag(rdp); kf[nxt][1] = ldsfrag((rdp) + 1024); }, if (LOAD) { vf[nxt][0] = ldsfrag((rdp) + 2048); vf[nxt][1] = ldsfrag((rdp) + 3072); }, \
            LOAD, A2, A3, B0, B1, B2, B3)
#define TILE(cur, nxt, rdp, PK) TILEX(cur, nxt, rdp, true, NOP_, NOP_, NOP_, PK, NOP_, NOP_)
#define TILED(cur, nxt, rdp, coff, slot) TILEX(cur, nxt, rdp, true, DKV(coff, slot, 0), DKV(coff, slot, 1), NOP_, NOP_, DKV(coff, slot, 2), DKV(coff, slot, 3))
#define PEEK seen = lds_peek(cntp)
#define SIGNAL() lds_signal(cnta)
#define BALANCE(m) do { if (wn == 0 && (m) == FLIP) __builtin_amdgcn_s_setprio(0); } while (0)
        if (wn == 0) __builtin_amdgcn_s_setprio(2); else __builtin_amdgcn_s_setprio(1);
        auto own = [&](int p) {
            const int lo = wm < 2;
            const int off = lo ? (p < 4 ? L_S0 + ((1 - wm) * 2 + p - 2) * 4096 : L_S1 + (p - 4) * 4096) : (p < 6 ? L_S0 + (p - 2) * 4096 : L_S1 + ((3 - wm) * 2 + p - 6) * 4096);
            return kvb + off; };
        TILEG(0, 1, { kf[1][0] = BF(kvp[1][0]); kf[1][1] = BF(kvp[1][1]); }, { vf[1][0] = BF(kvp[1][2]); vf[1][1] = BF(kvp[1][3]); }, true, NOP_, NOP_, NOP_, NOP_, NOP_, NOP_);
        lds_signal(cnta + 4); lds_await(0, cntp + 4, 4);
        TILE(1, 0, own(2), NOP_);
        TILE(0, 1, own(3), NOP_);
        WAITV(0); if (lane == 0) st_agent(kvfl + mb * 4 + wm, tag);
        TILE(1, 0, own(4), NOP_);
        u64 pollv = ld_agent(kvmine);
        TILE(0, 1, own(5), NOP_);
        if (!__all(pollv == tag)) spin_flags(kvmine, tag);
        SIGNAL();
        lds_await(0, lds + L_CNT + 32, 8);
        unsigned cn = chunk_off(2);
        TILEX(1, 0, own(6), true, DKV(cn, L_S2, 0), DKV(cn, L_S2, 1), NOP_, PEEK, DKV(cn, L_S2, 2), DKV(cn, L_S2, 3));
        lds_await(seen, cntp, 4);
        cn = chunk_off(3);
        TILED(0, 1, own(7), cn, L_S0);
        WAITV(4); SIGNAL(); lds_await(0, cntp, 8);
        TILE(1, 0, kvb + L_S2, NOP_);
        cn = chunk_off(4);
        TILED(0, 1, kvb + L_S2 + 4096, cn, L_S1);
        WAITV(4); SIGNAL();
        TILE(1, 0, kvb + L_S2 + 8192, NOP_);
        TILE(0, 1, kvb + L_S2 + 12288, PEEK);
        lds_await(seen, cntp, 12);
        WAITV(0); SIGNAL();
        TILE(1, 0, kvb + L_S0, PEEK);
        int sc = L_S0, sn = L_S1, sp = L_S2;
        for (int m = 3; m <= 13; ++m) {
            const LAS char* cp = kvb + sc;
            cn = chunk_off(m + 2);
            lds_await(seen, cntp, 4 * (m + 1));
            BALANCE(4 * m);
            TILED(0, 1, cp + 4096, cn, sp);
            TILE(1, 0, cp + 8192, NOP_);
            TILE(0, 1, cp + 12288, NOP_);
            WAITV(0); SIGNAL();
            TILE(1, 0, kvb + sn, PEEK);
            const int t_ = sc; sc = sn; sn = sp; sp = t_;
        }
        lds_await(seen, cntp, 60);
        BALANCE(56);
        TILEX(0, 1, kvb + L_S2 + 4096, true, dwu(wn, wo_rs(0), 0), dwu(wn, wo_rs(0), 1), NOP_, NOP_, NOP_, NOP_);
        BALANCE(57);
        TILEX(1, 0, kvb + L_S2 + 8192, true, dwu(2 + wn, wo_rs(1), 0), dwu(2 + wn, wo_rs(1), 1), NOP_, NOP_, NOP_, NOP_);
        BALANCE(58);
        TILEX(0, 1, kvb + L_S2 + 12288, true, dwu(4 + wn, wo_rs(2), 0), dwu(4 + wn, wo_rs(2), 1), NOP_, NOP_, NOP_, NOP_);
        BALANCE(59);
        TILE(1, 0, kvb + L_S0, NOP_);
        BALANCE(60);
        TILE(0, 1, kvb + L_S0 + 4096, NOP_);
        BALANCE(61);
        TILE(1, 0, kvb + L_S0 + 8192, NOP_);
        BALANCE(62);
        TILE(0, 1, kvb + L_S0 + 12288, NOP_);
        TILEX(1, 0, kvb, false, NOP_, NOP_, NOP_, NOP_, NOP_, NOP_);
        lacc[1] = SMF(sel, P, lacc[1], selidx); o[1] = MFMA32(vprev1, PHI(P), o[1]);
    }
    if (wn == 0) __builtin_amdgcn_s_setprio(0);
    {
        float l[2];
        auto rowsum = [&](int a) { const float t0 = __shfl(lacc[a][0] + lacc[a][1], lane & 15), t1 = __shfl(lacc[a][2] + lacc[a][3], lane & 15); return (lane & 16) ? t1 : t0; };
        bool bad = false;
#pragma unroll
        for (int a = 0; a < 2; ++a) {
            l[a] = rowsum(a);
            float z = l[a] * 0.f, z1 = 0.f;
#pragma unroll
            for (int i = 0; i < 16; i += 2) { z = __builtin_fmaf(o[a][i], 0.f, z); z1 = __builtin_fmaf(o[a][i + 1], 0.f, z1); }
            bad |= !(l[a] < 1.0e36f) || !(z + z1 == 0.f);
        }
        if (__any(bad)) {
            spin_flags(kvfl + (lane & 31), tag);
            const srd_t sk1 = make_srd(ws + WS_KV + (size_t)(b * 32 + h) * 64 * 4096, 64 * 4096);
#define LDKV(t, f) BF(__builtin_amdgcn_raw_buffer_load_b128(sk1, lane * 16 + (f) * 1024, (t) * 4096, 16))
            f32x16 negm[2];
#pragma unroll
            for (int a = 0; a < 2; ++a) {
                float m = -__builtin_inff();
                for (int t = 0; t < 64; ++t) {
                    const bf16x8 k0 = LDKV(t, 0), k1 = LDKV(t, 1);
                    f32x16 z;
#pragma unroll
                    for (int i = 0; i < 16; ++i) z[i] = 0.f;
                    z = MFMA32(k0, qf[a][0], z); z = MFMA32(k1, qf[a][1], z);
#pragma unroll
                    for (int i = 0; i < 16; ++i) m = fmaxf(m, z[i]);
                }
                m = halfmax(m);
#pragma unroll
                for (int i = 0; i < 16; ++i) { negm[a][i] = -m; o[a][i] = 0.f; }
                lacc[a] = (f32x4){0.f, 0.f, 0.f, 0.f};
            }
            for (int t = 0; t < 64; ++t) {
                const bf16x8 k0 = LDKV(t, 0), k1 = LDKV(t, 1), v0 = LDKV(t, 2), v1 = LDKV(t, 3);
#pragma unroll
                for (int a = 0; a < 2; ++a) {
                    f32x16 z = MFMA32(k0, qf[a][0], negm[a]); z = MFMA32(k1, qf[a][1], z);
                    u32x8 P;
#pragma unroll
                    for (int i = 0; i < 8; ++i) P[i] = cvtpk(ex2(z[2 * i]), ex2(z[2 * i + 1]));
                    o[a] = MFMA32(v0, PLO(P), o[a]); o[a] = MFMA32(v1, PHI(P), o[a]); lacc[a] = SMF(sel, P, lacc[a], selidx);
                }
            }
            l[0] = rowsum(0); l[1] = rowsum(1);
        }
        const srd_t sow = make_srd(ws + WS_O + (size_t)(b * 64 + mb * 8 + wm * 2) * 65536, 2 * 65536);
#pragma unroll
        for (int a = 0; a < 2; ++a) {
            const float rl = __builtin_amdgcn_rcpf(l[a]);
            __builtin_amdgcn_raw_buffer_store_b128(pack8(o[a], 0, rl), sow, lane * 16 + a * 65536 + h * 2048, 0, 16);
            __builtin_amdgcn_raw_buffer_store_b128(pack8(o[a], 1, rl), sow, lane * 16 + a * 65536 + h * 2048 + 1024, 0, 16);
        }
    }
    GAS u64* const ofl = flags + (FL_O / 8) + b * 1024;
    WAITV(0); if (lane == 0) st_agent(ofl + (mb * 4 + wm) * 32 + h, tag);

    {
        const int wt = wm;
        unsigned seen;
        SIGNAL(); lds_await(0, cntp, 64);
        for (int i = 0; i < 6; ++i) { dwu(2 * (3 + i) + wn, wo_hs(i), 0); dwu(2 * (3 + i) + wn, wo_hs(i), 1); }
        spin_flags(ofl + (tm * 2 + (wt >> 1)) * 32 + (lane & 15) * 2 + wn, tag);
        const srd_t sor = make_srd(ws + WS_O + (size_t)(b * 64 + tm * 4 + wt) * 65536 + wn * 2048, 65536 - 2048);
        bf16x8 of[8][2], wf[3][4];
#define P3_LO(u) do { of[(u) & 7][0] = BF(__builtin_amdgcn_raw_buffer_load_b128(sor, lane * 16, (u) * 4096, 16)); \
                      of[(u) & 7][1] = BF(__builtin_amdgcn_raw_buffer_load_b128(sor, lane * 16, (u) * 4096 + 1024, 16)); } while (0)
        P3_LO(0); P3_LO(1); P3_LO(2); P3_LO(3); P3_LO(4); P3_LO(5); P3_LO(6); P3_LO(7);
        SIGNAL();
        unsigned wn3[4];
#pragma unroll
        for (int n = 0; n < 4; ++n) wn3[n] = (4 * (g4 >> 1) + q4) * 256 + ((n ^ q4) << 6) + (g4 & 1) * 32 + p4 * 8;
        f32x16 acc[4];
#pragma unroll
        for (int n = 0; n < 4; ++n)
#pragma unroll
            for (int i = 0; i < 16; ++i) acc[n][i] = 0.f;
        auto uslot = [&](int u) { return u < 3 ? wo_rs(u) : u < 9 ? wo_hs(u - 3) : u < 12 ? wo_rs(u - 9) : wo_hs(u - 12); };
#define P3_RW(kk, n) do { const LAS char* w_ = lds + uslot((kk) >> 1) + ((kk) & 1) * 4096 + wn3[n]; wf[(kk) % 3][n] = cat8(ldstr(w_), ldstr(w_ + 2048)); } while (0)
#define P3_KS(kk, M0, M1) do { \
            acc[0] = MFMA32(wf[(kk) % 3][0], of[((kk) >> 1) & 7][(kk) & 1], acc[0]); if ((kk) + 2 < 32) P3_RW((kk) + 2, 0); SBAR(); \
            acc[1] = MFMA32(wf[(kk) % 3][1], of[((kk) >> 1) & 7][(kk) & 1], acc[1]); if ((kk) + 2 < 32) P3_RW((kk) + 2, 1); M0; SBAR(); \
            acc[2] = MFMA32(wf[(kk) % 3][2], of[((kk) >> 1) & 7][(kk) & 1], acc[2]); if ((kk) + 2 < 32) P3_RW((kk) + 2, 2); SBAR(); \
            acc[3] = MFMA32(wf[(kk) % 3][3], of[((kk) >> 1) & 7][(kk) & 1], acc[3]); if ((kk) + 2 < 32) P3_RW((kk) + 2, 3); M1; SBAR(); \
        } while (0)
#define P3_UNIT(u, M0) do { P3_KS(2 * (u), M0, NOP_); P3_KS(2 * (u) + 1, NOP_, if ((u) + 8 < 16) P3_LO((u) + 8)); } while (0)
#define DWU(u, slot) do { dwu(2 * (u) + wn, slot, 0); dwu(2 * (u) + wn, slot, 1); } while (0)
        P3_RW(0, 0); P3_RW(0, 1); P3_RW(0, 2); P3_RW(0, 3); P3_RW(1, 0); P3_RW(1, 1); P3_RW(1, 2); P3_RW(1, 3); SBAR();
        P3_UNIT(0, NOP_);
        P3_UNIT(1, PEEK);
        lds_await(seen, cntp, 68);
        SIGNAL();
        P3_UNIT(2, PEEK);
        lds_await(seen, cntp, 72); DWU(9, wo_rs(0)); DWU(10, wo_rs(1)); DWU(11, wo_rs(2));
        P3_UNIT(3, NOP_);
        P3_UNIT(4, NOP_);
        SIGNAL();
        P3_UNIT(5, PEEK);
        lds_await(seen, cntp, 76); DWU(12, wo_hs(0)); DWU(13, wo_hs(1)); DWU(14, wo_hs(2));
        P3_UNIT(6, NOP_);
        WAITV(14); SIGNAL();
        P3_UNIT(7, PEEK);
        lds_await(seen, cntp, 80);
        SIGNAL();
        P3_UNIT(8, PEEK);
        lds_await(seen, cntp, 84); DWU(15, wo_hs(3));
        P3_UNIT(9, NOP_);
        WAITV(6); SIGNAL();
        P3_UNIT(10, PEEK);
        lds_await(seen, cntp, 88);
        P3_UNIT(11, NOP_);
        P3_UNIT(12, NOP_);
        WAITV(0); SIGNAL();
        P3_UNIT(13, PEEK);
        lds_await(seen, cntp, 92);
        P3_UNIT(14, NOP_);
        P3_UNIT(15, NOP_);
        const int xoff = (wm < 3 ? (wm == 0 ? L_S0 : wm == 1 ? L_S1 : L_S2) : L_R0) + lane * 16;
        if (wn == 0) {
            SIGNAL(); lds_await(0, cntp, 96);
#pragma unroll
            for (int n = 0; n < 4; ++n)
#pragma unroll
                for (int qd = 0; qd < 4; ++qd)
                    *(LAS f32x4*)(lds + xoff + n * 4096 + qd * 1024) = (f32x4){acc[n][4 * qd], acc[n][4 * qd + 1], acc[n][4 * qd + 2], acc[n][4 * qd + 3]};
            SIGNAL();
        } else {
            lds_await(0, lds + L_CNT, 100);
            if (tid == 256 && (xcd & 3) == 0 && j == 0) st_agent(flags + b * 32, tag);
            const srd_t sout = make_srd(out, 2048 * 2048 * 2);
            LAS char* stg = lds + (wm < 3 ? wo_rs(wm) : wo_hs(0));
#pragma unroll
            for (int n = 0; n < 4; ++n) {
                f32x16 v = acc[n];
#pragma unroll
                for (int qd = 0; qd < 4; ++qd) {
                    const f32x4 t = *(const LAS f32x4*)(lds + xoff + n * 4096 + qd * 1024);
                    v[4 * qd] += t[0]; v[4 * qd + 1] += t[1]; v[4 * qd + 2] += t[2]; v[4 * qd + 3] += t[3];
                }
#pragma unroll
                for (int gp = 0; gp < 2; ++gp) {
                    const unsigned a0 = cvtpk(v[8 * gp], v[8 * gp + 1]), a1 = cvtpk(v[8 * gp + 2], v[8 * gp + 3]);
                    const unsigned b0 = cvtpk(v[8 * gp + 4], v[8 * gp + 5]), b1 = cvtpk(v[8 * gp + 6], v[8 * gp + 7]);
                    auto r0 = __builtin_amdgcn_permlane32_swap(a0, b0, false, false);
                    auto r1 = __builtin_amdgcn_permlane32_swap(a1, b1, false, false);
                    const u32x4 w = {r0[0], r1[0], r0[1], r1[1]};
                    *(LAS u32x4*)(stg + r32 * 256 + (((n * 4 + gp * 2 + hh) ^ (r32 & 15)) << 4)) = w;
                }
            }
            const unsigned oo = (unsigned)(tm * 128 + wt * 32 + g4) * 4096u + (unsigned)(b * 1024 + tn * 128) * 2u + (lane & 15) * 16;
#pragma unroll
            for (int i = 0; i < 8; ++i) {
                const int row = 4 * i + g4;
                const u32x4 w = *(const LAS u32x4*)(stg + row * 256 + (((lane & 15) ^ (row & 15)) << 4));
                __builtin_amdgcn_raw_buffer_store_b128(w, sout, oo + i * 4 * 4096, 0, 16);
            }
        }
    }
}

extern "C" void kernel_launch(void* const* d_in, const int* in_sizes, int n_in, void* d_out, int out_size, void* d_ws, size_t ws_size, hipStream_t stream) {
    (void)hipFuncSetAttribute((const void*)mha_fused, hipFuncAttributeMaxDynamicSharedMemorySize, L_END);
    mha_fused<<<256, 512, L_END, stream>>>((const uint16_t*)d_in[0], (const uint16_t*)d_in[1], (const uint16_t*)d_in[2], (const uint16_t*)d_in[3], (const uint16_t*)d_in[4],
                                           (const uint16_t*)d_in[5], (char*)d_ws, (uint16_t*)d_out);
}
```

```cpp
#define ROTX 4
#include <hip/hip_runtime.h>
#include <stdint.h>

typedef __bf16 bf16x8 __attribute__((ext_vector_type(8)));
typedef __bf16 bf16x16 __attribute__((ext_vector_type(16)));
typedef __bf16 bf16x2 __attribute__((ext_vector_type(2)));
typedef short s16x4 __attribute__((ext_vector_type(4)));
typedef short s16x8 __attribute__((ext_vector_type(8)));
typedef float f32x16 __attribute__((ext_vector_type(16)));
typedef float f32x4 __attribute__((ext_vector_type(4)));
typedef float f32x2 __attribute__((ext_vector_type(2)));
typedef unsigned u32x4 __attribute__((ext_vector_type(4)));
typedef unsigned u32x8 __attribute__((ext_vector_type(8)));
typedef unsigned long long u64;
#define LAS __attribute__((address_space(3)))
#define GAS __attribute__((address_space(1)))
#define DI __device__ __forceinline__
#define MFMA32(a, b, c) __builtin_amdgcn_mfma_f32_32x32x16_bf16((a), (b), (c), 0, 0, 0)
#define MFMA16(a, b, c) __builtin_amdgcn_mfma_f32_16x16x32_bf16((a), (b), (c), 0, 0, 0)
#define SMF(a, b, c, idx) __builtin_amdgcn_smfmac_f32_16x16x64_bf16((a), __builtin_bit_cast(bf16x16, b), (c), (idx), 0, 0)
#define PLO(P) __builtin_bit_cast(bf16x8, __builtin_shufflevector(P, P, 0, 1, 2, 3))
#define PHI(P) __builtin_bit_cast(bf16x8, __builtin_shufflevector(P, P, 4, 5, 6, 7))
#define SBAR() __builtin_amdgcn_sched_barrier(0)
#define BF(x) __builtin_bit_cast(bf16x8, x)
#define NOP_ (void)0
typedef __amdgpu_buffer_rsrc_t srd_t;

constexpr float QSCALE = 0.17677669529663687f * 1.4426950408889634f;
constexpr unsigned WS_KV = 0;
constexpr unsigned WS_O = 16u << 20;
constexpr unsigned WS_FL = 24u << 20;
constexpr unsigned FL_KV = 4096;
constexpr unsigned FL_O = FL_KV + 16384;
constexpr int L_S0 = 32768, L_S1 = 65536, L_S2 = 0, L_R0 = 98304, L_END = 163840, L_CNT = L_END - 64, L_DUMMY = 147456;
constexpr int P1_XS = 32768, P1_WS = 24576, P1_WOFF = 98304;

DI unsigned rfl(unsigned x) { return __builtin_amdgcn_readfirstlane(x); }
DI srd_t make_srd(const void* p, unsigned bytes) { return __builtin_amdgcn_make_buffer_rsrc((void*)p, (short)0, (int)bytes, 0x00020000); }
DI void dma16(unsigned ldsdst, unsigned voff, srd_t srd, unsigned soff) {
    asm volatile("s_mov_b32 m0, %0\n\ts_nop 0\n\tbuffer_load_dwordx4 %1, %2, %3 offen lds" :: "s"(ldsdst), "v"(voff), "s"(srd), "s"(soff) : "memory");
}
template <int I> DI void dma4c(unsigned ldsdst, unsigned voff, srd_t srd, unsigned soff) {
    if constexpr (I == 0) asm volatile("s_mov_b32 m0, %0\n\ts_nop 0\n\tbuffer_load_dwordx4 %1, %2, %3 offen sc1 lds" :: "s"(ldsdst), "v"(voff), "s"(srd), "s"(soff) : "memory");
    else asm volatile("buffer_load_dwordx4 %0, %1, %2 offen offset:%3 sc1 lds" :: "v"(voff), "s"(srd), "s"(soff), "i"(I * 1024) : "memory");
}
DI unsigned cvtpk(float lo, float hi) { f32x2 v = {lo, hi}; bf16x2 b = __builtin_convertvector(v, bf16x2); return __builtin_bit_cast(unsigned, b); }
DI bf16x8 ldsfrag(const LAS char* p) { return *(const LAS bf16x8*)p; }
typedef short v4i16_t __attribute__((ext_vector_type(4)));
DI s16x4 ldstr(const LAS char* p) { return __builtin_amdgcn_ds_read_tr16_b64_v4i16((LAS v4i16_t*)p); }
DI bf16x8 cat8(s16x4 lo, s16x4 hi) { s16x8 r = __builtin_shufflevector(lo, hi, 0, 1, 2, 3, 4, 5, 6, 7); return __builtin_bit_cast(bf16x8, r); }
DI u32x4 pack8(const f32x16& x, int s, float mul) {
    u32x4 p; p[0] = cvtpk(x[8 * s] * mul, x[8 * s + 1] * mul); p[1] = cvtpk(x[8 * s + 2] * mul, x[8 * s + 3] * mul);
    p[2] = cvtpk(x[8 * s + 4] * mul, x[8 * s + 5] * mul); p[3] = cvtpk(x[8 * s + 6] * mul, x[8 * s + 7] * mul); return p; }
#define WAITBAR(vm) asm volatile("s_waitcnt vmcnt(" #vm ") lgkmcnt(0)\n\ts_barrier" ::: "memory")
#define WAITV(vm) asm volatile("s_waitcnt vmcnt(" #vm ")" ::: "memory")
DI float ex2(float x) { return __builtin_amdgcn_exp2f(x); }
DI float halfmax(float m) { auto r = __builtin_amdgcn_permlane32_swap(__float_as_uint(m), __float_as_uint(m), false, false); return fmaxf(__uint_as_float(r[0]), __uint_as_float(r[1])); }
DI u64 mix64(u64 z) { z += 0x9E3779B97F4A7C15ull; z = (z ^ (z >> 30)) * 0xBF58476D1CE4E5B9ull; z = (z ^ (z >> 27)) * 0x94D049BB133111EBull; return z ^ (z >> 31); }
DI u64 ld_agent(const GAS u64* p) { return __hip_atomic_load(p, __ATOMIC_RELAXED, __HIP_MEMORY_SCOPE_AGENT); }
DI void st_agent(GAS u64* p, u64 v) { __hip_atomic_store(p, v, __ATOMIC_RELAXED, __HIP_MEMORY_SCOPE_AGENT); }
DI void spin_flags(const GAS u64* f, u64 tag) {
    const u64 t0 = __builtin_amdgcn_s_memrealtime();
    u64 v0 = ld_agent(f);
    for (;;) {
        __builtin_amdgcn_s_sleep(6);
        const u64 v1 = ld_agent(f);
        if (__all(v0 == tag)) break;
        __builtin_amdgcn_s_sleep(6);
        v0 = ld_agent(f);
        if (__all(v1 == tag)) break;
        if (__builtin_amdgcn_s_memrealtime() - t0 > 20000000ull) break;
    }
    asm volatile("" ::: "memory");
}
DI void lds_signal(unsigned addr) {
    unsigned one = 1;
    asm volatile("s_waitcnt lgkmcnt(0)\n\ts_mov_b64 exec, 1\n\tds_add_u32 %0, %1\n\ts_mov_b64 exec, -1" :: "v"(addr), "v"(one) : "memory");
}
DI unsigned lds_peek(const LAS char* p) { return *(const volatile LAS unsigned*)p; }
DI void lds_await(unsigned seen, const LAS char* p, unsigned need) {
    if (rfl(seen) < need) {
        _Pragma("nounroll") for (int it = 0; it < (1 << 22); ++it) { if (rfl(lds_peek(p)) >= need) break; __builtin_amdgcn_s_sleep(0); }
    }
    asm volatile("" ::: "memory");
}

#define STAMP_BEGIN
#define STAMP_MID(i)
#define STAMP_END(ws)
#define FLIP 56

__global__ __launch_bounds__(512, 2) void mha_fused(const uint16_t* xA, const uint16_t* wA, const uint16_t* woA, const uint16_t* xB, const uint16_t* wB, const uint16_t* woB,
                                                    char* ws, uint16_t* out) {
    extern __shared__ __attribute__((aligned(16))) char lds_[];
    LAS char* lds = (LAS char*)lds_;
    STAMP_BEGIN
    const int tid = threadIdx.x, lane = tid & 63, wid = rfl(tid >> 6);
    const int bid = blockIdx.x, xcd = bid & 7, j = bid >> 3;
    const int b = xcd >> 2;
    const int mb = (xcd & 1) * 4 + (j & 3), hp = ((xcd >> 1) & 1) * 8 + (j >> 2);
    const int wm = wid & 3, wn = wid >> 2, h = hp * 2 + wn;
    const unsigned lds0 = (unsigned)(uintptr_t)lds_;
    const int r32 = lane & 31, hh = lane >> 5, g4 = lane >> 4, q4 = (lane >> 2) & 3, p4 = lane & 3;
    GAS u64* const flags = (GAS u64*)(ws + WS_FL);
    u64 epoch;
    if (tid < 16) *(LAS unsigned*)(lds + L_CNT + tid * 4) = 0u;

    bf16x8 qf[2][2];
    u32x4 kvp[2][4];
    const srd_t skvw = make_srd(ws + WS_KV + (size_t)(b * 32 + h) * 64 * 4096 + (size_t)(mb * 8 + wm * 2) * 4096, 2 * 4096);
    {
        const srd_t sx = make_srd(b ? xB : xA, 2048 * 1024 * 2), sw = make_srd(b ? wB : wA, 1024 * 3072 * 2);
        const unsigned vx0 = (lane >> 3) * 2048 + (((lane & 7) ^ (lane >> 4)) << 4);
        const unsigned vx1 = (lane >> 3) * 2048 + (((lane & 7) ^ (4 + (lane >> 4))) << 4);
        const unsigned vw = (lane >> 3) * 6144 + (((lane & 7) ^ (((lane >> 4) & 1) << 2) ^ ((wid & 1) << 1)) << 4);
        const unsigned sx0 = (mb * 256 + wid * 32) * 2048, sw0 = wid * 8 * 6144 + hp * 128;
        const int rot = (xcd & 3) * ROTX;
        auto dx = [&](int kt, int i) { dma16(lds0 + (kt % 3) * P1_XS + wid * 4096 + i * 1024, (i & 1) ? vx1 : vx0, sx, sx0 + i * 8 * 2048 + ((kt + rot) & 15) * 128); };
        auto dw = [&](int kt, int i) { dma16(lds0 + P1_WOFF + (kt & 1) * P1_WS + wid * 1024 + i * 8192, vw, sw, sw0 + i * 2048 + ((kt + rot) & 15) * 64 * 6144); };
        for (int i = 0; i < 3; ++i) dw(0, i);
        for (int i = 0; i < 4; ++i) dx(0, i);
        for (int i = 0; i < 3; ++i) dw(1, i);
        for (int i = 0; i < 4; ++i) dx(1, i);
        for (int i = 0; i < 4; ++i) dx(2, i);
        constexpr int PFD = 4;
        const char* pfp; unsigned pfstep;
        {
            const int l = lane < 10 ? lane : 9;
            const int xrow = (xcd & 1) * 1024 + j * 32 + wid * 4 + (l & 3), wl = wid * 6 + (l - 4);
            const size_t woff = (size_t)(j * 2 + wl / 24) * 6144 + ((wl % 24) >> 3) * 2048 + (((xcd >> 1) & 1) * 8 + (wl & 7)) * 128;
            pfp = l < 4 ? (const char*)(b ? xB : xA) + (size_t)xrow * 2048 : (const char*)(b ? wB : wA) + woff;
            pfstep = l < 4 ? 128u : 64u * 6144u;
        }
        const unsigned pfdst = lds0 + L_DUMMY + wid * 1024;
        auto pf = [&](int kt) {
            const char* g = pfp + (size_t)((kt + rot) & 15) * pfstep;
            asm volatile("s_mov_b32 m0, %0\n\ts_nop 0\n\tglobal_load_lds_dword %1, off" :: "s"(pfdst), "v"(g) : "memory"); };
        pf(3);
        epoch = ld_agent(flags + b * 32);
        const int r16 = lane & 15;
        unsigned xa[2], wb[2];
#pragma unroll
        for (int s = 0; s < 2; ++s) xa[s] = (wm * 64 + r16) * 128 + (((4 * s + g4) ^ (r16 >> 1)) << 4);
#pragma unroll
        for (int c = 0; c < 2; ++c) wb[c] = P1_WOFF + (8 * g4 + q4) * 128 + ((wn ^ (q4 >> 1)) << 6) + ((c ^ (g4 & 1)) << 5) + p4 * 8;
        f32x4 acc[4][6];
#pragma unroll
        for (int a = 0; a < 4; ++a)
#pragma unroll
            for (int c = 0; c < 6; ++c) acc[a][c] = (f32x4){0.f, 0.f, 0.f, 0.f};
        bf16x8 xf[2][4][2];
        s16x4 wlo[3][2], whi[3][2];
#define P1_RX(kt, tb, s) xf[(kt) & 1][tb][s] = ldsfrag(lds + ((kt) % 3) * P1_XS + xa[s] + (tb) * 2048)
#define P1_WL(kt, fb, s) wlo[(fb) % 3][s] = ldstr(lds + wb[(fb) & 1] + ((kt) & 1) * P1_WS + ((fb) >> 1) * 8192 + (s) * 4096)
#define P1_WH(kt, fb, s) whi[(fb) % 3][s] = ldstr(lds + wb[(fb) & 1] + ((kt) & 1) * P1_WS + ((fb) >> 1) * 8192 + (s) * 4096 + 512)
#define P1_M1(kt, tb, fb, s) do { const bf16x8 w_ = cat8(wlo[(fb) % 3][s], whi[(fb) % 3][s]); if ((fb) < 4) acc[tb][fb] = MFMA16(w_, xf[(kt) & 1][tb][s], acc[tb][fb]); else acc[tb][fb] = MFMA16(xf[(kt) & 1][tb][s], w_, acc[tb][fb]); } while (0)
#define P1_FB8(kt, fb, R0, R1, R2, R3, R4, R5, R6, R7) do { \
            P1_M1(kt, 0, fb, 0); R0; SBAR(); P1_M1(kt, 1, fb, 0); R1; SBAR(); P1_M1(kt, 2, fb, 0); R2; SBAR(); P1_M1(kt, 3, fb, 0); R3; SBAR(); \
            P1_M1(kt, 0, fb, 1); R4; SBAR(); P1_M1(kt, 1, fb, 1); R5; SBAR(); P1_M1(kt, 2, fb, 1); R6; SBAR(); P1_M1(kt, 3, fb, 1); R7; SBAR(); } while (0)
        WAITBAR(13);
        STAMP_MID(0)
        P1_RX(0, 0, 0); P1_WL(0, 0, 0); P1_WH(0, 0, 0); P1_RX(0, 1, 0); P1_RX(0, 2, 0); P1_RX(0, 3, 0); P1_WL(0, 0, 1); P1_WH(0, 0, 1);
        P1_RX(0, 0, 1); P1_RX(0, 1, 1); P1_RX(0, 2, 1); P1_RX(0, 3, 1); P1_WL(0, 1, 0); P1_WH(0, 1, 0); P1_WL(0, 1, 1); P1_WH(0, 1, 1); SBAR();
#pragma unroll
        for (int t = 0; t < 16; ++t) {
            const bool xs = (t >= 1 && t + 2 < 16);
            P1_FB8(t, 0, P1_WL(t, 2, 0), NOP_, P1_WH(t, 2, 0), NOP_, P1_WL(t, 2, 1), if (xs) dx(t + 2, 0), P1_WH(t, 2, 1), NOP_);
            P1_FB8(t, 1, P1_WL(t, 3, 0), NOP_, P1_WH(t, 3, 0), NOP_, P1_WL(t, 3, 1), if (xs) dx(t + 2, 1), P1_WH(t, 3, 1), NOP_);
            P1_FB8(t, 2, P1_WL(t, 4, 0), NOP_, P1_WH(t, 4, 0), NOP_, P1_WL(t, 4, 1), if (xs) dx(t + 2, 2), P1_WH(t, 4, 1), NOP_);
            P1_FB8(t, 3, P1_WL(t, 5, 0), NOP_, P1_WH(t, 5, 0), NOP_, P1_WL(t, 5, 1), if (xs) dx(t + 2, 3), P1_WH(t, 5, 1), NOP_);
            if (t < 15) {
                if (t == 0) WAITBAR(6); else if (t == 14) WAITBAR(0); else if (t - 1 + PFD < 16) WAITBAR(5); else WAITBAR(4);
                const bool wsch = (t + 2 < 16);
                P1_FB8(t, 4, P1_RX(t + 1, 0, 0), P1_WL(t + 1, 0, 0), P1_WH(t + 1, 0, 0), P1_RX(t + 1, 1, 0), P1_RX(t + 1, 2, 0), P1_RX(t + 1, 3, 0), P1_WL(t + 1, 0, 1), { P1_WH(t + 1, 0, 1); if (wsch) dw(t + 2, 0); });
                P1_FB8(t, 5, P1_RX(t + 1, 0, 1), P1_RX(t + 1, 1, 1), P1_RX(t + 1, 2, 1), { P1_RX(t + 1, 3, 1); if (wsch) dw(t + 2, 1); }, P1_WL(t + 1, 1, 0), { P1_WH(t + 1, 1, 0); if (wsch) dw(t + 2, 2); }, P1_WL(t + 1, 1, 1), { P1_WH(t + 1, 1, 1); if (t + PFD < 16) pf(t + PFD); });
            } else {
                P1_FB8(t, 4, NOP_, NOP_, NOP_, NOP_, NOP_, NOP_, NOP_, NOP_);
                P1_FB8(t, 5, NOP_, NOP_, NOP_, NOP_, NOP_, NOP_, NOP_, NOP_);
            }
        }
        lds_signal(lds0 + L_CNT + 32);
        STAMP_MID(1)
        auto swap16 = [](unsigned& x, unsigned& y) { auto r = __builtin_amdgcn_permlane16_swap(x, y, false, false); x = r[0]; y = r[1]; };
        auto swap32 = [](unsigned& x, unsigned& y) { auto r = __builtin_amdgcn_permlane32_swap(x, y, false, false); x = r[0]; y = r[1]; };
        auto qkfrag = [&](const f32x4& t0, const f32x4& t1, float mul) {
            unsigned a0 = cvtpk(t0[0] * mul, t0[1] * mul), a1 = cvtpk(t0[2] * mul, t0[3] * mul), b0 = cvtpk(t1[0] * mul, t1[1] * mul), b1 = cvtpk(t1[2] * mul, t1[3] * mul);
            swap16(a0, b0); swap16(a1, b1);
            return (u32x4){a0, a1, b0, b1}; };
        auto vfrag = [&](const f32x4& x0, const f32x4& x1) {
            unsigned a0 = cvtpk(x0[0], x0[1]), a1 = cvtpk(x0[2], x0[3]), b0 = cvtpk(x1[0], x1[1]), b1 = cvtpk(x1[2], x1[3]);
            swap16(a0, b0); swap16(a1, b1); swap32(a0, b0); swap32(a1, b1);
            return (u32x4){a0, a1, b0, b1}; };
#pragma unroll
        for (int a = 0; a < 2; ++a)
#pragma unroll
            for (int s = 0; s < 2; ++s) {
                kvp[a][s] = qkfrag(acc[2 * a][2 + s], acc[2 * a + 1][2 + s], 1.0f); kvp[a][2 + s] = vfrag(acc[2 * a + s][4], acc[2 * a + s][5]);
                __builtin_amdgcn_raw_buffer_store_b128(kvp[a][s], skvw, lane * 16 + a * 4096 + s * 1024, 0, 16);
                __builtin_amdgcn_raw_buffer_store_b128(kvp[a][2 + s], skvw, lane * 16 + a * 4096 + 2048 + s * 1024, 0, 16);
            }
#pragma unroll
        for (int a = 0; a < 2; ++a) {
            const int tl = wm * 2 + a;
            LAS char* lp = lds + L_S0 + (tl >> 2) * 32768 + wn * 16384 + (tl & 3) * 4096 + lane * 16;
#pragma unroll
            for (int f = 0; f < 4; ++f) *(LAS u32x4*)(lp + f * 1024) = kvp[a][f];
        }
        SBAR();
#pragma unroll
        for (int a = 0; a < 2; ++a)
#pragma unroll
            for (int s = 0; s < 2; ++s) qf[a][s] = BF(qkfrag(acc[2 * a][s], acc[2 * a + 1][s], QSCALE));
    }
    asm volatile("" : "+v"(epoch));
    const u64 tag = mix64(((u64)rfl((unsigned)(epoch >> 32)) << 32) | rfl((unsigned)epoch));
    STAMP_MID(2)

    const srd_t skv = make_srd(ws + WS_KV + (size_t)(b * 32 + hp * 2) * 64 * 4096, 2 * 64 * 4096);
    const srd_t swo = make_srd(b ? woB : woA, 1024 * 1024 * 2);
    const int tm = (xcd & 3) * 4 + (j >> 3), tn = j & 7;
    const unsigned vl = lane * 16;
    const unsigned vwo = (lane >> 4) * 2048 + (((lane & 15) ^ (((lane >> 4) & 3) << 2)) << 4);
    auto chunk_off = [&](int c) { const int i = (c - 2) >> 1; const int blk = i < mb ? i : i + 1; return (unsigned)((wn * 64 + blk * 8 + (c & 1) * 4 + wm) * 4096); };
    const unsigned ldw = lds0 + wid * 4096;
#define DKV(coff, slotoff, i) dma4c<i>(ldw + (slotoff), vl, skv, coff)
    const unsigned wo0 = wm * 8 * 2048 + tn * 256;
    auto dwu = [&](int head, unsigned slotaddr, int p) { dma16(lds0 + slotaddr + wm * 2048 + p * 1024, vwo, swo, wo0 + (head * 32 + p * 4) * 2048); };
    auto wo_rs = [&](int k) { return (unsigned)(L_R0 + wn * 24576 + k * 8192); };
    auto wo_hs = [&](int k) { return (unsigned)((k >> 1 == 0 ? L_S0 : k >> 1 == 1 ? L_S1 : L_S2) + wn * 16384 + (k & 1) * 8192); };
    bf16x8 sel; int selidx;
    { const int i16 = lane & 15; const short lo = (i16 < 2) ? (short)0x3F80 : (short)0, hi = (i16 == 2 || i16 == 3) ? (short)0x3F80 : (short)0;
      s16x8 t = {lo, lo, lo, lo, hi, hi, hi, hi}; sel = __builtin_bit_cast(bf16x8, t); selidx = (i16 & 1) ? 0xEEEE : 0x4444; }
    const LAS char* kvb = lds + wn * 16384 + lane * 16;
    GAS u64* const kvfl = flags + (FL_KV / 8) + (b * 32 + h) * 32;
    const GAS u64* const kvmine = kvfl + ((lane >> 1) & 7) * 4 + (lane & 1) * 2 + (wm >> 1);
    const LAS char* cntp = lds + L_CNT + wn * 16;
    const unsigned cnta = lds0 + L_CNT + wn * 16;
    f32x16 o[2]; f32x4 lacc[2];
    {
        f32x16 negm[2], S[2];
        bf16x8 kf[2][2], vf[2][2];
        u32x8 P;
        kf[0][0] = BF(kvp[0][0]); kf[0][1] = BF(kvp[0][1]); vf[0][0] = BF(kvp[0][2]); vf[0][1] = BF(kvp[0][3]);
#pragma unroll
        for (int a = 0; a < 2; ++a) {
            f32x16 z;
#pragma unroll
            for (int i = 0; i < 16; ++i) z[i] = 0.f;
            z = MFMA32(kf[0][0], qf[a][0], z); z = MFMA32(kf[0][1], qf[a][1], z);
            float m = z[0];
#pragma unroll
            for (int i = 1; i < 16; ++i) m = fmaxf(m, z[i]);
            m = halfmax(m);
#pragma unroll
            for (int i = 0; i < 16; ++i) { negm[a][i] = -m; o[a][i] = 0.f; }
            lacc[a] = (f32x4){0.f, 0.f, 0.f, 0.f};
        }
        S[0] = MFMA32(kf[0][0], qf[0][0], negm[0]); S[0] = MFMA32(kf[0][1], qf[0][1], S[0]);
        P = (u32x8){0u, 0u, 0u, 0u, 0u, 0u, 0u, 0u};
        bf16x8 vprev1 = vf[0][1];
        unsigned seen;
#define EXPI(X, i) X[i] = ex2(X[i])
#define UNIT(SX, SN, KA0, KA1, QB0, QB1, NEG, DOQK, OPREV, LPREV, VPREV1, OCUR, VCUR0, H0, H1, H2, H3) do { \
            if (DOQK) SN = MFMA32(KA0, QB0, NEG); H0; SBAR(); \
            EXPI(SX, 0); EXPI(SX, 1); EXPI(SX, 2); SBAR(); \
            LPREV = SMF(sel, P, LPREV, selidx); SBAR(); \
            EXPI(SX, 3); P[0] = cvtpk(SX[0], SX[1]); P[1] = cvtpk(SX[2], SX[3]); SBAR(); \
            OPREV = MFMA32(VPREV1, PHI(P), OPREV); H1; SBAR(); \
            EXPI(SX, 4); EXPI(SX, 5); EXPI(SX, 6); EXPI(SX, 7); P[2] = cvtpk(SX[4], SX[5]); P[3] = cvtpk(SX[6], SX[7]); SBAR(); \
            if (DOQK) SN = MFMA32(KA1, QB1, SN); H2; SBAR(); \
            EXPI(SX, 8); EXPI(SX, 9); EXPI(SX, 10); EXPI(SX, 11); P[4] = cvtpk(SX[8], SX[9]); P[5] = cvtpk(SX[10], SX[11]); SBAR(); \
            OCUR = MFMA32(VCUR0, PLO(P), OCUR); H3; SBAR(); \
            EXPI(SX, 12); EXPI(SX, 13); EXPI(SX, 14); EXPI(SX, 15); P[6] = cvtpk(SX[12], SX[13]); P[7] = cvtpk(SX[14], SX[15]); SBAR(); \
        } while (0)
#define TILEG(cur, nxt, LK, LV, LOAD, A2, A3, B0, B1, B2, B3) do { \
            UNIT(S[0], S[1], kf[cur][0], kf[cur][1], qf[1][0], qf[1][1], negm[1], true, o[1], lacc[1], vprev1, o[0], vf[cur][0], LK, LV, A2, A3); \
            UNIT(S[1], S[0], kf[nxt][0], kf[nxt][1], qf[0][0], qf[0][1], negm[0], LOAD, o[0], lacc[0], vf[cur][1], o[1], vf[cur][0], B0, B1, B2, B3); \
            vprev1 = vf[cur][1]; \
        } while (0)
#define TILEX(cur, nxt, rdp, LOAD, A2, A3, B0, B1, B2, B3) TILEG(cur, nxt, \
            if (LOAD) { kf[nxt][0] = ldsfrag(rdp); kf[nxt][1] = ldsfrag((rdp) + 1024); }, if (LOAD) { vf[nxt][0] = ldsfrag((rdp) + 2048); vf[nxt][1] = ldsfrag((rdp) + 3072); }, \
            LOAD, A2, A3, B0, B1, B2, B3)
#define TILE(cur, nxt, rdp, PK) TILEX(cur, nxt, rdp, true, NOP_, NOP_, NOP_, PK, NOP_, NOP_)
#define TILED(cur, nxt, rdp, coff, slot) TILEX(cur, nxt, rdp, true, DKV(coff, slot, 0), DKV(coff, slot, 1), NOP_, NOP_, DKV(coff, slot, 2), DKV(coff, slot, 3))
#define PEEK seen = lds_peek(cntp)
#define SIGNAL() lds_signal(cnta)
#define BALANCE(m) do { if (wn == 0 && (m) == FLIP) __builtin_amdgcn_s_setprio(0); } while (0)
        if (wn == 0) __builtin_amdgcn_s_setprio(2); else __builtin_amdgcn_s_setprio(1);
        auto own = [&](int p) {
            const int lo = wm < 2;
            const int off = lo ? (p < 4 ? L_S0 + ((1 - wm) * 2 + p - 2) * 4096 : L_S1 + (p - 4) * 4096) : (p < 6 ? L_S0 + (p - 2) * 4096 : L_S1 + ((3 - wm) * 2 + p - 6) * 4096);
            return kvb + off; };
        TILEG(0, 1, { kf[1][0] = BF(kvp[1][0]); kf[1][1] = BF(kvp[1][1]); }, { vf[1][0] = BF(kvp[1][2]); vf[1][1] = BF(kvp[1][3]); }, true, NOP_, NOP_, NOP_, NOP_, NOP_, NOP_);
        lds_signal(cnta + 4); lds_await(0, cntp + 4, 4);
        TILE(1, 0, own(2), NOP_);
        TILE(0, 1, own(3), NOP_);
        WAITV(0); if (lane == 0) st_agent(kvfl + mb * 4 + wm, tag);
        TILE(1, 0, own(4), NOP_);
        u64 pollv = ld_agent(kvmine);
        TILE(0, 1, own(5), NOP_);
        if (!__all(pollv == tag)) spin_flags(kvmine, tag);
        SIGNAL();
        lds_await(0, lds + L_CNT + 32, 8);
        unsigned cn = chunk_off(2);
        TILEX(1, 0, own(6), true, DKV(cn, L_S2, 0), DKV(cn, L_S2, 1), NOP_, PEEK, DKV(cn, L_S2, 2), DKV(cn, L_S2, 3));
        lds_await(seen, cntp, 4);
        cn = chunk_off(3);
        TILED(0, 1, own(7), cn, L_S0);
        WAITV(4); SIGNAL(); lds_await(0, cntp, 8);
        TILE(1, 0, kvb + L_S2, NOP_);
        cn = chunk_off(4);
        TILED(0, 1, kvb + L_S2 + 4096, cn, L_S1);
        WAITV(4); SIGNAL();
        TILE(1, 0, kvb + L_S2 + 8192, NOP_);
        TILE(0, 1, kvb + L_S2 + 12288, PEEK);
        lds_await(seen, cntp, 12);
        WAITV(0); SIGNAL();
        TILE(1, 0, kvb + L_S0, PEEK);
        STAMP_MID(7)
        int sc = L_S0, sn = L_S1, sp = L_S2;
        for (int m = 3; m <= 13; ++m) {
            const LAS char* cp = kvb + sc;
            cn = chunk_off(m + 2);
            lds_await(seen, cntp, 4 * (m + 1));
            BALANCE(4 * m);
            TILED(0, 1, cp + 4096, cn, sp);
            TILE(1, 0, cp + 8192, NOP_);
            TILE(0, 1, cp + 12288, NOP_);
            WAITV(0); SIGNAL();
            TILE(1, 0, kvb + sn, PEEK);
            const int t_ = sc; sc = sn; sn = sp; sp = t_;
        }
        lds_await(seen, cntp, 60);
        BALANCE(56);
        TILEX(0, 1, kvb + L_S2 + 4096, true, dwu(wn, wo_rs(0), 0), dwu(wn, wo_rs(0), 1), NOP_, NOP_, NOP_, NOP_);
        BALANCE(57);
        TILEX(1, 0, kvb + L_S2 + 8192, true, dwu(2 + wn, wo_rs(1), 0), dwu(2 + wn, wo_rs(1), 1), NOP_, NOP_, NOP_, NOP_);
        BALANCE(58);
        TILEX(0, 1, kvb + L_S2 + 12288, true, dwu(4 + wn, wo_rs(2), 0), dwu(4 + wn, wo_rs(2), 1), NOP_, NOP_, NOP_, NOP_);
        BALANCE(59);
        TILE(1, 0, kvb + L_S0, NOP_);
        BALANCE(60);
        TILE(0, 1, kvb + L_S0 + 4096, NOP_);
        BALANCE(61);
        TILE(1, 0, kvb + L_S0 + 8192, NOP_);
        BALANCE(62);
        TILE(0, 1, kvb + L_S0 + 12288, NOP_);
        TILEX(1, 0, kvb, false, NOP_, NOP_, NOP_, NOP_, NOP_, NOP_);
        lacc[1] = SMF(sel, P, lacc[1], selidx); o[1] = MFMA32(vprev1, PHI(P), o[1]);
    }
    if (wn == 0) __builtin_amdgcn_s_setprio(0);
    STAMP_MID(3)
    {
        float l[2];
        auto rowsum = [&](int a) { const float t0 = __shfl(lacc[a][0] + lacc[a][1], lane & 15), t1 = __shfl(lacc[a][2] + lacc[a][3], lane & 15); return (lane & 16) ? t1 : t0; };
        bool bad = false;
#pragma unroll
        for (int a = 0; a < 2; ++a) {
            l[a] = rowsum(a);
            float z = l[a] * 0.f, z1 = 0.f;
#pragma unroll
            for (int i = 0; i < 16; i += 2) { z = __builtin_fmaf(o[a][i], 0.f, z); z1 = __builtin_fmaf(o[a][i + 1], 0.f, z1); }
            bad |= !(l[a] < 1.0e36f) || !(z + z1 == 0.f);
        }
        if (__any(bad)) {
            spin_flags(kvfl + (lane & 31), tag);
            const srd_t sk1 = make_srd(ws + WS_KV + (size_t)(b * 32 + h) * 64 * 4096, 64 * 4096);
#define LDKV(t, f) BF(__builtin_amdgcn_raw_buffer_load_b128(sk1, lane * 16 + (f) * 1024, (t) * 4096, 16))
            f32x16 negm[2];
#pragma unroll
            for (int a = 0; a < 2; ++a) {
                float m = -__builtin_inff();
                for (int t = 0; t < 64; ++t) {
                    const bf16x8 k0 = LDKV(t, 0), k1 = LDKV(t, 1);
                    f32x16 z;
#pragma unroll
                    for (int i = 0; i < 16; ++i) z[i] = 0.f;
                    z = MFMA32(k0, qf[a][0], z); z = MFMA32(k1, qf[a][1], z);
#pragma unroll
                    for (int i = 0; i < 16; ++i) m = fmaxf(m, z[i]);
                }
                m = halfmax(m);
#pragma unroll
                for (int i = 0; i < 16; ++i) { negm[a][i] = -m; o[a][i] = 0.f; }
                lacc[a] = (f32x4){0.f, 0.f, 0.f, 0.f};
            }
            for (int t = 0; t < 64; ++t) {
                const bf16x8 k0 = LDKV(t, 0), k1 = LDKV(t, 1), v0 = LDKV(t, 2), v1 = LDKV(t, 3);
#pragma unroll
                for (int a = 0; a < 2; ++a) {
                    f32x16 z = MFMA32(k0, qf[a][0], negm[a]); z = MFMA32(k1, qf[a][1], z);
                    u32x8 P;
#pragma unroll
                    for (int i = 0; i < 8; ++i) P[i] = cvtpk(ex2(z[2 * i]), ex2(z[2 * i + 1]));
                    o[a] = MFMA32(v0, PLO(P), o[a]); o[a] = MFMA32(v1, PHI(P), o[a]); lacc[a] = SMF(sel, P, lacc[a], selidx);
                }
            }
            l[0] = rowsum(0); l[1] = rowsum(1);
        }
        const srd_t sow = make_srd(ws + WS_O + (size_t)(b * 64 + mb * 8 + wm * 2) * 65536, 2 * 65536);
#pragma unroll
        for (int a = 0; a < 2; ++a) {
            const float rl = __builtin_amdgcn_rcpf(l[a]);
            __builtin_amdgcn_raw_buffer_store_b128(pack8(o[a], 0, rl), sow, lane * 16 + a * 65536 + h * 2048, 0, 16);
            __builtin_amdgcn_raw_buffer_store_b128(pack8(o[a], 1, rl), sow, lane * 16 + a * 65536 + h * 2048 + 1024, 0, 16);
        }
    }
    GAS u64* const ofl = flags + (FL_O / 8) + b * 1024;
    WAITV(0); if (lane == 0) st_agent(ofl + (mb * 4 + wm) * 32 + h, tag);
    STAMP_MID(4)

    {
        const int wt = wm;
        unsigned seen;
        SIGNAL(); lds_await(0, cntp, 64);
        for (int i = 0; i < 6; ++i) { dwu(2 * (3 + i) + wn, wo_hs(i), 0); dwu(2 * (3 + i) + wn, wo_hs(i), 1); }
        spin_flags(ofl + (tm * 2 + (wt >> 1)) * 32 + (lane & 15) * 2 + wn, tag);
        STAMP_MID(5)
        const srd_t sor = make_srd(ws + WS_O + (size_t)(b * 64 + tm * 4 + wt) * 65536 + wn * 2048, 65536 - 2048);
        bf16x8 of[8][2], wf[3][4];
#define P3_LO(u) do { of[(u) & 7][0] = BF(__builtin_amdgcn_raw_buffer_load_b128(sor, lane * 16, (u) * 4096, 16)); \
                      of[(u) & 7][1] = BF(__builtin_amdgcn_raw_buffer_load_b128(sor, lane * 16, (u) * 4096 + 1024, 16)); } while (0)
        P3_LO(0); P3_LO(1); P3_LO(2); P3_LO(3); P3_LO(4); P3_LO(5); P3_LO(6); P3_LO(7);
        SIGNAL();
        unsigned wn3[4];
#pragma unroll
        for (int n = 0; n < 4; ++n) wn3[n] = (4 * (g4 >> 1) + q4) * 256 + ((n ^ q4) << 6) + (g4 & 1) * 32 + p4 * 8;
        f32x16 acc[4];
#pragma unroll
        for (int n = 0; n < 4; ++n)
#pragma unroll
            for (int i = 0; i < 16; ++i) acc[n][i] = 0.f;
        auto uslot = [&](int u) { return u < 3 ? wo_rs(u) : u < 9 ? wo_hs(u - 3) : u < 12 ? wo_rs(u - 9) : wo_hs(u - 12); };
#define P3_RW(kk, n) do { const LAS char* w_ = lds + uslot((kk) >> 1) + ((kk) & 1) * 4096 + wn3[n]; wf[(kk) % 3][n] = cat8(ldstr(w_), ldstr(w_ + 2048)); } while (0)
#define P3_KS(kk, M0, M1) do { \
            acc[0] = MFMA32(wf[(kk) % 3][0], of[((kk) >> 1) & 7][(kk) & 1], acc[0]); if ((kk) + 2 < 32) P3_RW((kk) + 2, 0); SBAR(); \
            acc[1] = MFMA32(wf[(kk) % 3][1], of[((kk) >> 1) & 7][(kk) & 1], acc[1]); if ((kk) + 2 < 32) P3_RW((kk) + 2, 1); M0; SBAR(); \
            acc[2] = MFMA32(wf[(kk) % 3][2], of[((kk) >> 1) & 7][(kk) & 1], acc[2]); if ((kk) + 2 < 32) P3_RW((kk) + 2, 2); SBAR(); \
            acc[3] = MFMA32(wf[(kk) % 3][3], of[((kk) >> 1) & 7][(kk) & 1], acc[3]); if ((kk) + 2 < 32) P3_RW((kk) + 2, 3); M1; SBAR(); \
        } while (0)
#define P3_UNIT(u, M0) do { P3_KS(2 * (u), M0, NOP_); P3_KS(2 * (u) + 1, NOP_, if ((u) + 8 < 16) P3_LO((u) + 8)); } while (0)
#define DWU(u, slot) do { dwu(2 * (u) + wn, slot, 0); dwu(2 * (u) + wn, slot, 1); } while (0)
        P3_RW(0, 0); P3_RW(0, 1); P3_RW(0, 2); P3_RW(0, 3); P3_RW(1, 0); P3_RW(1, 1); P3_RW(1, 2); P3_RW(1, 3); SBAR();
        P3_UNIT(0, NOP_);
        P3_UNIT(1, PEEK);
        lds_await(seen, cntp, 68);
        SIGNAL();
        P3_UNIT(2, PEEK);
        lds_await(seen, cntp, 72); DWU(9, wo_rs(0)); DWU(10, wo_rs(1)); DWU(11, wo_rs(2));
        P3_UNIT(3, NOP_);
        P3_UNIT(4, NOP_);
        SIGNAL();
        P3_UNIT(5, PEEK);
        lds_await(seen, cntp, 76); DWU(12, wo_hs(0)); DWU(13, wo_hs(1)); DWU(14, wo_hs(2));
        P3_UNIT(6, NOP_);
        WAITV(14); SIGNAL();
        P3_UNIT(7, PEEK);
        lds_await(seen, cntp, 80);
        SIGNAL();
        P3_UNIT(8, PEEK);
        lds_await(seen, cntp, 84); DWU(15, wo_hs(3));
        P3_UNIT(9, NOP_);
        WAITV(6); SIGNAL();
        P3_UNIT(10, PEEK);
        lds_await(seen, cntp, 88);
        P3_UNIT(11, NOP_);
        P3_UNIT(12, NOP_);
        WAITV(0); SIGNAL();
        P3_UNIT(13, PEEK);
        lds_await(seen, cntp, 92);
        P3_UNIT(14, NOP_);
        P3_UNIT(15, NOP_);
        STAMP_MID(6)
        const int xoff = (wm < 3 ? (wm == 0 ? L_S0 : wm == 1 ? L_S1 : L_S2) : L_R0) + lane * 16;
        if (wn == 0) {
            SIGNAL(); lds_await(0, cntp, 96);
#pragma unroll
            for (int n = 0; n < 4; ++n)
#pragma unroll
                for (int qd = 0; qd < 4; ++qd)
                    *(LAS f32x4*)(lds + xoff + n * 4096 + qd * 1024) = (f32x4){acc[n][4 * qd], acc[n][4 * qd + 1], acc[n][4 * qd + 2], acc[n][4 * qd + 3]};
            SIGNAL();
        } else {
            lds_await(0, lds + L_CNT, 100);
            if (tid == 256 && (xcd & 3) == 0 && j == 0) st_agent(flags + b * 32, tag);
            const srd_t sout = make_srd(out, 2048 * 2048 * 2);
            LAS char* stg = lds + (wm < 3 ? wo_rs(wm) : wo_hs(0));
#pragma unroll
            for (int n = 0; n < 4; ++n) {
                f32x16 v = acc[n];
#pragma unroll
                for (int qd = 0; qd < 4; ++qd) {
                    const f32x4 t = *(const LAS f32x4*)(lds + xoff + n * 4096 + qd * 1024);
                    v[4 * qd] += t[0]; v[4 * qd + 1] += t[1]; v[4 * qd + 2] += t[2]; v[4 * qd + 3] += t[3];
                }
#pragma unroll
                for (int gp = 0; gp < 2; ++gp) {
                    const unsigned a0 = cvtpk(v[8 * gp], v[8 * gp + 1]), a1 = cvtpk(v[8 * gp + 2], v[8 * gp + 3]);
                    const unsigned b0 = cvtpk(v[8 * gp + 4], v[8 * gp + 5]), b1 = cvtpk(v[8 * gp + 6], v[8 * gp + 7]);
                    auto r0 = __builtin_amdgcn_permlane32_swap(a0, b0, false, false);
                    auto r1 = __builtin_amdgcn_permlane32_swap(a1, b1, false, false);
                    const u32x4 w = {r0[0], r1[0], r0[1], r1[1]};
                    *(LAS u32x4*)(stg + r32 * 256 + (((n * 4 + gp * 2 + hh) ^ (r32 & 15)) << 4)) = w;
                }
            }
            const unsigned oo = (unsigned)(tm * 128 + wt * 32 + g4) * 4096u + (unsigned)(b * 1024 + tn * 128) * 2u + (lane & 15) * 16;
#pragma unroll
            for (int i = 0; i < 8; ++i) {
                const int row = 4 * i + g4;
                const u32x4 w = *(const LAS u32x4*)(stg + row * 256 + (((lane & 15) ^ (row & 15)) << 4));
                __builtin_amdgcn_raw_buffer_store_b128(w, sout, oo + i * 4 * 4096, 0, 16);
            }
        }
    }
    STAMP_END(ws)
}

extern "C" void kernel_launch(void* const* d_in, const int* in_sizes, int n_in, void* d_out, int out_size, void* d_ws, size_t ws_size, hipStream_t stream) {
    (void)hipFuncSetAttribute((const void*)mha_fused, hipFuncAttributeMaxDynamicSharedMemorySize, L_END);
    mha_fused<<<256, 512, L_END, stream>>>((const uint16_t*)d_in[0], (const uint16_t*)d_in[1], (const uint16_t*)d_in[2], (const uint16_t*)d_in[3], (const uint16_t*)d_in[4],
                                           (const uint16_t*)d_in[5], (char*)d_ws, (uint16_t*)d_out);
}
```

```cpp
#define TLBW
#include <hip/hip_runtime.h>
#include <stdint.h>

typedef __bf16 bf16x8 __attribute__((ext_vector_type(8)));
typedef __bf16 bf16x16 __attribute__((ext_vector_type(16)));
typedef __bf16 bf16x2 __attribute__((ext_vector_type(2)));
typedef short s16x4 __attribute__((ext_vector_type(4)));
typedef short s16x8 __attribute__((ext_vector_type(8)));
typedef float f32x16 __attribute__((ext_vector_type(16)));
typedef float f32x4 __attribute__((ext_vector_type(4)));
typedef float f32x2 __attribute__((ext_vector_type(2)));
typedef unsigned u32x4 __attribute__((ext_vector_type(4)));
typedef unsigned u32x8 __attribute__((ext_vector_type(8)));
typedef unsigned long long u64;
#define LAS __attribute__((address_space(3)))
#define GAS __attribute__((address_space(1)))
#define DI __device__ __forceinline__
#define MFMA32(a, b, c) __builtin_amdgcn_mfma_f32_32x32x16_bf16((a), (b), (c), 0, 0, 0)
#define MFMA16(a, b, c) __builtin_amdgcn_mfma_f32_16x16x32_bf16((a), (b), (c), 0, 0, 0)
#define SMF(a, b, c, idx) __builtin_amdgcn_smfmac_f32_16x16x64_bf16((a), __builtin_bit_cast(bf16x16, b), (c), (idx), 0, 0)
#define PLO(P) __builtin_bit_cast(bf16x8, __builtin_shufflevector(P, P, 0, 1, 2, 3))
#define PHI(P) __builtin_bit_cast(bf16x8, __builtin_shufflevector(P, P, 4, 5, 6, 7))
#define SBAR() __builtin_amdgcn_sched_barrier(0)
#define BF(x) __builtin_bit_cast(bf16x8, x)
#define NOP_ (void)0
typedef __amdgpu_buffer_rsrc_t srd_t;

constexpr float QSCALE = 0.17677669529663687f * 1.4426950408889634f;
constexpr unsigned WS_KV = 0;
constexpr unsigned WS_O = 16u << 20;
constexpr unsigned WS_FL = 24u << 20;
constexpr unsigned FL_KV = 4096;
constexpr unsigned FL_O = FL_KV + 16384;
constexpr int L_S0 = 32768, L_S1 = 65536, L_S2 = 0, L_R0 = 98304, L_END = 163840, L_CNT = L_END - 64, L_DUMMY = 147456;
constexpr int P1_XS = 32768, P1_WS = 24576, P1_WOFF = 98304;

DI unsigned rfl(unsigned x) { return __builtin_amdgcn_readfirstlane(x); }
DI srd_t make_srd(const void* p, unsigned bytes) { return __builtin_amdgcn_make_buffer_rsrc((void*)p, (short)0, (int)bytes, 0x00020000); }
DI void dma16(unsigned ldsdst, unsigned voff, srd_t srd, unsigned soff) {
    asm volatile("s_mov_b32 m0, %0\n\ts_nop 0\n\tbuffer_load_dwordx4 %1, %2, %3 offen lds" :: "s"(ldsdst), "v"(voff), "s"(srd), "s"(soff) : "memory");
}
template <int I> DI void dma4c(unsigned ldsdst, unsigned voff, srd_t srd, unsigned soff) {
    if constexpr (I == 0) asm volatile("s_mov_b32 m0, %0\n\ts_nop 0\n\tbuffer_load_dwordx4 %1, %2, %3 offen sc1 lds" :: "s"(ldsdst), "v"(voff), "s"(srd), "s"(soff) : "memory");
    else asm volatile("buffer_load_dwordx4 %0, %1, %2 offen offset:%3 sc1 lds" :: "v"(voff), "s"(srd), "s"(soff), "i"(I * 1024) : "memory");
}
DI unsigned cvtpk(float lo, float hi) { f32x2 v = {lo, hi}; bf16x2 b = __builtin_convertvector(v, bf16x2); return __builtin_bit_cast(unsigned, b); }
DI bf16x8 ldsfrag(const LAS char* p) { return *(const LAS bf16x8*)p; }
typedef short v4i16_t __attribute__((ext_vector_type(4)));
DI s16x4 ldstr(const LAS char* p) { return __builtin_amdgcn_ds_read_tr16_b64_v4i16((LAS v4i16_t*)p); }
DI bf16x8 cat8(s16x4 lo, s16x4 hi) { s16x8 r = __builtin_shufflevector(lo, hi, 0, 1, 2, 3, 4, 5, 6, 7); return __builtin_bit_cast(bf16x8, r); }
DI u32x4 pack8(const f32x16& x, int s, float mul) {
    u32x4 p; p[0] = cvtpk(x[8 * s] * mul, x[8 * s + 1] * mul); p[1] = cvtpk(x[8 * s + 2] * mul, x[8 * s + 3] * mul);
    p[2] = cvtpk(x[8 * s + 4] * mul, x[8 * s + 5] * mul); p[3] = cvtpk(x[8 * s + 6] * mul, x[8 * s + 7] * mul); return p; }
#define WAITBAR(vm) asm volatile("s_waitcnt vmcnt(" #vm ") lgkmcnt(0)\n\ts_barrier" ::: "memory")
#define WAITV(vm) asm volatile("s_waitcnt vmcnt(" #vm ")" ::: "memory")
DI float ex2(float x) { return __builtin_amdgcn_exp2f(x); }
DI float halfmax(float m) { auto r = __builtin_amdgcn_permlane32_swap(__float_as_uint(m), __float_as_uint(m), false, false); return fmaxf(__uint_as_float(r[0]), __uint_as_float(r[1])); }
DI u64 mix64(u64 z) { z += 0x9E3779B97F4A7C15ull; z = (z ^ (z >> 30)) * 0xBF58476D1CE4E5B9ull; z = (z ^ (z >> 27)) * 0x94D049BB133111EBull; return z ^ (z >> 31); }
DI u64 ld_agent(const GAS u64* p) { return __hip_atomic_load(p, __ATOMIC_RELAXED, __HIP_MEMORY_SCOPE_AGENT); }
DI void st_agent(GAS u64* p, u64 v) { __hip_atomic_store(p, v, __ATOMIC_RELAXED, __HIP_MEMORY_SCOPE_AGENT); }
DI void spin_flags(const GAS u64* f, u64 tag) {
    const u64 t0 = __builtin_amdgcn_s_memrealtime();
    u64 v0 = ld_agent(f);
    for (;;) {
        __builtin_amdgcn_s_sleep(6);
        const u64 v1 = ld_agent(f);
        if (__all(v0 == tag)) break;
        __builtin_amdgcn_s_sleep(6);
        v0 = ld_agent(f);
        if (__all(v1 == tag)) break;
        if (__builtin_amdgcn_s_memrealtime() - t0 > 20000000ull) break;
    }
    asm volatile("" ::: "memory");
}
DI void lds_signal(unsigned addr) {
    unsigned one = 1;
    asm volatile("s_waitcnt lgkmcnt(0)\n\ts_mov_b64 exec, 1\n\tds_add_u32 %0, %1\n\ts_mov_b64 exec, -1" :: "v"(addr), "v"(one) : "memory");
}
DI unsigned lds_peek(const LAS char* p) { return *(const volatile LAS unsigned*)p; }
DI void lds_await(unsigned seen, const LAS char* p, unsigned need) {
    if (rfl(seen) < need) {
        _Pragma("nounroll") for (int it = 0; it < (1 << 22); ++it) { if (rfl(lds_peek(p)) >= need) break; __builtin_amdgcn_s_sleep(0); }
    }
    asm volatile("" ::: "memory");
}

#define STAMP_BEGIN
#define STAMP_MID(i)
#define STAMP_END(ws)
#define FLIP 56

__global__ __launch_bounds__(512, 2) void mha_fused(const uint16_t* xA, const uint16_t* wA, const uint16_t* woA, const uint16_t* xB, const uint16_t* wB, const uint16_t* woB,
                                                    char* ws, uint16_t* out) {
    extern __shared__ __attribute__((aligned(16))) char lds_[];
    LAS char* lds = (LAS char*)lds_;
    STAMP_BEGIN
    const int tid = threadIdx.x, lane = tid & 63, wid = rfl(tid >> 6);
    const int bid = blockIdx.x, xcd = bid & 7, j = bid >> 3;
    const int b = xcd >> 2;
    const int mb = (xcd & 1) * 4 + (j & 3), hp = ((xcd >> 1) & 1) * 8 + (j >> 2);
    const int wm = wid & 3, wn = wid >> 2, h = hp * 2 + wn;
    const unsigned lds0 = (unsigned)(uintptr_t)lds_;
    const int r32 = lane & 31, hh = lane >> 5, g4 = lane >> 4, q4 = (lane >> 2) & 3, p4 = lane & 3;
    GAS u64* const flags = (GAS u64*)(ws + WS_FL);
    u64 epoch = ld_agent(flags + b * 32);
    if (tid < 16) *(LAS unsigned*)(lds + L_CNT + tid * 4) = 0u;

    bf16x8 qf[2][2];
    u32x4 kvp[2][4];
    const srd_t skvw = make_srd(ws + WS_KV + (size_t)(b * 32 + h) * 64 * 4096 + (size_t)(mb * 8 + wm * 2) * 4096, 2 * 4096);
    {
        const srd_t sx = make_srd(b ? xB : xA, 2048 * 1024 * 2), sw = make_srd(b ? wB : wA, 1024 * 3072 * 2);
        const unsigned vx0 = (lane >> 3) * 2048 + (((lane & 7) ^ (lane >> 4)) << 4);
        const unsigned vx1 = (lane >> 3) * 2048 + (((lane & 7) ^ (4 + (lane >> 4))) << 4);
        const unsigned vw = (lane >> 3) * 6144 + (((lane & 7) ^ (((lane >> 4) & 1) << 2) ^ ((wid & 1) << 1)) << 4);
        const unsigned sx0 = (mb * 256 + wid * 32) * 2048, sw0 = wid * 8 * 6144 + hp * 128;
        auto dx = [&](int kt, int i) { dma16(lds0 + (kt % 3) * P1_XS + wid * 4096 + i * 1024, (i & 1) ? vx1 : vx0, sx, sx0 + i * 8 * 2048 + kt * 128); };
        auto dw = [&](int kt, int i) { dma16(lds0 + P1_WOFF + (kt & 1) * P1_WS + wid * 1024 + i * 8192, vw, sw, sw0 + i * 2048 + kt * 64 * 6144); };
        for (int i = 0; i < 3; ++i) dw(0, i);
        for (int i = 0; i < 4; ++i) dx(0, i);
        for (int i = 0; i < 3; ++i) dw(1, i);
        for (int i = 0; i < 4; ++i) dx(1, i);
        for (int i = 0; i < 4; ++i) dx(2, i);
        constexpr int PFD = 4;
        const char* pfp; unsigned pfstep;
        {
            const int l = lane < 10 ? lane : 9;
            const int xrow = (xcd & 1) * 1024 + j * 32 + wid * 4 + (l & 3), wl = wid * 6 + (l - 4);
            const size_t woff = (size_t)(j * 2 + wl / 24) * 6144 + ((wl % 24) >> 3) * 2048 + (((xcd >> 1) & 1) * 8 + (wl & 7)) * 128;
            pfp = l < 4 ? (const char*)(b ? xB : xA) + (size_t)xrow * 2048 : (const char*)(b ? wB : wA) + woff;
            pfstep = l < 4 ? 128u : 64u * 6144u;
        }
        const unsigned pfdst = lds0 + L_DUMMY + wid * 1024;
        auto pf = [&](int kt) {
            const char* g = pfp + (size_t)kt * pfstep;
            asm volatile("s_mov_b32 m0, %0\n\ts_nop 0\n\tglobal_load_lds_dword %1, off" :: "s"(pfdst), "v"(g) : "memory"); };
        pf(3);
        {
            const uintptr_t p0 = (uintptr_t)(ws + WS_KV + (size_t)(b * 32 + hp * 2) * 64 * 4096) + (tid & 7) * 65536, p1 = (uintptr_t)(ws + WS_O + (size_t)b * 64 * 65536) + ((tid - 8) & 63) * 65536,
                            p2 = (uintptr_t)(b ? woB : woA) + ((tid - 72) & 31) * 65536, p3 = (uintptr_t)out + ((tid - 104) & 127) * 65536;
            const char* g = (const char*)(tid < 8 ? p0 : tid < 72 ? p1 : tid < 104 ? p2 : p3);
            asm volatile("s_mov_b32 m0, %0\n\ts_nop 0\n\tglobal_load_lds_dword %1, off" :: "s"(pfdst), "v"(g) : "memory");
        }
#define XTRA 1
        const int r16 = lane & 15;
        unsigned xa[2], wb[2];
#pragma unroll
        for (int s = 0; s < 2; ++s) xa[s] = (wm * 64 + r16) * 128 + (((4 * s + g4) ^ (r16 >> 1)) << 4);
#pragma unroll
        for (int c = 0; c < 2; ++c) wb[c] = P1_WOFF + (8 * g4 + q4) * 128 + ((wn ^ (q4 >> 1)) << 6) + ((c ^ (g4 & 1)) << 5) + p4 * 8;
        f32x4 acc[4][6];
#pragma unroll
        for (int a = 0; a < 4; ++a)
#pragma unroll
            for (int c = 0; c < 6; ++c) acc[a][c] = (f32x4){0.f, 0.f, 0.f, 0.f};
        bf16x8 xf[2][4][2];
        s16x4 wlo[3][2], whi[3][2];
#define P1_RX(kt, tb, s) xf[(kt) & 1][tb][s] = ldsfrag(lds + ((kt) % 3) * P1_XS + xa[s] + (tb) * 2048)
#define P1_WL(kt, fb, s) wlo[(fb) % 3][s] = ldstr(lds + wb[(fb) & 1] + ((kt) & 1) * P1_WS + ((fb) >> 1) * 8192 + (s) * 4096)
#define P1_WH(kt, fb, s) whi[(fb) % 3][s] = ldstr(lds + wb[(fb) & 1] + ((kt) & 1) * P1_WS + ((fb) >> 1) * 8192 + (s) * 4096 + 512)
#define P1_M1(kt, tb, fb, s) do { const bf16x8 w_ = cat8(wlo[(fb) % 3][s], whi[(fb) % 3][s]); if ((fb) < 4) acc[tb][fb] = MFMA16(w_, xf[(kt) & 1][tb][s], acc[tb][fb]); else acc[tb][fb] = MFMA16(xf[(kt) & 1][tb][s], w_, acc[tb][fb]); } while (0)
#define P1_FB8(kt, fb, R0, R1, R2, R3, R4, R5, R6, R7) do { \
            P1_M1(kt, 0, fb, 0); R0; SBAR(); P1_M1(kt, 1, fb, 0); R1; SBAR(); P1_M1(kt, 2, fb, 0); R2; SBAR(); P1_M1(kt, 3, fb, 0); R3; SBAR(); \
            P1_M1(kt, 0, fb, 1); R4; SBAR(); P1_M1(kt, 1, fb, 1); R5; SBAR(); P1_M1(kt, 2, fb, 1); R6; SBAR(); P1_M1(kt, 3, fb, 1); R7; SBAR(); } while (0)
        WAITBAR(13);
        STAMP_MID(0)
        P1_RX(0, 0, 0); P1_WL(0, 0, 0); P1_WH(0, 0, 0); P1_RX(0, 1, 0); P1_RX(0, 2, 0); P1_RX(0, 3, 0); P1_WL(0, 0, 1); P1_WH(0, 0, 1);
        P1_RX(0, 0, 1); P1_RX(0, 1, 1); P1_RX(0, 2, 1); P1_RX(0, 3, 1); P1_WL(0, 1, 0); P1_WH(0, 1, 0); P1_WL(0, 1, 1); P1_WH(0, 1, 1); SBAR();
#pragma unroll
        for (int t = 0; t < 16; ++t) {
            const bool xs = (t >= 1 && t + 2 < 16);
            P1_FB8(t, 0, P1_WL(t, 2, 0), NOP_, P1_WH(t, 2, 0), NOP_, P1_WL(t, 2, 1), if (xs) dx(t + 2, 0), P1_WH(t, 2, 1), NOP_);
            P1_FB8(t, 1, P1_WL(t, 3, 0), NOP_, P1_WH(t, 3, 0), NOP_, P1_WL(t, 3, 1), if (xs) dx(t + 2, 1), P1_WH(t, 3, 1), NOP_);
            P1_FB8(t, 2, P1_WL(t, 4, 0), NOP_, P1_WH(t, 4, 0), NOP_, P1_WL(t, 4, 1), if (xs) dx(t + 2, 2), P1_WH(t, 4, 1), NOP_);
            P1_FB8(t, 3, P1_WL(t, 5, 0), NOP_, P1_WH(t, 5, 0), NOP_, P1_WL(t, 5, 1), if (xs) dx(t + 2, 3), P1_WH(t, 5, 1), NOP_);
            if (t < 15) {
                if (t == 0 && XTRA) WAITBAR(6); else if (t == 14) WAITBAR(0); else if (t - 1 + PFD < 16) WAITBAR(5); else WAITBAR(4);
                const bool wsch = (t + 2 < 16);
                P1_FB8(t, 4, P1_RX(t + 1, 0, 0), P1_WL(t + 1, 0, 0), P1_WH(t + 1, 0, 0), P1_RX(t + 1, 1, 0), P1_RX(t + 1, 2, 0), P1_RX(t + 1, 3, 0), P1_WL(t + 1, 0, 1), { P1_WH(t + 1, 0, 1); if (wsch) dw(t + 2, 0); });
                P1_FB8(t, 5, P1_RX(t + 1, 0, 1), P1_RX(t + 1, 1, 1), P1_RX(t + 1, 2, 1), { P1_RX(t + 1, 3, 1); if (wsch) dw(t + 2, 1); }, P1_WL(t + 1, 1, 0), { P1_WH(t + 1, 1, 0); if (wsch) dw(t + 2, 2); }, P1_WL(t + 1, 1, 1), { P1_WH(t + 1, 1, 1); if (t + PFD < 16) pf(t + PFD); });
            } else {
                P1_FB8(t, 4, NOP_, NOP_, NOP_, NOP_, NOP_, NOP_, NOP_, NOP_);
                P1_FB8(t, 5, NOP_, NOP_, NOP_, NOP_, NOP_, NOP_, NOP_, NOP_);
            }
        }
        lds_signal(lds0 + L_CNT + 32);
        STAMP_MID(1)
        auto swap16 = [](unsigned& x, unsigned& y) { auto r = __builtin_amdgcn_permlane16_swap(x, y, false, false); x = r[0]; y = r[1]; };
        auto swap32 = [](unsigned& x, unsigned& y) { auto r = __builtin_amdgcn_permlane32_swap(x, y, false, false); x = r[0]; y = r[1]; };
        auto qkfrag = [&](const f32x4& t0, const f32x4& t1, float mul) {
            unsigned a0 = cvtpk(t0[0] * mul, t0[1] * mul), a1 = cvtpk(t0[2] * mul, t0[3] * mul), b0 = cvtpk(t1[0] * mul, t1[1] * mul), b1 = cvtpk(t1[2] * mul, t1[3] * mul);
            swap16(a0, b0); swap16(a1, b1);
            return (u32x4){a0, a1, b0, b1}; };
        auto vfrag = [&](const f32x4& x0, const f32x4& x1) {
            unsigned a0 = cvtpk(x0[0], x0[1]), a1 = cvtpk(x0[2], x0[3]), b0 = cvtpk(x1[0], x1[1]), b1 = cvtpk(x1[2], x1[3]);
            swap16(a0, b0); swap16(a1, b1); swap32(a0, b0); swap32(a1, b1);
            return (u32x4){a0, a1, b0, b1}; };
#pragma unroll
        for (int a = 0; a < 2; ++a)
#pragma unroll
            for (int s = 0; s < 2; ++s) {
                kvp[a][s] = qkfrag(acc[2 * a][2 + s], acc[2 * a + 1][2 + s], 1.0f); kvp[a][2 + s] = vfrag(acc[2 * a + s][4], acc[2 * a + s][5]);
                __builtin_amdgcn_raw_buffer_store_b128(kvp[a][s], skvw, lane * 16 + a * 4096 + s * 1024, 0, 16);
                __builtin_amdgcn_raw_buffer_store_b128(kvp[a][2 + s], skvw, lane * 16 + a * 4096 + 2048 + s * 1024, 0, 16);
            }
#pragma unroll
        for (int a = 0; a < 2; ++a) {
            const int tl = wm * 2 + a;
            LAS char* lp = lds + L_S0 + (tl >> 2) * 32768 + wn * 16384 + (tl & 3) * 4096 + lane * 16;
#pragma unroll
            for (int f = 0; f < 4; ++f) *(LAS u32x4*)(lp + f * 1024) = kvp[a][f];
        }
        SBAR();
#pragma unroll
        for (int a = 0; a < 2; ++a)
#pragma unroll
            for (int s = 0; s < 2; ++s) qf[a][s] = BF(qkfrag(acc[2 * a][s], acc[2 * a + 1][s], QSCALE));
    }
    asm volatile("" : "+v"(epoch));
    const u64 tag = mix64(((u64)rfl((unsigned)(epoch >> 32)) << 32) | rfl((unsigned)epoch));
    STAMP_MID(2)

    const srd_t skv = make_srd(ws + WS_KV + (size_t)(b * 32 + hp * 2) * 64 * 4096, 2 * 64 * 4096);
    const srd_t swo = make_srd(b ? woB : woA, 1024 * 1024 * 2);
    const int tm = (xcd & 3) * 4 + (j >> 3), tn = j & 7;
    const unsigned vl = lane * 16;
    const unsigned vwo = (lane >> 4) * 2048 + (((lane & 15) ^ (((lane >> 4) & 3) << 2)) << 4);
    auto chunk_off = [&](int c) { const int i = (c - 2) >> 1; const int blk = i < mb ? i : i + 1; return (unsigned)((wn * 64 + blk * 8 + (c & 1) * 4 + wm) * 4096); };
    const unsigned ldw = lds0 + wid * 4096;
#define DKV(coff, slotoff, i) dma4c<i>(ldw + (slotoff), vl, skv, coff)
    const unsigned wo0 = wm * 8 * 2048 + tn * 256;
    auto dwu = [&](int head, unsigned slotaddr, int p) { dma16(lds0 + slotaddr + wm * 2048 + p * 1024, vwo, swo, wo0 + (head * 32 + p * 4) * 2048); };
    auto wo_rs = [&](int k) { return (unsigned)(L_R0 + wn * 24576 + k * 8192); };
    auto wo_hs = [&](int k) { return (unsigned)((k >> 1 == 0 ? L_S0 : k >> 1 == 1 ? L_S1 : L_S2) + wn * 16384 + (k & 1) * 8192); };
    bf16x8 sel; int selidx;
    { const int i16 = lane & 15; const short lo = (i16 < 2) ? (short)0x3F80 : (short)0, hi = (i16 == 2 || i16 == 3) ? (short)0x3F80 : (short)0;
      s16x8 t = {lo, lo, lo, lo, hi, hi, hi, hi}; sel = __builtin_bit_cast(bf16x8, t); selidx = (i16 & 1) ? 0xEEEE : 0x4444; }
    const LAS char* kvb = lds + wn * 16384 + lane * 16;
    GAS u64* const kvfl = flags + (FL_KV / 8) + (b * 32 + h) * 32;
    const GAS u64* const kvmine = kvfl + ((lane >> 1) & 7) * 4 + (lane & 1) * 2 + (wm >> 1);
    const LAS char* cntp = lds + L_CNT + wn * 16;
    const unsigned cnta = lds0 + L_CNT + wn * 16;
    f32x16 o[2]; f32x4 lacc[2];
    {
        f32x16 negm[2], S[2];
        bf16x8 kf[2][2], vf[2][2];
        u32x8 P;
        kf[0][0] = BF(kvp[0][0]); kf[0][1] = BF(kvp[0][1]); vf[0][0] = BF(kvp[0][2]); vf[0][1] = BF(kvp[0][3]);
#pragma unroll
        for (int a = 0; a < 2; ++a) {
            f32x16 z;
#pragma unroll
            for (int i = 0; i < 16; ++i) z[i] = 0.f;
            z = MFMA32(kf[0][0], qf[a][0], z); z = MFMA32(kf[0][1], qf[a][1], z);
            float m = z[0];
#pragma unroll
            for (int i = 1; i < 16; ++i) m = fmaxf(m, z[i]);
            m = halfmax(m);
#pragma unroll
            for (int i = 0; i < 16; ++i) { negm[a][i] = -m; o[a][i] = 0.f; }
            lacc[a] = (f32x4){0.f, 0.f, 0.f, 0.f};
        }
        S[0] = MFMA32(kf[0][0], qf[0][0], negm[0]); S[0] = MFMA32(kf[0][1], qf[0][1], S[0]);
        P = (u32x8){0u, 0u, 0u, 0u, 0u, 0u, 0u, 0u};
        bf16x8 vprev1 = vf[0][1];
        unsigned seen;
#define EXPI(X, i) X[i] = ex2(X[i])
#define UNIT(SX, SN, KA0, KA1, QB0, QB1, NEG, DOQK, OPREV, LPREV, VPREV1, OCUR, VCUR0, H0, H1, H2, H3) do { \
            if (DOQK) SN = MFMA32(KA0, QB0, NEG); H0; SBAR(); \
            EXPI(SX, 0); EXPI(SX, 1); EXPI(SX, 2); SBAR(); \
            LPREV = SMF(sel, P, LPREV, selidx); SBAR(); \
            EXPI(SX, 3); P[0] = cvtpk(SX[0], SX[1]); P[1] = cvtpk(SX[2], SX[3]); SBAR(); \
            OPREV = MFMA32(VPREV1, PHI(P), OPREV); H1; SBAR(); \
            EXPI(SX, 4); EXPI(SX, 5); EXPI(SX, 6); EXPI(SX, 7); P[2] = cvtpk(SX[4], SX[5]); P[3] = cvtpk(SX[6], SX[7]); SBAR(); \
            if (DOQK) SN = MFMA32(KA1, QB1, SN); H2; SBAR(); \
            EXPI(SX, 8); EXPI(SX, 9); EXPI(SX, 10); EXPI(SX, 11); P[4] = cvtpk(SX[8], SX[9]); P[5] = cvtpk(SX[10], SX[11]); SBAR(); \
            OCUR = MFMA32(VCUR0, PLO(P), OCUR); H3; SBAR(); \
            EXPI(SX, 12); EXPI(SX, 13); EXPI(SX, 14); EXPI(SX, 15); P[6] = cvtpk(SX[12], SX[13]); P[7] = cvtpk(SX[14], SX[15]); SBAR(); \
        } while (0)
#define TILEG(cur, nxt, LK, LV, LOAD, A2, A3, B0, B1, B2, B3) do { \
            UNIT(S[0], S[1], kf[cur][0], kf[cur][1], qf[1][0], qf[1][1], negm[1], true, o[1], lacc[1], vprev1, o[0], vf[cur][0], LK, LV, A2, A3); \
            UNIT(S[1], S[0], kf[nxt][0], kf[nxt][1], qf[0][0], qf[0][1], negm[0], LOAD, o[0], lacc[0], vf[cur][1], o[1], vf[cur][0], B0, B1, B2, B3); \
            vprev1 = vf[cur][1]; \
        } while (0)
#define TILEX(cur, nxt, rdp, LOAD, A2, A3, B0, B1, B2, B3) TILEG(cur, nxt, \
            if (LOAD) { kf[nxt][0] = ldsfrag(rdp); kf[nxt][1] = ldsfrag((rdp) + 1024); }, if (LOAD) { vf[nxt][0] = ldsfrag((rdp) + 2048); vf[nxt][1] = ldsfrag((rdp) + 3072); }, \
            LOAD, A2, A3, B0, B1, B2, B3)
#define TILE(cur, nxt, rdp, PK) TILEX(cur, nxt, rdp, true, NOP_, NOP_, NOP_, PK, NOP_, NOP_)
#define TILED(cur, nxt, rdp, coff, slot) TILEX(cur, nxt, rdp, true, DKV(coff, slot, 0), DKV(coff, slot, 1), NOP_, NOP_, DKV(coff, slot, 2), DKV(coff, slot, 3))
#define PEEK seen = lds_peek(cntp)
#define SIGNAL() lds_signal(cnta)
#define BALANCE(m) do { if (wn == 0 && (m) == FLIP) __builtin_amdgcn_s_setprio(0); } while (0)
        if (wn == 0) __builtin_amdgcn_s_setprio(2); else __builtin_amdgcn_s_setprio(1);
        auto own = [&](int p) {
            const int lo = wm < 2;
            const int off = lo ? (p < 4 ? L_S0 + ((1 - wm) * 2 + p - 2) * 4096 : L_S1 + (p - 4) * 4096) : (p < 6 ? L_S0 + (p - 2) * 4096 : L_S1 + ((3 - wm) * 2 + p - 6) * 4096);
            return kvb + off; };
        TILEG(0, 1, { kf[1][0] = BF(kvp[1][0]); kf[1][1] = BF(kvp[1][1]); }, { vf[1][0] = BF(kvp[1][2]); vf[1][1] = BF(kvp[1][3]); }, true, NOP_, NOP_, NOP_, NOP_, NOP_, NOP_);
        lds_signal(cnta + 4); lds_await(0, cntp + 4, 4);
        TILE(1, 0, own(2), NOP_);
        TILE(0, 1, own(3), NOP_);
        WAITV(0); if (lane == 0) st_agent(kvfl + mb * 4 + wm, tag);
        TILE(1, 0, own(4), NOP_);
        u64 pollv = ld_agent(kvmine);
        TILE(0, 1, own(5), NOP_);
        if (!__all(pollv == tag)) spin_flags(kvmine, tag);
        SIGNAL();
        lds_await(0, lds + L_CNT + 32, 8);
        unsigned cn = chunk_off(2);
        TILEX(1, 0, own(6), true, DKV(cn, L_S2, 0), DKV(cn, L_S2, 1), NOP_, PEEK, DKV(cn, L_S2, 2), DKV(cn, L_S2, 3));
        lds_await(seen, cntp, 4);
        cn = chunk_off(3);
        TILED(0, 1, own(7), cn, L_S0);
        WAITV(4); SIGNAL(); lds_await(0, cntp, 8);
        TILE(1, 0, kvb + L_S2, NOP_);
        cn = chunk_off(4);
        TILED(0, 1, kvb + L_S2 + 4096, cn, L_S1);
        WAITV(4); SIGNAL();
        TILE(1, 0, kvb + L_S2 + 8192, NOP_);
        TILE(0, 1, kvb + L_S2 + 12288, PEEK);
        lds_await(seen, cntp, 12);
        WAITV(0); SIGNAL();
        TILE(1, 0, kvb + L_S0, PEEK);
        STAMP_MID(7)
        int sc = L_S0, sn = L_S1, sp = L_S2;
        for (int m = 3; m <= 13; ++m) {
            const LAS char* cp = kvb + sc;
            cn = chunk_off(m + 2);
            lds_await(seen, cntp, 4 * (m + 1));
            BALANCE(4 * m);
            TILED(0, 1, cp + 4096, cn, sp);
            TILE(1, 0, cp + 8192, NOP_);
            TILE(0, 1, cp + 12288, NOP_);
            WAITV(0); SIGNAL();
            TILE(1, 0, kvb + sn, PEEK);
            const int t_ = sc; sc = sn; sn = sp; sp = t_;
        }
        lds_await(seen, cntp, 60);
        BALANCE(56);
        TILEX(0, 1, kvb + L_S2 + 4096, true, dwu(wn, wo_rs(0), 0), dwu(wn, wo_rs(0), 1), NOP_, NOP_, NOP_, NOP_);
        BALANCE(57);
        TILEX(1, 0, kvb + L_S2 + 8192, true, dwu(2 + wn, wo_rs(1), 0), dwu(2 + wn, wo_rs(1), 1), NOP_, NOP_, NOP_, NOP_);
        BALANCE(58);
        TILEX(0, 1, kvb + L_S2 + 12288, true, dwu(4 + wn, wo_rs(2), 0), dwu(4 + wn, wo_rs(2), 1), NOP_, NOP_, NOP_, NOP_);
        BALANCE(59);
        TILE(1, 0, kvb + L_S0, NOP_);
        BALANCE(60);
        TILE(0, 1, kvb + L_S0 + 4096, NOP_);
        BALANCE(61);
        TILE(1, 0, kvb + L_S0 + 8192, NOP_);
        BALANCE(62);
        TILE(0, 1, kvb + L_S0 + 12288, NOP_);
        TILEX(1, 0, kvb, false, NOP_, NOP_, NOP_, NOP_, NOP_, NOP_);
        lacc[1] = SMF(sel, P, lacc[1], selidx); o[1] = MFMA32(vprev1, PHI(P), o[1]);
    }
    if (wn == 0) __builtin_amdgcn_s_setprio(0);
    STAMP_MID(3)
    {
        float l[2];
        auto rowsum = [&](int a) { const float t0 = __shfl(lacc[a][0] + lacc[a][1], lane & 15), t1 = __shfl(lacc[a][2] + lacc[a][3], lane & 15); return (lane & 16) ? t1 : t0; };
        bool bad = false;
#pragma unroll
        for (int a = 0; a < 2; ++a) {
            l[a] = rowsum(a);
            float z = l[a] * 0.f, z1 = 0.f;
#pragma unroll
            for (int i = 0; i < 16; i += 2) { z = __builtin_fmaf(o[a][i], 0.f, z); z1 = __builtin_fmaf(o[a][i + 1], 0.f, z1); }
            bad |= !(l[a] < 1.0e36f) || !(z + z1 == 0.f);
        }
        if (__any(bad)) {
            spin_flags(kvfl + (lane & 31), tag);
            const srd_t sk1 = make_srd(ws + WS_KV + (size_t)(b * 32 + h) * 64 * 4096, 64 * 4096);
#define LDKV(t, f) BF(__builtin_amdgcn_raw_buffer_load_b128(sk1, lane * 16 + (f) * 1024, (t) * 4096, 16))
            f32x16 negm[2];
#pragma unroll
            for (int a = 0; a < 2; ++a) {
                float m = -__builtin_inff();
                for (int t = 0; t < 64; ++t) {
                    const bf16x8 k0 = LDKV(t, 0), k1 = LDKV(t, 1);
                    f32x16 z;
#pragma unroll
                    for (int i = 0; i < 16; ++i) z[i] = 0.f;
                    z = MFMA32(k0, qf[a][0], z); z = MFMA32(k1, qf[a][1], z);
#pragma unroll
                    for (int i = 0; i < 16; ++i) m = fmaxf(m, z[i]);
                }
                m = halfmax(m);
#pragma unroll
                for (int i = 0; i < 16; ++i) { negm[a][i] = -m; o[a][i] = 0.f; }
                lacc[a] = (f32x4){0.f, 0.f, 0.f, 0.f};
            }
            for (int t = 0; t < 64; ++t) {
                const bf16x8 k0 = LDKV(t, 0), k1 = LDKV(t, 1), v0 = LDKV(t, 2), v1 = LDKV(t, 3);
#pragma unroll
                for (int a = 0; a < 2; ++a) {
                    f32x16 z = MFMA32(k0, qf[a][0], negm[a]); z = MFMA32(k1, qf[a][1], z);
                    u32x8 P;
#pragma unroll
                    for (int i = 0; i < 8; ++i) P[i] = cvtpk(ex2(z[2 * i]), ex2(z[2 * i + 1]));
                    o[a] = MFMA32(v0, PLO(P), o[a]); o[a] = MFMA32(v1, PHI(P), o[a]); lacc[a] = SMF(sel, P, lacc[a], selidx);
                }
            }
            l[0] = rowsum(0); l[1] = rowsum(1);
        }
        const srd_t sow = make_srd(ws + WS_O + (size_t)(b * 64 + mb * 8 + wm * 2) * 65536, 2 * 65536);
#pragma unroll
        for (int a = 0; a < 2; ++a) {
            const float rl = __builtin_amdgcn_rcpf(l[a]);
            __builtin_amdgcn_raw_buffer_store_b128(pack8(o[a], 0, rl), sow, lane * 16 + a * 65536 + h * 2048, 0, 16);
            __builtin_amdgcn_raw_buffer_store_b128(pack8(o[a], 1, rl), sow, lane * 16 + a * 65536 + h * 2048 + 1024, 0, 16);
        }
    }
    GAS u64* const ofl = flags + (FL_O / 8) + b * 1024;
    WAITV(0); if (lane == 0) st_agent(ofl + (mb * 4 + wm) * 32 + h, tag);
    STAMP_MID(4)

    {
        const int wt = wm;
        unsigned seen;
        SIGNAL(); lds_await(0, cntp, 64);
        for (int i = 0; i < 6; ++i) { dwu(2 * (3 + i) + wn, wo_hs(i), 0); dwu(2 * (3 + i) + wn, wo_hs(i), 1); }
        spin_flags(ofl + (tm * 2 + (wt >> 1)) * 32 + (lane & 15) * 2 + wn, tag);
        STAMP_MID(5)
        const srd_t sor = make_srd(ws + WS_O + (size_t)(b * 64 + tm * 4 + wt) * 65536 + wn * 2048, 65536 - 2048);
        bf16x8 of[8][2], wf[3][4];
#define P3_LO(u) do { of[(u) & 7][0] = BF(__builtin_amdgcn_raw_buffer_load_b128(sor, lane * 16, (u) * 4096, 16)); \
                      of[(u) & 7][1] = BF(__builtin_amdgcn_raw_buffer_load_b128(sor, lane * 16, (u) * 4096 + 1024, 16)); } while (0)
        P3_LO(0); P3_LO(1); P3_LO(2); P3_LO(3); P3_LO(4); P3_LO(5); P3_LO(6); P3_LO(7);
        SIGNAL();
        unsigned wn3[4];
#pragma unroll
        for (int n = 0; n < 4; ++n) wn3[n] = (4 * (g4 >> 1) + q4) * 256 + ((n ^ q4) << 6) + (g4 & 1) * 32 + p4 * 8;
        f32x16 acc[4];
#pragma unroll
        for (int n = 0; n < 4; ++n)
#pragma unroll
            for (int i = 0; i < 16; ++i) acc[n][i] = 0.f;
        auto uslot = [&](int u) { return u < 3 ? wo_rs(u) : u < 9 ? wo_hs(u - 3) : u < 12 ? wo_rs(u - 9) : wo_hs(u - 12); };
#define P3_RW(kk, n) do { const LAS char* w_ = lds + uslot((kk) >> 1) + ((kk) & 1) * 4096 + wn3[n]; wf[(kk) % 3][n] = cat8(ldstr(w_), ldstr(w_ + 2048)); } while (0)
#define P3_KS(kk, M0, M1) do { \
            acc[0] = MFMA32(wf[(kk) % 3][0], of[((kk) >> 1) & 7][(kk) & 1], acc[0]); if ((kk) + 2 < 32) P3_RW((kk) + 2, 0); SBAR(); \
            acc[1] = MFMA32(wf[(kk) % 3][1], of[((kk) >> 1) & 7][(kk) & 1], acc[1]); if ((kk) + 2 < 32) P3_RW((kk) + 2, 1); M0; SBAR(); \
            acc[2] = MFMA32(wf[(kk) % 3][2], of[((kk) >> 1) & 7][(kk) & 1], acc[2]); if ((kk) + 2 < 32) P3_RW((kk) + 2, 2); SBAR(); \
            acc[3] = MFMA32(wf[(kk) % 3][3], of[((kk) >> 1) & 7][(kk) & 1], acc[3]); if ((kk) + 2 < 32) P3_RW((kk) + 2, 3); M1; SBAR(); \
        } while (0)
#define P3_UNIT(u, M0) do { P3_KS(2 * (u), M0, NOP_); P3_KS(2 * (u) + 1, NOP_, if ((u) + 8 < 16) P3_LO((u) + 8)); } while (0)
#define DWU(u, slot) do { dwu(2 * (u) + wn, slot, 0); dwu(2 * (u) + wn, slot, 1); } while (0)
        P3_RW(0, 0); P3_RW(0, 1); P3_RW(0, 2); P3_RW(0, 3); P3_RW(1, 0); P3_RW(1, 1); P3_RW(1, 2); P3_RW(1, 3); SBAR();
        P3_UNIT(0, NOP_);
        P3_UNIT(1, PEEK);
        lds_await(seen, cntp, 68);
        SIGNAL();
        P3_UNIT(2, PEEK);
        lds_await(seen, cntp, 72); DWU(9, wo_rs(0)); DWU(10, wo_rs(1)); DWU(11, wo_rs(2));
        P3_UNIT(3, NOP_);
        P3_UNIT(4, NOP_);
        SIGNAL();
        P3_UNIT(5, PEEK);
        lds_await(seen, cntp, 76); DWU(12, wo_hs(0)); DWU(13, wo_hs(1)); DWU(14, wo_hs(2));
        P3_UNIT(6, NOP_);
        WAITV(14); SIGNAL();
        P3_UNIT(7, PEEK);
        lds_await(seen, cntp, 80);
        SIGNAL();
        P3_UNIT(8, PEEK);
        lds_await(seen, cntp, 84); DWU(15, wo_hs(3));
        P3_UNIT(9, NOP_);
        WAITV(6); SIGNAL();
        P3_UNIT(10, PEEK);
        lds_await(seen, cntp, 88);
        P3_UNIT(11, NOP_);
        P3_UNIT(12, NOP_);
        WAITV(0); SIGNAL();
        P3_UNIT(13, PEEK);
        lds_await(seen, cntp, 92);
        P3_UNIT(14, NOP_);
        P3_UNIT(15, NOP_);
        STAMP_MID(6)
        const int xoff = (wm < 3 ? (wm == 0 ? L_S0 : wm == 1 ? L_S1 : L_S2) : L_R0) + lane * 16;
        if (wn == 0) {
            SIGNAL(); lds_await(0, cntp, 96);
#pragma unroll
            for (int n = 0; n < 4; ++n)
#pragma unroll
                for (int qd = 0; qd < 4; ++qd)
                    *(LAS f32x4*)(lds + xoff + n * 4096 + qd * 1024) = (f32x4){acc[n][4 * qd], acc[n][4 * qd + 1], acc[n][4 * qd + 2], acc[n][4 * qd + 3]};
            SIGNAL();
        } else {
            lds_await(0, lds + L_CNT, 100);
            if (tid == 256 && (xcd & 3) == 0 && j == 0) st_agent(flags + b * 32, tag);
            const srd_t sout = make_srd(out, 2048 * 2048 * 2);
            LAS char* stg = lds + (wm < 3 ? wo_rs(wm) : wo_hs(0));
#pragma unroll
            for (int n = 0; n < 4; ++n) {
                f32x16 v = acc[n];
#pragma unroll
                for (int qd = 0; qd < 4; ++qd) {
                    const f32x4 t = *(const LAS f32x4*)(lds + xoff + n * 4096 + qd * 1024);
                    v[4 * qd] += t[0]; v[4 * qd + 1] += t[1]; v[4 * qd + 2] += t[2]; v[4 * qd + 3] += t[3];
                }
#pragma unroll
                for (int gp = 0; gp < 2; ++gp) {
                    const unsigned a0 = cvtpk(v[8 * gp], v[8 * gp + 1]), a1 = cvtpk(v[8 * gp + 2], v[8 * gp + 3]);
                    const unsigned b0 = cvtpk(v[8 * gp + 4], v[8 * gp + 5]), b1 = cvtpk(v[8 * gp + 6], v[8 * gp + 7]);
                    auto r0 = __builtin_amdgcn_permlane32_swap(a0, b0, false, false);
                    auto r1 = __builtin_amdgcn_permlane32_swap(a1, b1, false, false);
                    const u32x4 w = {r0[0], r1[0], r0[1], r1[1]};
                    *(LAS u32x4*)(stg + r32 * 256 + (((n * 4 + gp * 2 + hh) ^ (r32 & 15)) << 4)) = w;
                }
            }
            const unsigned oo = (unsigned)(tm * 128 + wt * 32 + g4) * 4096u + (unsigned)(b * 1024 + tn * 128) * 2u + (lane & 15) * 16;
#pragma unroll
            for (int i = 0; i < 8; ++i) {
                const int row = 4 * i + g4;
                const u32x4 w = *(const LAS u32x4*)(stg + row * 256 + (((lane & 15) ^ (row & 15)) << 4));
                __builtin_amdgcn_raw_buffer_store_b128(w, sout, oo + i * 4 * 4096, 0, 16);
            }
        }
    }
    STAMP_END(ws)
}

extern "C" void kernel_launch(void* const* d_in, const int* in_sizes, int n_in, void* d_out, int out_size, void* d_ws, size_t ws_size, hipStream_t stream) {
    (void)hipFuncSetAttribute((const void*)mha_fused, hipFuncAttributeMaxDynamicSharedMemorySize, L_END);
    mha_fused<<<256, 512, L_END, stream>>>((const uint16_t*)d_in[0], (const uint16_t*)d_in[1], (const uint16_t*)d_in[2], (const uint16_t*)d_in[3], (const uint16_t*)d_in[4],
                                           (const uint16_t*)d_in[5], (char*)d_ws, (uint16_t*)d_out);
}
```

```cpp
#define ROTX 4
#include <hip/hip_runtime.h>
#include <stdint.h>

typedef __bf16 bf16x8 __attribute__((ext_vector_type(8)));
typedef __bf16 bf16x16 __attribute__((ext_vector_type(16)));
typedef __bf16 bf16x2 __attribute__((ext_vector_type(2)));
typedef short s16x4 __attribute__((ext_vector_type(4)));
typedef short s16x8 __attribute__((ext_vector_type(8)));
typedef float f32x16 __attribute__((ext_vector_type(16)));
typedef float f32x4 __attribute__((ext_vector_type(4)));
typedef float f32x2 __attribute__((ext_vector_type(2)));
typedef unsigned u32x4 __attribute__((ext_vector_type(4)));
typedef unsigned u32x8 __attribute__((ext_vector_type(8)));
typedef unsigned long long u64;
#define LAS __attribute__((address_space(3)))
#define GAS __attribute__((address_space(1)))
#define DI __device__ __forceinline__
#define MFMA32(a, b, c) __builtin_amdgcn_mfma_f32_32x32x16_bf16((a), (b), (c), 0, 0, 0)
#define MFMA16(a, b, c) __builtin_amdgcn_mfma_f32_16x16x32_bf16((a), (b), (c), 0, 0, 0)
#define SMF(a, b, c, idx) __builtin_amdgcn_smfmac_f32_16x16x64_bf16((a), __builtin_bit_cast(bf16x16, b), (c), (idx), 0, 0)
#define PLO(P) __builtin_bit_cast(bf16x8, __builtin_shufflevector(P, P, 0, 1, 2, 3))
#define PHI(P) __builtin_bit_cast(bf16x8, __builtin_shufflevector(P, P, 4, 5, 6, 7))
#define SBAR() __builtin_amdgcn_sched_barrier(0)
#define BF(x) __builtin_bit_cast(bf16x8, x)
#define NOP_ (void)0
typedef __amdgpu_buffer_rsrc_t srd_t;

constexpr float QSCALE = 0.17677669529663687f * 1.4426950408889634f;
constexpr unsigned WS_KV = 0;
constexpr unsigned WS_O = 16u << 20;
constexpr unsigned WS_FL = 24u << 20;
constexpr unsigned FL_KV = 4096;
constexpr unsigned FL_O = FL_KV + 16384;
constexpr int L_S0 = 32768, L_S1 = 65536, L_S2 = 0, L_R0 = 98304, L_END = 163840, L_CNT = L_END - 64, L_DUMMY = 147456;
constexpr int P1_XS = 32768, P1_WS = 24576, P1_WOFF = 98304;

DI unsigned rfl(unsigned x) { return __builtin_amdgcn_readfirstlane(x); }
DI srd_t make_srd(const void* p, unsigned bytes) { return __builtin_amdgcn_make_buffer_rsrc((void*)p, (short)0, (int)bytes, 0x00020000); }
DI void dma16(unsigned ldsdst, unsigned voff, srd_t srd, unsigned soff) {
    asm volatile("s_mov_b32 m0, %0\n\ts_nop 0\n\tbuffer_load_dwordx4 %1, %2, %3 offen lds" :: "s"(ldsdst), "v"(voff), "s"(srd), "s"(soff) : "memory");
}
template <int I> DI void dma4c(unsigned ldsdst, unsigned voff, srd_t srd, unsigned soff) {
    if constexpr (I == 0) asm volatile("s_mov_b32 m0, %0\n\ts_nop 0\n\tbuffer_load_dwordx4 %1, %2, %3 offen sc1 lds" :: "s"(ldsdst), "v"(voff), "s"(srd), "s"(soff) : "memory");
    else asm volatile("buffer_load_dwordx4 %0, %1, %2 offen offset:%3 sc1 lds" :: "v"(voff), "s"(srd), "s"(soff), "i"(I * 1024) : "memory");
}
DI unsigned cvtpk(float lo, float hi) { f32x2 v = {lo, hi}; bf16x2 b = __builtin_convertvector(v, bf16x2); return __builtin_bit_cast(unsigned, b); }
DI bf16x8 ldsfrag(const LAS char* p) { return *(const LAS bf16x8*)p; }
typedef short v4i16_t __attribute__((ext_vector_type(4)));
DI s16x4 ldstr(const LAS char* p) { return __builtin_amdgcn_ds_read_tr16_b64_v4i16((LAS v4i16_t*)p); }
DI bf16x8 cat8(s16x4 lo, s16x4 hi) { s16x8 r = __builtin_shufflevector(lo, hi, 0, 1, 2, 3, 4, 5, 6, 7); return __builtin_bit_cast(bf16x8, r); }
DI u32x4 pack8(const f32x16& x, int s, float mul) {
    u32x4 p; p[0] = cvtpk(x[8 * s] * mul, x[8 * s + 1] * mul); p[1] = cvtpk(x[8 * s + 2] * mul, x[8 * s + 3] * mul);
    p[2] = cvtpk(x[8 * s + 4] * mul, x[8 * s + 5] * mul); p[3] = cvtpk(x[8 * s + 6] * mul, x[8 * s + 7] * mul); return p; }
#define WAITBAR(vm) asm volatile("s_waitcnt vmcnt(" #vm ") lgkmcnt(0)\n\ts_barrier" ::: "memory")
#define WAITV(vm) asm volatile("s_waitcnt vmcnt(" #vm ")" ::: "memory")
DI float ex2(float x) { return __builtin_amdgcn_exp2f(x); }
DI float halfmax(float m) { auto r = __builtin_amdgcn_permlane32_swap(__float_as_uint(m), __float_as_uint(m), false, false); return fmaxf(__uint_as_float(r[0]), __uint_as_float(r[1])); }
DI u64 mix64(u64 z) { z += 0x9E3779B97F4A7C15ull; z = (z ^ (z >> 30)) * 0xBF58476D1CE4E5B9ull; z = (z ^ (z >> 27)) * 0x94D049BB133111EBull; return z ^ (z >> 31); }
DI u64 ld_agent(const GAS u64* p) { return __hip_atomic_load(p, __ATOMIC_RELAXED, __HIP_MEMORY_SCOPE_AGENT); }
DI void st_agent(GAS u64* p, u64 v) { __hip_atomic_store(p, v, __ATOMIC_RELAXED, __HIP_MEMORY_SCOPE_AGENT); }
DI void spin_flags(const GAS u64* f, u64 tag) {
    const u64 t0 = __builtin_amdgcn_s_memrealtime();
    u64 v0 = ld_agent(f);
    for (;;) {
        __builtin_amdgcn_s_sleep(6);
        const u64 v1 = ld_agent(f);
        if (__all(v0 == tag)) break;
        __builtin_amdgcn_s_sleep(6);
        v0 = ld_agent(f);
        if (__all(v1 == tag)) break;
        if (__builtin_amdgcn_s_memrealtime() - t0 > 20000000ull) break;
    }
    asm volatile("" ::: "memory");
}
DI void lds_signal(unsigned addr) {
    unsigned one = 1;
    asm volatile("s_waitcnt lgkmcnt(0)\n\ts_mov_b64 exec, 1\n\tds_add_u32 %0, %1\n\ts_mov_b64 exec, -1" :: "v"(addr), "v"(one) : "memory");
}
DI unsigned lds_peek(const LAS char* p) { return *(const volatile LAS unsigned*)p; }
DI void lds_await(unsigned seen, const LAS char* p, unsigned need) {
    if (rfl(seen) < need) {
        _Pragma("nounroll") for (int it = 0; it < (1 << 22); ++it) { if (rfl(lds_peek(p)) >= need) break; __builtin_amdgcn_s_sleep(0); }
    }
    asm volatile("" ::: "memory");
}

#define STAMP_BEGIN
#define STAMP_MID(i)
#define STAMP_END(ws)
#define FLIP 56

__global__ __launch_bounds__(512, 2) void mha_fused(const uint16_t* xA, const uint16_t* wA, const uint16_t* woA, const uint16_t* xB, const uint16_t* wB, const uint16_t* woB,
                                                    char* ws, uint16_t* out) {
    extern __shared__ __attribute__((aligned(16))) char lds_[];
    LAS char* lds = (LAS char*)lds_;
    STAMP_BEGIN
    const int tid = threadIdx.x, lane = tid & 63, wid = rfl(tid >> 6);
    const int bid = blockIdx.x, xcd = bid & 7, j = bid >> 3;
    const int b = xcd >> 2;
    const int mb = (xcd & 1) * 4 + (j & 3), hp = ((xcd >> 1) & 1) * 8 + (j >> 2);
    const int wm = wid & 3, wn = wid >> 2, h = hp * 2 + wn;
    const unsigned lds0 = (unsigned)(uintptr_t)lds_;
    const int r32 = lane & 31, hh = lane >> 5, g4 = lane >> 4, q4 = (lane >> 2) & 3, p4 = lane & 3;
    GAS u64* const flags = (GAS u64*)(ws + WS_FL);
    u64 epoch = ld_agent(flags + b * 32);
    if (tid < 16) *(LAS unsigned*)(lds + L_CNT + tid * 4) = 0u;

    bf16x8 qf[2][2];
    u32x4 kvp[2][4];
    const srd_t skvw = make_srd(ws + WS_KV + (size_t)(b * 32 + h) * 64 * 4096 + (size_t)(mb * 8 + wm * 2) * 4096, 2 * 4096);
    {
        const srd_t sx = make_srd(b ? xB : xA, 2048 * 1024 * 2), sw = make_srd(b ? wB : wA, 1024 * 3072 * 2);
        const unsigned vx0 = (lane >> 3) * 2048 + (((lane & 7) ^ (lane >> 4)) << 4);
        const unsigned vx1 = (lane >> 3) * 2048 + (((lane & 7) ^ (4 + (lane >> 4))) << 4);
        const unsigned vw = (lane >> 3) * 6144 + (((lane & 7) ^ (((lane >> 4) & 1) << 2) ^ ((wid & 1) << 1)) << 4);
        const unsigned sx0 = (mb * 256 + wid * 32) * 2048, sw0 = wid * 8 * 6144 + hp * 128;
        const int rot = (xcd & 3) * ROTX;
        auto dx = [&](int kt, int i) { dma16(lds0 + (kt % 3) * P1_XS + wid * 4096 + i * 1024, (i & 1) ? vx1 : vx0, sx, sx0 + i * 8 * 2048 + ((kt + rot) & 15) * 128); };
        auto dw = [&](int kt, int i) { dma16(lds0 + P1_WOFF + (kt & 1) * P1_WS + wid * 1024 + i * 8192, vw, sw, sw0 + i * 2048 + ((kt + rot) & 15) * 64 * 6144); };
        for (int i = 0; i < 3; ++i) dw(0, i);
        for (int i = 0; i < 4; ++i) dx(0, i);
        for (int i = 0; i < 3; ++i) dw(1, i);
        for (int i = 0; i < 4; ++i) dx(1, i);
        for (int i = 0; i < 4; ++i) dx(2, i);
        constexpr int PFD = 4;
        const char* pfp; unsigned pfstep;
        {
            const int l = lane < 10 ? lane : 9;
            const int xrow = (xcd & 1) * 1024 + j * 32 + wid * 4 + (l & 3), wl = wid * 6 + (l - 4);
            const size_t woff = (size_t)(j * 2 + wl / 24) * 6144 + ((wl % 24) >> 3) * 2048 + (((xcd >> 1) & 1) * 8 + (wl & 7)) * 128;
            pfp = l < 4 ? (const char*)(b ? xB : xA) + (size_t)xrow * 2048 : (const char*)(b ? wB : wA) + woff;
            pfstep = l < 4 ? 128u : 64u * 6144u;
        }
        const unsigned pfdst = lds0 + L_DUMMY + wid * 1024;
        auto pf = [&](int kt) {
            const char* g = pfp + (size_t)((kt + rot) & 15) * pfstep;
            asm volatile("s_mov_b32 m0, %0\n\ts_nop 0\n\tglobal_load_lds_dword %1, off" :: "s"(pfdst), "v"(g) : "memory"); };
        pf(3);
#define XTRA 0
        const int r16 = lane & 15;
        unsigned xa[2], wb[2];
#pragma unroll
        for (int s = 0; s < 2; ++s) xa[s] = (wm * 64 + r16) * 128 + (((4 * s + g4) ^ (r16 >> 1)) << 4);
#pragma unroll
        for (int c = 0; c < 2; ++c) wb[c] = P1_WOFF + (8 * g4 + q4) * 128 + ((wn ^ (q4 >> 1)) << 6) + ((c ^ (g4 & 1)) << 5) + p4 * 8;
        f32x4 acc[4][6];
#pragma unroll
        for (int a = 0; a < 4; ++a)
#pragma unroll
            for (int c = 0; c < 6; ++c) acc[a][c] = (f32x4){0.f, 0.f, 0.f, 0.f};
        bf16x8 xf[2][4][2];
        s16x4 wlo[3][2], whi[3][2];
#define P1_RX(kt, tb, s) xf[(kt) & 1][tb][s] = ldsfrag(lds + ((kt) % 3) * P1_XS + xa[s] + (tb) * 2048)
#define P1_WL(kt, fb, s) wlo[(fb) % 3][s] = ldstr(lds + wb[(fb) & 1] + ((kt) & 1) * P1_WS + ((fb) >> 1) * 8192 + (s) * 4096)
#define P1_WH(kt, fb, s) whi[(fb) % 3][s] = ldstr(lds + wb[(fb) & 1] + ((kt) & 1) * P1_WS + ((fb) >> 1) * 8192 + (s) * 4096 + 512)
#define P1_M1(kt, tb, fb, s) do { const bf16x8 w_ = cat8(wlo[(fb) % 3][s], whi[(fb) % 3][s]); if ((fb) < 4) acc[tb][fb] = MFMA16(w_, xf[(kt) & 1][tb][s], acc[tb][fb]); else acc[tb][fb] = MFMA16(xf[(kt) & 1][tb][s], w_, acc[tb][fb]); } while (0)
#define P1_FB8(kt, fb, R0, R1, R2, R3, R4, R5, R6, R7) do { \
            P1_M1(kt, 0, fb, 0); R0; SBAR(); P1_M1(kt, 1, fb, 0); R1; SBAR(); P1_M1(kt, 2, fb, 0); R2; SBAR(); P1_M1(kt, 3, fb, 0); R3; SBAR(); \
            P1_M1(kt, 0, fb, 1); R4; SBAR(); P1_M1(kt, 1, fb, 1); R5; SBAR(); P1_M1(kt, 2, fb, 1); R6; SBAR(); P1_M1(kt, 3, fb, 1); R7; SBAR(); } while (0)
        WAITBAR(12);
        STAMP_MID(0)
        P1_RX(0, 0, 0); P1_WL(0, 0, 0); P1_WH(0, 0, 0); P1_RX(0, 1, 0); P1_RX(0, 2, 0); P1_RX(0, 3, 0); P1_WL(0, 0, 1); P1_WH(0, 0, 1);
        P1_RX(0, 0, 1); P1_RX(0, 1, 1); P1_RX(0, 2, 1); P1_RX(0, 3, 1); P1_WL(0, 1, 0); P1_WH(0, 1, 0); P1_WL(0, 1, 1); P1_WH(0, 1, 1); SBAR();
#pragma unroll
        for (int t = 0; t < 16; ++t) {
            const bool xs = (t >= 1 && t + 2 < 16);
            P1_FB8(t, 0, P1_WL(t, 2, 0), NOP_, P1_WH(t, 2, 0), NOP_, P1_WL(t, 2, 1), if (xs) dx(t + 2, 0), P1_WH(t, 2, 1), NOP_);
            P1_FB8(t, 1, P1_WL(t, 3, 0), NOP_, P1_WH(t, 3, 0), NOP_, P1_WL(t, 3, 1), if (xs) dx(t + 2, 1), P1_WH(t, 3, 1), NOP_);
            P1_FB8(t, 2, P1_WL(t, 4, 0), NOP_, P1_WH(t, 4, 0), NOP_, P1_WL(t, 4, 1), if (xs) dx(t + 2, 2), P1_WH(t, 4, 1), NOP_);
            P1_FB8(t, 3, P1_WL(t, 5, 0), NOP_, P1_WH(t, 5, 0), NOP_, P1_WL(t, 5, 1), if (xs) dx(t + 2, 3), P1_WH(t, 5, 1), NOP_);
            if (t < 15) {
                if (t == 0 && XTRA) WAITBAR(6); else if (t == 14) WAITBAR(0); else if (t - 1 + PFD < 16) WAITBAR(5); else WAITBAR(4);
                const bool wsch = (t + 2 < 16);
                P1_FB8(t, 4, P1_RX(t + 1, 0, 0), P1_WL(t + 1, 0, 0), P1_WH(t + 1, 0, 0), P1_RX(t + 1, 1, 0), P1_RX(t + 1, 2, 0), P1_RX(t + 1, 3, 0), P1_WL(t + 1, 0, 1), { P1_WH(t + 1, 0, 1); if (wsch) dw(t + 2, 0); });
                P1_FB8(t, 5, P1_RX(t + 1, 0, 1), P1_RX(t + 1, 1, 1), P1_RX(t + 1, 2, 1), { P1_RX(t + 1, 3, 1); if (wsch) dw(t + 2, 1); }, P1_WL(t + 1, 1, 0), { P1_WH(t + 1, 1, 0); if (wsch) dw(t + 2, 2); }, P1_WL(t + 1, 1, 1), { P1_WH(t + 1, 1, 1); if (t + PFD < 16) pf(t + PFD); });
            } else {
                P1_FB8(t, 4, NOP_, NOP_, NOP_, NOP_, NOP_, NOP_, NOP_, NOP_);
                P1_FB8(t, 5, NOP_, NOP_, NOP_, NOP_, NOP_, NOP_, NOP_, NOP_);
            }
        }
        lds_signal(lds0 + L_CNT + 32);
        STAMP_MID(1)
        auto swap16 = [](unsigned& x, unsigned& y) { auto r = __builtin_amdgcn_permlane16_swap(x, y, false, false); x = r[0]; y = r[1]; };
        auto swap32 = [](unsigned& x, unsigned& y) { auto r = __builtin_amdgcn_permlane32_swap(x, y, false, false); x = r[0]; y = r[1]; };
        auto qkfrag = [&](const f32x4& t0, const f32x4& t1, float mul) {
            unsigned a0 = cvtpk(t0[0] * mul, t0[1] * mul), a1 = cvtpk(t0[2] * mul, t0[3] * mul), b0 = cvtpk(t1[0] * mul, t1[1] * mul), b1 = cvtpk(t1[2] * mul, t1[3] * mul);
            swap16(a0, b0); swap16(a1, b1);
            return (u32x4){a0, a1, b0, b1}; };
        auto vfrag = [&](const f32x4& x0, const f32x4& x1) {
            unsigned a0 = cvtpk(x0[0], x0[1]), a1 = cvtpk(x0[2], x0[3]), b0 = cvtpk(x1[0], x1[1]), b1 = cvtpk(x1[2], x1[3]);
            swap16(a0, b0); swap16(a1, b1); swap32(a0, b0); swap32(a1, b1);
            return (u32x4){a0, a1, b0, b1}; };
#pragma unroll
        for (int a = 0; a < 2; ++a)
#pragma unroll
            for (int s = 0; s < 2; ++s) {
                kvp[a][s] = qkfrag(acc[2 * a][2 + s], acc[2 * a + 1][2 + s], 1.0f); kvp[a][2 + s] = vfrag(acc[2 * a + s][4], acc[2 * a + s][5]);
                __builtin_amdgcn_raw_buffer_store_b128(kvp[a][s], skvw, lane * 16 + a * 4096 + s * 1024, 0, 16);
                __builtin_amdgcn_raw_buffer_store_b128(kvp[a][2 + s], skvw, lane * 16 + a * 4096 + 2048 + s * 1024, 0, 16);
            }
#pragma unroll
        for (int a = 0; a < 2; ++a) {
            const int tl = wm * 2 + a;
            LAS char* lp = lds + L_S0 + (tl >> 2) * 32768 + wn * 16384 + (tl & 3) * 4096 + lane * 16;
#pragma unroll
            for (int f = 0; f < 4; ++f) *(LAS u32x4*)(lp + f * 1024) = kvp[a][f];
        }
        SBAR();
#pragma unroll
        for (int a = 0; a < 2; ++a)
#pragma unroll
            for (int s = 0; s < 2; ++s) qf[a][s] = BF(qkfrag(acc[2 * a][s], acc[2 * a + 1][s], QSCALE));
    }
    asm volatile("" : "+v"(epoch));
    const u64 tag = mix64(((u64)rfl((unsigned)(epoch >> 32)) << 32) | rfl((unsigned)epoch));
    STAMP_MID(2)

    const srd_t skv = make_srd(ws + WS_KV + (size_t)(b * 32 + hp * 2) * 64 * 4096, 2 * 64 * 4096);
    const srd_t swo = make_srd(b ? woB : woA, 1024 * 1024 * 2);
    const int tm = (xcd & 3) * 4 + (j >> 3), tn = j & 7;
    const unsigned vl = lane * 16;
    const unsigned vwo = (lane >> 4) * 2048 + (((lane & 15) ^ (((lane >> 4) & 3) << 2)) << 4);
    auto chunk_off = [&](int c) { const int i = (c - 2) >> 1; const int blk = i < mb ? i : i + 1; return (unsigned)((wn * 64 + blk * 8 + (c & 1) * 4 + wm) * 4096); };
    const unsigned ldw = lds0 + wid * 4096;
#define DKV(coff, slotoff, i) dma4c<i>(ldw + (slotoff), vl, skv, coff)
    const unsigned wo0 = wm * 8 * 2048 + tn * 256;
    auto dwu = [&](int head, unsigned slotaddr, int p) { dma16(lds0 + slotaddr + wm * 2048 + p * 1024, vwo, swo, wo0 + (head * 32 + p * 4) * 2048); };
    auto wo_rs = [&](int k) { return (unsigned)(L_R0 + wn * 24576 + k * 8192); };
    auto wo_hs = [&](int k) { return (unsigned)((k >> 1 == 0 ? L_S0 : k >> 1 == 1 ? L_S1 : L_S2) + wn * 16384 + (k & 1) * 8192); };
    bf16x8 sel; int selidx;
    { const int i16 = lane & 15; const short lo = (i16 < 2) ? (short)0x3F80 : (short)0, hi = (i16 == 2 || i16 == 3) ? (short)0x3F80 : (short)0;
      s16x8 t = {lo, lo, lo, lo, hi, hi, hi, hi}; sel = __builtin_bit_cast(bf16x8, t); selidx = (i16 & 1) ? 0xEEEE : 0x4444; }
    const LAS char* kvb = lds + wn * 16384 + lane * 16;
    GAS u64* const kvfl = flags + (FL_KV / 8) + (b * 32 + h) * 32;
    const GAS u64* const kvmine = kvfl + ((lane >> 1) & 7) * 4 + (lane & 1) * 2 + (wm >> 1);
    const LAS char* cntp = lds + L_CNT + wn * 16;
    const unsigned cnta = lds0 + L_CNT + wn * 16;
    f32x16 o[2]; f32x4 lacc[2];
    {
        f32x16 negm[2], S[2];
        bf16x8 kf[2][2], vf[2][2];
        u32x8 P;
        kf[0][0] = BF(kvp[0][0]); kf[0][1] = BF(kvp[0][1]); vf[0][0] = BF(kvp[0][2]); vf[0][1] = BF(kvp[0][3]);
#pragma unroll
        for (int a = 0; a < 2; ++a) {
            f32x16 z;
#pragma unroll
            for (int i = 0; i < 16; ++i) z[i] = 0.f;
            z = MFMA32(kf[0][0], qf[a][0], z); z = MFMA32(kf[0][1], qf[a][1], z);
            float m = z[0];
#pragma unroll
            for (int i = 1; i < 16; ++i) m = fmaxf(m, z[i]);
            m = halfmax(m);
#pragma unroll
            for (int i = 0; i < 16; ++i) { negm[a][i] = -m; o[a][i] = 0.f; }
            lacc[a] = (f32x4){0.f, 0.f, 0.f, 0.f};
        }
        S[0] = MFMA32(kf[0][0], qf[0][0], negm[0]); S[0] = MFMA32(kf[0][1], qf[0][1], S[0]);
        P = (u32x8){0u, 0u, 0u, 0u, 0u, 0u, 0u, 0u};
        bf16x8 vprev1 = vf[0][1];
        unsigned seen;
#define EXPI(X, i) X[i] = ex2(X[i])
#define UNIT(SX, SN, KA0, KA1, QB0, QB1, NEG, DOQK, OPREV, LPREV, VPREV1, OCUR, VCUR0, H0, H1, H2, H3) do { \
            if (DOQK) SN = MFMA32(KA0, QB0, NEG); H0; SBAR(); \
            EXPI(SX, 0); EXPI(SX, 1); EXPI(SX, 2); SBAR(); \
            LPREV = SMF(sel, P, LPREV, selidx); SBAR(); \
            EXPI(SX, 3); P[0] = cvtpk(SX[0], SX[1]); P[1] = cvtpk(SX[2], SX[3]); SBAR(); \
            OPREV = MFMA32(VPREV1, PHI(P), OPREV); H1; SBAR(); \
            EXPI(SX, 4); EXPI(SX, 5); EXPI(SX, 6); EXPI(SX, 7); P[2] = cvtpk(SX[4], SX[5]); P[3] = cvtpk(SX[6], SX[7]); SBAR(); \
            if (DOQK) SN = MFMA32(KA1, QB1, SN); H2; SBAR(); \
            EXPI(SX, 8); EXPI(SX, 9); EXPI(SX, 10); EXPI(SX, 11); P[4] = cvtpk(SX[8], SX[9]); P[5] = cvtpk(SX[10], SX[11]); SBAR(); \
            OCUR = MFMA32(VCUR0, PLO(P), OCUR); H3; SBAR(); \
            EXPI(SX, 12); EXPI(SX, 13); EXPI(SX, 14); EXPI(SX, 15); P[6] = cvtpk(SX[12], SX[13]); P[7] = cvtpk(SX[14], SX[15]); SBAR(); \
        } while (0)
#define TILEG(cur, nxt, LK, LV, LOAD, A2, A3, B0, B1, B2, B3) do { \
            UNIT(S[0], S[1], kf[cur][0], kf[cur][1], qf[1][0], qf[1][1], negm[1], true, o[1], lacc[1], vprev1, o[0], vf[cur][0], LK, LV, A2, A3); \
            UNIT(S[1], S[0], kf[nxt][0], kf[nxt][1], qf[0][0], qf[0][1], negm[0], LOAD, o[0], lacc[0], vf[cur][1], o[1], vf[cur][0], B0, B1, B2, B3); \
            vprev1 = vf[cur][1]; \
        } while (0)
#define TILEX(cur, nxt, rdp, LOAD, A2, A3, B0, B1, B2, B3) TILEG(cur, nxt, \
            if (LOAD) { kf[nxt][0] = ldsfrag(rdp); kf[nxt][1] = ldsfrag((rdp) + 1024); }, if (LOAD) { vf[nxt][0] = ldsfrag((rdp) + 2048); vf[nxt][1] = ldsfrag((rdp) + 3072); }, \
            LOAD, A2, A3, B0, B1, B2, B3)
#define TILE(cur, nxt, rdp, PK) TILEX(cur, nxt, rdp, true, NOP_, NOP_, NOP_, PK, NOP_, NOP_)
#define TILED(cur, nxt, rdp, coff, slot) TILEX(cur, nxt, rdp, true, DKV(coff, slot, 0), DKV(coff, slot, 1), NOP_, NOP_, DKV(coff, slot, 2), DKV(coff, slot, 3))
#define PEEK seen = lds_peek(cntp)
#define SIGNAL() lds_signal(cnta)
#define BALANCE(m) do { if (wn == 0 && (m) == FLIP) __builtin_amdgcn_s_setprio(0); } while (0)
        if (wn == 0) __builtin_amdgcn_s_setprio(2); else __builtin_amdgcn_s_setprio(1);
        auto own = [&](int p) {
            const int lo = wm < 2;
            const int off = lo ? (p < 4 ? L_S0 + ((1 - wm) * 2 + p - 2) * 4096 : L_S1 + (p - 4) * 4096) : (p < 6 ? L_S0 + (p - 2) * 4096 : L_S1 + ((3 - wm) * 2 + p - 6) * 4096);
            return kvb + off; };
        TILEG(0, 1, { kf[1][0] = BF(kvp[1][0]); kf[1][1] = BF(kvp[1][1]); }, { vf[1][0] = BF(kvp[1][2]); vf[1][1] = BF(kvp[1][3]); }, true, NOP_, NOP_, NOP_, NOP_, NOP_, NOP_);
        lds_signal(cnta + 4); lds_await(0, cntp + 4, 4);
        TILE(1, 0, own(2), NOP_);
        TILE(0, 1, own(3), NOP_);
        WAITV(0); if (lane == 0) st_agent(kvfl + mb * 4 + wm, tag);
        TILE(1, 0, own(4), NOP_);
        u64 pollv = ld_agent(kvmine);
        TILE(0, 1, own(5), NOP_);
        if (!__all(pollv == tag)) spin_flags(kvmine, tag);
        SIGNAL();
        lds_await(0, lds + L_CNT + 32, 8);
        unsigned cn = chunk_off(2);
        TILEX(1, 0, own(6), true, DKV(cn, L_S2, 0), DKV(cn, L_S2, 1), NOP_, PEEK, DKV(cn, L_S2, 2), DKV(cn, L_S2, 3));
        lds_await(seen, cntp, 4);
        cn = chunk_off(3);
        TILED(0, 1, own(7), cn, L_S0);
        WAITV(4); SIGNAL(); lds_await(0, cntp, 8);
        TILE(1, 0, kvb + L_S2, NOP_);
        cn = chunk_off(4);
        TILED(0, 1, kvb + L_S2 + 4096, cn, L_S1);
        WAITV(4); SIGNAL();
        TILE(1, 0, kvb + L_S2 + 8192, NOP_);
        TILE(0, 1, kvb + L_S2 + 12288, PEEK);
        lds_await(seen, cntp, 12);
        WAITV(0); SIGNAL();
        TILE(1, 0, kvb + L_S0, PEEK);
        STAMP_MID(7)
        int sc = L_S0, sn = L_S1, sp = L_S2;
        for (int m = 3; m <= 13; ++m) {
            const LAS char* cp = kvb + sc;
            cn = chunk_off(m + 2);
            lds_await(seen, cntp, 4 * (m + 1));
            BALANCE(4 * m);
            TILED(0, 1, cp + 4096, cn, sp);
            TILE(1, 0, cp + 8192, NOP_);
            TILE(0, 1, cp + 12288, NOP_);
            WAITV(0); SIGNAL();
            TILE(1, 0, kvb + sn, PEEK);
            const int t_ = sc; sc = sn; sn = sp; sp = t_;
        }
        lds_await(seen, cntp, 60);
        BALANCE(56);
        TILEX(0, 1, kvb + L_S2 + 4096, true, dwu(wn, wo_rs(0), 0), dwu(wn, wo_rs(0), 1), NOP_, NOP_, NOP_, NOP_);
        BALANCE(57);
        TILEX(1, 0, kvb + L_S2 + 8192, true, dwu(2 + wn, wo_rs(1), 0), dwu(2 + wn, wo_rs(1), 1), NOP_, NOP_, NOP_, NOP_);
        BALANCE(58);
        TILEX(0, 1, kvb + L_S2 + 12288, true, dwu(4 + wn, wo_rs(2), 0), dwu(4 + wn, wo_rs(2), 1), NOP_, NOP_, NOP_, NOP_);
        BALANCE(59);
        TILE(1, 0, kvb + L_S0, NOP_);
        BALANCE(60);
        TILE(0, 1, kvb + L_S0 + 4096, NOP_);
        BALANCE(61);
        TILE(1, 0, kvb + L_S0 + 8192, NOP_);
        BALANCE(62);
        TILE(0, 1, kvb + L_S0 + 12288, NOP_);
        TILEX(1, 0, kvb, false, NOP_, NOP_, NOP_, NOP_, NOP_, NOP_);
        lacc[1] = SMF(sel, P, lacc[1], selidx); o[1] = MFMA32(vprev1, PHI(P), o[1]);
    }
    if (wn == 0) __builtin_amdgcn_s_setprio(0);
    STAMP_MID(3)
    {
        float l[2];
        auto rowsum = [&](int a) { const float t0 = __shfl(lacc[a][0] + lacc[a][1], lane & 15), t1 = __shfl(lacc[a][2] + lacc[a][3], lane & 15); return (lane & 16) ? t1 : t0; };
        bool bad = false;
#pragma unroll
        for (int a = 0; a < 2; ++a) {
            l[a] = rowsum(a);
            float z = l[a] * 0.f, z1 = 0.f;
#pragma unroll
            for (int i = 0; i < 16; i += 2) { z = __builtin_fmaf(o[a][i], 0.f, z); z1 = __builtin_fmaf(o[a][i + 1], 0.f, z1); }
            bad |= !(l[a] < 1.0e36f) || !(z + z1 == 0.f);
        }
        if (__any(bad)) {
            spin_flags(kvfl + (lane & 31), tag);
            const srd_t sk1 = make_srd(ws + WS_KV + (size_t)(b * 32 + h) * 64 * 4096, 64 * 4096);
#define LDKV(t, f) BF(__builtin_amdgcn_raw_buffer_load_b128(sk1, lane * 16 + (f) * 1024, (t) * 4096, 16))
            f32x16 negm[2];
#pragma unroll
            for (int a = 0; a < 2; ++a) {
                float m = -__builtin_inff();
                for (int t = 0; t < 64; ++t) {
                    const bf16x8 k0 = LDKV(t, 0), k1 = LDKV(t, 1);
                    f32x16 z;
#pragma unroll
                    for (int i = 0; i < 16; ++i) z[i] = 0.f;
                    z = MFMA32(k0, qf[a][0], z); z = MFMA32(k1, qf[a][1], z);
#pragma unroll
                    for (int i = 0; i < 16; ++i) m = fmaxf(m, z[i]);
                }
                m = halfmax(m);
#pragma unroll
                for (int i = 0; i < 16; ++i) { negm[a][i] = -m; o[a][i] = 0.f; }
                lacc[a] = (f32x4){0.f, 0.f, 0.f, 0.f};
            }
            for (int t = 0; t < 64; ++t) {
                const bf16x8 k0 = LDKV(t, 0), k1 = LDKV(t, 1), v0 = LDKV(t, 2), v1 = LDKV(t, 3);
#pragma unroll
                for (int a = 0; a < 2; ++a) {
                    f32x16 z = MFMA32(k0, qf[a][0], negm[a]); z = MFMA32(k1, qf[a][1], z);
                    u32x8 P;
#pragma unroll
                    for (int i = 0; i < 8; ++i) P[i] = cvtpk(ex2(z[2 * i]), ex2(z[2 * i + 1]));
                    o[a] = MFMA32(v0, PLO(P), o[a]); o[a] = MFMA32(v1, PHI(P), o[a]); lacc[a] = SMF(sel, P, lacc[a], selidx);
                }
            }
            l[0] = rowsum(0); l[1] = rowsum(1);
        }
        const srd_t sow = make_srd(ws + WS_O + (size_t)(b * 64 + mb * 8 + wm * 2) * 65536, 2 * 65536);
#pragma unroll
        for (int a = 0; a < 2; ++a) {
            const float rl = __builtin_amdgcn_rcpf(l[a]);
            __builtin_amdgcn_raw_buffer_store_b128(pack8(o[a], 0, rl), sow, lane * 16 + a * 65536 + h * 2048, 0, 16);
            __builtin_amdgcn_raw_buffer_store_b128(pack8(o[a], 1, rl), sow, lane * 16 + a * 65536 + h * 2048 + 1024, 0, 16);
        }
    }
    GAS u64* const ofl = flags + (FL_O / 8) + b * 1024;
    WAITV(0); if (lane == 0) st_agent(ofl + (mb * 4 + wm) * 32 + h, tag);
    STAMP_MID(4)

    {
        const int wt = wm;
        unsigned seen;
        SIGNAL(); lds_await(0, cntp, 64);
        for (int i = 0; i < 6; ++i) { dwu(2 * (3 + i) + wn, wo_hs(i), 0); dwu(2 * (3 + i) + wn, wo_hs(i), 1); }
        spin_flags(ofl + (tm * 2 + (wt >> 1)) * 32 + (lane & 15) * 2 + wn, tag);
        STAMP_MID(5)
        const srd_t sor = make_srd(ws + WS_O + (size_t)(b * 64 + tm * 4 + wt) * 65536 + wn * 2048, 65536 - 2048);
        bf16x8 of[8][2], wf[3][4];
#define P3_LO(u) do { of[(u) & 7][0] = BF(__builtin_amdgcn_raw_buffer_load_b128(sor, lane * 16, (u) * 4096, 16)); \
                      of[(u) & 7][1] = BF(__builtin_amdgcn_raw_buffer_load_b128(sor, lane * 16, (u) * 4096 + 1024, 16)); } while (0)
        P3_LO(0); P3_LO(1); P3_LO(2); P3_LO(3); P3_LO(4); P3_LO(5); P3_LO(6); P3_LO(7);
        SIGNAL();
        unsigned wn3[4];
#pragma unroll
        for (int n = 0; n < 4; ++n) wn3[n] = (4 * (g4 >> 1) + q4) * 256 + ((n ^ q4) << 6) + (g4 & 1) * 32 + p4 * 8;
        f32x16 acc[4];
#pragma unroll
        for (int n = 0; n < 4; ++n)
#pragma unroll
            for (int i = 0; i < 16; ++i) acc[n][i] = 0.f;
        auto uslot = [&](int u) { return u < 3 ? wo_rs(u) : u < 9 ? wo_hs(u - 3) : u < 12 ? wo_rs(u - 9) : wo_hs(u - 12); };
#define P3_RW(kk, n) do { const LAS char* w_ = lds + uslot((kk) >> 1) + ((kk) & 1) * 4096 + wn3[n]; wf[(kk) % 3][n] = cat8(ldstr(w_), ldstr(w_ + 2048)); } while (0)
#define P3_KS(kk, M0, M1) do { \
            acc[0] = MFMA32(wf[(kk) % 3][0], of[((kk) >> 1) & 7][(kk) & 1], acc[0]); if ((kk) + 2 < 32) P3_RW((kk) + 2, 0); SBAR(); \
            acc[1] = MFMA32(wf[(kk) % 3][1], of[((kk) >> 1) & 7][(kk) & 1], acc[1]); if ((kk) + 2 < 32) P3_RW((kk) + 2, 1); M0; SBAR(); \
            acc[2] = MFMA32(wf[(kk) % 3][2], of[((kk) >> 1) & 7][(kk) & 1], acc[2]); if ((kk) + 2 < 32) P3_RW((kk) + 2, 2); SBAR(); \
            acc[3] = MFMA32(wf[(kk) % 3][3], of[((kk) >> 1) & 7][(kk) & 1], acc[3]); if ((kk) + 2 < 32) P3_RW((kk) + 2, 3); M1; SBAR(); \
        } while (0)
#define P3_UNIT(u, M0) do { P3_KS(2 * (u), M0, NOP_); P3_KS(2 * (u) + 1, NOP_, if ((u) + 8 < 16) P3_LO((u) + 8)); } while (0)
#define DWU(u, slot) do { dwu(2 * (u) + wn, slot, 0); dwu(2 * (u) + wn, slot, 1); } while (0)
        P3_RW(0, 0); P3_RW(0, 1); P3_RW(0, 2); P3_RW(0, 3); P3_RW(1, 0); P3_RW(1, 1); P3_RW(1, 2); P3_RW(1, 3); SBAR();
        P3_UNIT(0, NOP_);
        P3_UNIT(1, PEEK);
        lds_await(seen, cntp, 68);
        SIGNAL();
        P3_UNIT(2, PEEK);
        lds_await(seen, cntp, 72); DWU(9, wo_rs(0)); DWU(10, wo_rs(1)); DWU(11, wo_rs(2));
        P3_UNIT(3, NOP_);
        P3_UNIT(4, NOP_);
        SIGNAL();
        P3_UNIT(5, PEEK);
        lds_await(seen, cntp, 76); DWU(12, wo_hs(0)); DWU(13, wo_hs(1)); DWU(14, wo_hs(2));
        P3_UNIT(6, NOP_);
        WAITV(14); SIGNAL();
        P3_UNIT(7, PEEK);
        lds_await(seen, cntp, 80);
        SIGNAL();
        P3_UNIT(8, PEEK);
        lds_await(seen, cntp, 84); DWU(15, wo_hs(3));
        P3_UNIT(9, NOP_);
        WAITV(6); SIGNAL();
        P3_UNIT(10, PEEK);
        lds_await(seen, cntp, 88);
        P3_UNIT(11, NOP_);
        P3_UNIT(12, NOP_);
        WAITV(0); SIGNAL();
        P3_UNIT(13, PEEK);
        lds_await(seen, cntp, 92);
        P3_UNIT(14, NOP_);
        P3_UNIT(15, NOP_);
        STAMP_MID(6)
        const int xoff = (wm < 3 ? (wm == 0 ? L_S0 : wm == 1 ? L_S1 : L_S2) : L_R0) + lane * 16;
        if (wn == 0) {
            SIGNAL(); lds_await(0, cntp, 96);
#pragma unroll
            for (int n = 0; n < 4; ++n)
#pragma unroll
                for (int qd = 0; qd < 4; ++qd)
                    *(LAS f32x4*)(lds + xoff + n * 4096 + qd * 1024) = (f32x4){acc[n][4 * qd], acc[n][4 * qd + 1], acc[n][4 * qd + 2], acc[n][4 * qd + 3]};
            SIGNAL();
        } else {
            lds_await(0, lds + L_CNT, 100);
            if (tid == 256 && (xcd & 3) == 0 && j == 0) st_agent(flags + b * 32, tag);
            const srd_t sout = make_srd(out, 2048 * 2048 * 2);
            LAS char* stg = lds + (wm < 3 ? wo_rs(wm) : wo_hs(0));
#pragma unroll
            for (int n = 0; n < 4; ++n) {
                f32x16 v = acc[n];
#pragma unroll
                for (int qd = 0; qd < 4; ++qd) {
                    const f32x4 t = *(const LAS f32x4*)(lds + xoff + n * 4096 + qd * 1024);
                    v[4 * qd] += t[0]; v[4 * qd + 1] += t[1]; v[4 * qd + 2] += t[2]; v[4 * qd + 3] += t[3];
                }
#pragma unroll
                for (int gp = 0; gp < 2; ++gp) {
                    const unsigned a0 = cvtpk(v[8 * gp], v[8 * gp + 1]), a1 = cvtpk(v[8 * gp + 2], v[8 * gp + 3]);
                    const unsigned b0 = cvtpk(v[8 * gp + 4], v[8 * gp + 5]), b1 = cvtpk(v[8 * gp + 6], v[8 * gp + 7]);
                    auto r0 = __builtin_amdgcn_permlane32_swap(a0, b0, false, false);
                    auto r1 = __builtin_amdgcn_permlane32_swap(a1, b1, false, false);
                    const u32x4 w = {r0[0], r1[0], r0[1], r1[1]};
                    *(LAS u32x4*)(stg + r32 * 256 + (((n * 4 + gp * 2 + hh) ^ (r32 & 15)) << 4)) = w;
                }
            }
            const unsigned oo = (unsigned)(tm * 128 + wt * 32 + g4) * 4096u + (unsigned)(b * 1024 + tn * 128) * 2u + (lane & 15) * 16;
#pragma unroll
            for (int i = 0; i < 8; ++i) {
                const int row = 4 * i + g4;
                const u32x4 w = *(const LAS u32x4*)(stg + row * 256 + (((lane & 15) ^ (row & 15)) << 4));
                __builtin_amdgcn_raw_buffer_store_b128(w, sout, oo + i * 4 * 4096, 0, 16);
            }
        }
    }
    STAMP_END(ws)
}

extern "C" void kernel_launch(void* const* d_in, const int* in_sizes, int n_in, void* d_out, int out_size, void* d_ws, size_t ws_size, hipStream_t stream) {
    (void)hipFuncSetAttribute((const void*)mha_fused, hipFuncAttributeMaxDynamicSharedMemorySize, L_END);
    mha_fused<<<256, 512, L_END, stream>>>((const uint16_t*)d_in[0], (const uint16_t*)d_in[1], (const uint16_t*)d_in[2], (const uint16_t*)d_in[3], (const uint16_t*)d_in[4],
                                           (const uint16_t*)d_in[5], (char*)d_ws, (uint16_t*)d_out);
}
```

```cpp
#include <hip/hip_runtime.h>
#include <stdint.h>

typedef __bf16 bf16x8 __attribute__((ext_vector_type(8)));
typedef __bf16 bf16x16 __attribute__((ext_vector_type(16)));
typedef __bf16 bf16x2 __attribute__((ext_vector_type(2)));
typedef short s16x4 __attribute__((ext_vector_type(4)));
typedef short s16x8 __attribute__((ext_vector_type(8)));
typedef float f32x16 __attribute__((ext_vector_type(16)));
typedef float f32x4 __attribute__((ext_vector_type(4)));
typedef float f32x2 __attribute__((ext_vector_type(2)));
typedef unsigned u32x4 __attribute__((ext_vector_type(4)));
typedef unsigned u32x8 __attribute__((ext_vector_type(8)));
typedef unsigned long long u64;
#define LAS __attribute__((address_space(3)))
#define GAS __attribute__((address_space(1)))
#define DI __device__ __forceinline__
#define MFMA32(a, b, c) __builtin_amdgcn_mfma_f32_32x32x16_bf16((a), (b), (c), 0, 0, 0)
#define MFMA16(a, b, c) __builtin_amdgcn_mfma_f32_16x16x32_bf16((a), (b), (c), 0, 0, 0)
#define SMF(a, b, c, idx) __builtin_amdgcn_smfmac_f32_16x16x64_bf16((a), __builtin_bit_cast(bf16x16, b), (c), (idx), 0, 0)
#define PLO(P) __builtin_bit_cast(bf16x8, __builtin_shufflevector(P, P, 0, 1, 2, 3))
#define PHI(P) __builtin_bit_cast(bf16x8, __builtin_shufflevector(P, P, 4, 5, 6, 7))
#define SBAR() __builtin_amdgcn_sched_barrier(0)
#define BF(x) __builtin_bit_cast(bf16x8, x)
#define NOP_ (void)0
typedef __amdgpu_buffer_rsrc_t srd_t;

constexpr float QSCALE = 0.17677669529663687f * 1.4426950408889634f;
constexpr unsigned WS_KV = 0;
constexpr unsigned WS_O = 16u << 20;
constexpr unsigned WS_FL = 24u << 20;
constexpr unsigned FL_KV = 4096;
constexpr unsigned FL_O = FL_KV + 16384;
constexpr int L_S0 = 32768, L_S1 = 65536, L_S2 = 0, L_R0 = 98304, L_END = 163840, L_CNT = L_END - 64, L_DUMMY = 147456;
constexpr int P1_XS = 32768, P1_WS = 24576, P1_WOFF = 98304;

DI unsigned rfl(unsigned x) { return __builtin_amdgcn_readfirstlane(x); }
DI srd_t make_srd(const void* p, unsigned bytes) { return __builtin_amdgcn_make_buffer_rsrc((void*)p, (short)0, (int)bytes, 0x00020000); }
DI void dma16(unsigned ldsdst, unsigned voff, srd_t srd, unsigned soff) {
    asm volatile("s_mov_b32 m0, %0\n\ts_nop 0\n\tbuffer_load_dwordx4 %1, %2, %3 offen lds" :: "s"(ldsdst), "v"(voff), "s"(srd), "s"(soff) : "memory");
}
template <int I> DI void dma4c(unsigned ldsdst, unsigned voff, srd_t srd, unsigned soff) {
    if constexpr (I == 0) asm volatile("s_mov_b32 m0, %0\n\ts_nop 0\n\tbuffer_load_dwordx4 %1, %2, %3 offen sc1 lds" :: "s"(ldsdst), "v"(voff), "s"(srd), "s"(soff) : "memory");
    else asm volatile("buffer_load_dwordx4 %0, %1, %2 offen offset:%3 sc1 lds" :: "v"(voff), "s"(srd), "s"(soff), "i"(I * 1024) : "memory");
}
DI unsigned cvtpk(float lo, float hi) { f32x2 v = {lo, hi}; bf16x2 b = __builtin_convertvector(v, bf16x2); return __builtin_bit_cast(unsigned, b); }
DI bf16x8 ldsfrag(const LAS char* p) { return *(const LAS bf16x8*)p; }
typedef short v4i16_t __attribute__((ext_vector_type(4)));
DI s16x4 ldstr(const LAS char* p) { return __builtin_amdgcn_ds_read_tr16_b64_v4i16((LAS v4i16_t*)p); }
DI bf16x8 cat8(s16x4 lo, s16x4 hi) { s16x8 r = __builtin_shufflevector(lo, hi, 0, 1, 2, 3, 4, 5, 6, 7); return __builtin_bit_cast(bf16x8, r); }
DI u32x4 pack8(const f32x16& x, int s, float mul) {
    u32x4 p; p[0] = cvtpk(x[8 * s] * mul, x[8 * s + 1] * mul); p[1] = cvtpk(x[8 * s + 2] * mul, x[8 * s + 3] * mul);
    p[2] = cvtpk(x[8 * s + 4] * mul, x[8 * s + 5] * mul); p[3] = cvtpk(x[8 * s + 6] * mul, x[8 * s + 7] * mul); return p; }
#define WAITBAR(vm) asm volatile("s_waitcnt vmcnt(" #vm ") lgkmcnt(0)\n\ts_barrier" ::: "memory")
#define WAITV(vm) asm volatile("s_waitcnt vmcnt(" #vm ")" ::: "memory")
DI float ex2(float x) { return __builtin_amdgcn_exp2f(x); }
DI float halfmax(float m) { auto r = __builtin_amdgcn_permlane32_swap(__float_as_uint(m), __float_as_uint(m), false, false); return fmaxf(__uint_as_float(r[0]), __uint_as_float(r[1])); }
DI u64 mix64(u64 z) { z += 0x9E3779B97F4A7C15ull; z = (z ^ (z >> 30)) * 0xBF58476D1CE4E5B9ull; z = (z ^ (z >> 27)) * 0x94D049BB133111EBull; return z ^ (z >> 31); }
DI u64 ld_agent(const GAS u64* p) { return __hip_atomic_load(p, __ATOMIC_RELAXED, __HIP_MEMORY_SCOPE_AGENT); }
DI void st_agent(GAS u64* p, u64 v) { __hip_atomic_store(p, v, __ATOMIC_RELAXED, __HIP_MEMORY_SCOPE_AGENT); }
DI void spin_flags(const GAS u64* f, u64 tag) {
    const u64 t0 = __builtin_amdgcn_s_memrealtime();
    u64 v0 = ld_agent(f);
    for (;;) {
        __builtin_amdgcn_s_sleep(6);
        const u64 v1 = ld_agent(f);
        if (__all(v0 == tag)) break;
        __builtin_amdgcn_s_sleep(6);
        v0 = ld_agent(f);
        if (__all(v1 == tag)) break;
        if (__builtin_amdgcn_s_memrealtime() - t0 > 20000000ull) break;
    }
    asm volatile("" ::: "memory");
}
DI void lds_signal(unsigned addr) {
    unsigned one = 1;
    asm volatile("s_waitcnt lgkmcnt(0)\n\ts_mov_b64 exec, 1\n\tds_add_u32 %0, %1\n\ts_mov_b64 exec, -1" :: "v"(addr), "v"(one) : "memory");
}
DI unsigned lds_peek(const LAS char* p) { return *(const volatile LAS unsigned*)p; }
DI void lds_await(unsigned seen, const LAS char* p, unsigned need) {
    if (rfl(seen) < need) {
        _Pragma("nounroll") for (int it = 0; it < (1 << 22); ++it) { if (rfl(lds_peek(p)) >= need) break; __builtin_amdgcn_s_sleep(0); }
    }
    asm volatile("" ::: "memory");
}

constexpr int FLIP = 56;

__global__ __launch_bounds__(512, 2) void mha_fused(const uint16_t* xA, const uint16_t* wA, const uint16_t* woA, const uint16_t* xB, const uint16_t* wB, const uint16_t* woB,
                                                    char* ws, uint16_t* out) {
    extern __shared__ __attribute__((aligned(16))) char lds_[];
    LAS char* lds = (LAS char*)lds_;
    const int tid = threadIdx.x, lane = tid & 63, wid = rfl(tid >> 6);
    const int bid = blockIdx.x, xcd = bid & 7, j = bid >> 3;
    const int b = xcd >> 2;
    const int mb = (xcd & 1) * 4 + (j & 3), hp = ((xcd >> 1) & 1) * 8 + (j >> 2);
    const int wm = wid & 3, wn = wid >> 2, h = hp * 2 + wn;
    const unsigned lds0 = (unsigned)(uintptr_t)lds_;
    const int r32 = lane & 31, hh = lane >> 5, g4 = lane >> 4, q4 = (lane >> 2) & 3, p4 = lane & 3;
    GAS u64* const flags = (GAS u64*)(ws + WS_FL);
    u64 epoch = ld_agent(flags + b * 32);
    if (tid < 16) *(LAS unsigned*)(lds + L_CNT + tid * 4) = 0u;

    bf16x8 qf[2][2];
    u32x4 kvp[2][4];
    const srd_t skvw = make_srd(ws + WS_KV + (size_t)(b * 32 + h) * 64 * 4096 + (size_t)(mb * 8 + wm * 2) * 4096, 2 * 4096);
    {
        const srd_t sx = make_srd(b ? xB : xA, 2048 * 1024 * 2), sw = make_srd(b ? wB : wA, 1024 * 3072 * 2);
        const unsigned vx0 = (lane >> 3) * 2048 + (((lane & 7) ^ (lane >> 4)) << 4);
        const unsigned vx1 = (lane >> 3) * 2048 + (((lane & 7) ^ (4 + (lane >> 4))) << 4);
        const unsigned vw = (lane >> 3) * 6144 + (((lane & 7) ^ (((lane >> 4) & 1) << 2) ^ ((wid & 1) << 1)) << 4);
        const unsigned sx0 = (mb * 256 + wid * 32) * 2048, sw0 = wid * 8 * 6144 + hp * 128;
        const int rot = (xcd & 3) * 4;
        auto dx = [&](int kt, int i) { dma16(lds0 + (kt % 3) * P1_XS + wid * 4096 + i * 1024, (i & 1) ? vx1 : vx0, sx, sx0 + i * 8 * 2048 + ((kt + rot) & 15) * 128); };
        auto dw = [&](int kt, int i) { dma16(lds0 + P1_WOFF + (kt & 1) * P1_WS + wid * 1024 + i * 8192, vw, sw, sw0 + i * 2048 + ((kt + rot) & 15) * 64 * 6144); };
        for (int i = 0; i < 3; ++i) dw(0, i);
        for (int i = 0; i < 4; ++i) dx(0, i);
        for (int i = 0; i < 3; ++i) dw(1, i);
        for (int i = 0; i < 4; ++i) dx(1, i);
        for (int i = 0; i < 4; ++i) dx(2, i);
        constexpr int PFD = 4;
        const char* pfp; unsigned pfstep;
        {
            const int l = lane < 10 ? lane : 9;
            const int xrow = (xcd & 1) * 1024 + j * 32 + wid * 4 + (l & 3), wl = wid * 6 + (l - 4);
            const size_t woff = (size_t)(j * 2 + wl / 24) * 6144 + ((wl % 24) >> 3) * 2048 + (((xcd >> 1) & 1) * 8 + (wl & 7)) * 128;
            pfp = l < 4 ? (const char*)(b ? xB : xA) + (size_t)xrow * 2048 : (const char*)(b ? wB : wA) + woff;
            pfstep = l < 4 ? 128u : 64u * 6144u;
        }
        const unsigned pfdst = lds0 + L_DUMMY + wid * 1024;
        auto pf = [&](int kt) {
            const char* g = pfp + (size_t)((kt + rot) & 15) * pfstep;
            asm volatile("s_mov_b32 m0, %0\n\ts_nop 0\n\tglobal_load_lds_dword %1, off" :: "s"(pfdst), "v"(g) : "memory"); };
        pf(3);
        const int r16 = lane & 15;
        unsigned xa[2], wb[2];
#pragma unroll
        for (int s = 0; s < 2; ++s) xa[s] = (wm * 64 + r16) * 128 + (((4 * s + g4) ^ (r16 >> 1)) << 4);
#pragma unroll
        for (int c = 0; c < 2; ++c) wb[c] = P1_WOFF + (8 * g4 + q4) * 128 + ((wn ^ (q4 >> 1)) << 6) + ((c ^ (g4 & 1)) << 5) + p4 * 8;
        f32x4 acc[4][6];
#pragma unroll
        for (int a = 0; a < 4; ++a)
#pragma unroll
            for (int c = 0; c < 6; ++c) acc[a][c] = (f32x4){0.f, 0.f, 0.f, 0.f};
        bf16x8 xf[2][4][2];
        s16x4 wlo[3][2], whi[3][2];
#define P1_RX(kt, tb, s) xf[(kt) & 1][tb][s] = ldsfrag(lds + ((kt) % 3) * P1_XS + xa[s] + (tb) * 2048)
#define P1_WL(kt, fb, s) wlo[(fb) % 3][s] = ldstr(lds + wb[(fb) & 1] + ((kt) & 1) * P1_WS + ((fb) >> 1) * 8192 + (s) * 4096)
#define P1_WH(kt, fb, s) whi[(fb) % 3][s] = ldstr(lds + wb[(fb) & 1] + ((kt) & 1) * P1_WS + ((fb) >> 1) * 8192 + (s) * 4096 + 512)
#define P1_M1(kt, tb, fb, s) do { const bf16x8 w_ = cat8(wlo[(fb) % 3][s], whi[(fb) % 3][s]); if ((fb) < 4) acc[tb][fb] = MFMA16(w_, xf[(kt) & 1][tb][s], acc[tb][fb]); else acc[tb][fb] = MFMA16(xf[(kt) & 1][tb][s], w_, acc[tb][fb]); } while (0)
#define P1_FB8(kt, fb, R0, R1, R2, R3, R4, R5, R6, R7) do { \
            P1_M1(kt, 0, fb, 0); R0; SBAR(); P1_M1(kt, 1, fb, 0); R1; SBAR(); P1_M1(kt, 2, fb, 0); R2; SBAR(); P1_M1(kt, 3, fb, 0); R3; SBAR(); \
            P1_M1(kt, 0, fb, 1); R4; SBAR(); P1_M1(kt, 1, fb, 1); R5; SBAR(); P1_M1(kt, 2, fb, 1); R6; SBAR(); P1_M1(kt, 3, fb, 1); R7; SBAR(); } while (0)
        WAITBAR(12);
        P1_RX(0, 0, 0); P1_WL(0, 0, 0); P1_WH(0, 0, 0); P1_RX(0, 1, 0); P1_RX(0, 2, 0); P1_RX(0, 3, 0); P1_WL(0, 0, 1); P1_WH(0, 0, 1);
        P1_RX(0, 0, 1); P1_RX(0, 1, 1); P1_RX(0, 2, 1); P1_RX(0, 3, 1); P1_WL(0, 1, 0); P1_WH(0, 1, 0); P1_WL(0, 1, 1); P1_WH(0, 1, 1); SBAR();
#pragma unroll
        for (int t = 0; t < 16; ++t) {
            const bool xs = (t >= 1 && t + 2 < 16);
            P1_FB8(t, 0, P1_WL(t, 2, 0), NOP_, P1_WH(t, 2, 0), NOP_, P1_WL(t, 2, 1), if (xs) dx(t + 2, 0), P1_WH(t, 2, 1), NOP_);
            P1_FB8(t, 1, P1_WL(t, 3, 0), NOP_, P1_WH(t, 3, 0), NOP_, P1_WL(t, 3, 1), if (xs) dx(t + 2, 1), P1_WH(t, 3, 1), NOP_);
            P1_FB8(t, 2, P1_WL(t, 4, 0), NOP_, P1_WH(t, 4, 0), NOP_, P1_WL(t, 4, 1), if (xs) dx(t + 2, 2), P1_WH(t, 4, 1), NOP_);
            P1_FB8(t, 3, P1_WL(t, 5, 0), NOP_, P1_WH(t, 5, 0), NOP_, P1_WL(t, 5, 1), if (xs) dx(t + 2, 3), P1_WH(t, 5, 1), NOP_);
            if (t < 15) {
                if (t == 14) WAITBAR(0); else if (t - 1 + PFD < 16) WAITBAR(5); else WAITBAR(4);
                const bool wsch = (t + 2 < 16);
                P1_FB8(t, 4, P1_RX(t + 1, 0, 0), P1_WL(t + 1, 0, 0), P1_WH(t + 1, 0, 0), P1_RX(t + 1, 1, 0), P1_RX(t + 1, 2, 0), P1_RX(t + 1, 3, 0), P1_WL(t + 1, 0, 1), { P1_WH(t + 1, 0, 1); if (wsch) dw(t + 2, 0); });
                P1_FB8(t, 5, P1_RX(t + 1, 0, 1), P1_RX(t + 1, 1, 1), P1_RX(t + 1, 2, 1), { P1_RX(t + 1, 3, 1); if (wsch) dw(t + 2, 1); }, P1_WL(t + 1, 1, 0), { P1_WH(t + 1, 1, 0); if (wsch) dw(t + 2, 2); }, P1_WL(t + 1, 1, 1), { P1_WH(t + 1, 1, 1); if (t + PFD < 16) pf(t + PFD); });
            } else {
                P1_FB8(t, 4, NOP_, NOP_, NOP_, NOP_, NOP_, NOP_, NOP_, NOP_);
                P1_FB8(t, 5, NOP_, NOP_, NOP_, NOP_, NOP_, NOP_, NOP_, NOP_);
            }
        }
        lds_signal(lds0 + L_CNT + 32);
        auto swap16 = [](unsigned& x, unsigned& y) { auto r = __builtin_amdgcn_permlane16_swap(x, y, false, false); x = r[0]; y = r[1]; };
        auto swap32 = [](unsigned& x, unsigned& y) { auto r = __builtin_amdgcn_permlane32_swap(x, y, false, false); x = r[0]; y = r[1]; };
        auto qkfrag = [&](const f32x4& t0, const f32x4& t1, float mul) {
            unsigned a0 = cvtpk(t0[0] * mul, t0[1] * mul), a1 = cvtpk(t0[2] * mul, t0[3] * mul), b0 = cvtpk(t1[0] * mul, t1[1] * mul), b1 = cvtpk(t1[2] * mul, t1[3] * mul);
            swap16(a0, b0); swap16(a1, b1);
            return (u32x4){a0, a1, b0, b1}; };
        auto vfrag = [&](const f32x4& x0, const f32x4& x1) {
            unsigned a0 = cvtpk(x0[0], x0[1]), a1 = cvtpk(x0[2], x0[3]), b0 = cvtpk(x1[0], x1[1]), b1 = cvtpk(x1[2], x1[3]);
            swap16(a0, b0); swap16(a1, b1); swap32(a0, b0); swap32(a1, b1);
            return (u32x4){a0, a1, b0, b1}; };
#pragma unroll
        for (int a = 0; a < 2; ++a)
#pragma unroll
            for (int s = 0; s < 2; ++s) {
                kvp[a][s] = qkfrag(acc[2 * a][2 + s], acc[2 * a + 1][2 + s], 1.0f); kvp[a][2 + s] = vfrag(acc[2 * a + s][4], acc[2 * a + s][5]);
                __builtin_amdgcn_raw_buffer_store_b128(kvp[a][s], skvw, lane * 16 + a * 4096 + s * 1024, 0, 16);
                __builtin_amdgcn_raw_buffer_store_b128(kvp[a][2 + s], skvw, lane * 16 + a * 4096 + 2048 + s * 1024, 0, 16);
            }
#pragma unroll
        for (int a = 0; a < 2; ++a) {
            const int tl = wm * 2 + a;
            LAS char* lp = lds + L_S0 + (tl >> 2) * 32768 + wn * 16384 + (tl & 3) * 4096 + lane * 16;
#pragma unroll
            for (int f = 0; f < 4; ++f) *(LAS u32x4*)(lp + f * 1024) = kvp[a][f];
        }
        SBAR();
#pragma unroll
        for (int a = 0; a < 2; ++a)
#pragma unroll
            for (int s = 0; s < 2; ++s) qf[a][s] = BF(qkfrag(acc[2 * a][s], acc[2 * a + 1][s], QSCALE));
    }
    asm volatile("" : "+v"(epoch));
    const u64 tag = mix64(((u64)rfl((unsigned)(epoch >> 32)) << 32) | rfl((unsigned)epoch));

    const srd_t skv = make_srd(ws + WS_KV + (size_t)(b * 32 + hp * 2) * 64 * 4096, 2 * 64 * 4096);
    const srd_t swo = make_srd(b ? woB : woA, 1024 * 1024 * 2);
    const int tm = (xcd & 3) * 4 + (j >> 3), tn = j & 7;
    const unsigned vl = lane * 16;
    const unsigned vwo = (lane >> 4) * 2048 + (((lane & 15) ^ (((lane >> 4) & 3) << 2)) << 4);
    auto chunk_off = [&](int c) { const int i = (c - 2) >> 1; const int blk = i < mb ? i : i + 1; return (unsigned)((wn * 64 + blk * 8 + (c & 1) * 4 + wm) * 4096); };
    const unsigned ldw = lds0 + wid * 4096;
#define DKV(coff, slotoff, i) dma4c<i>(ldw + (slotoff), vl, skv, coff)
    const unsigned wo0 = wm * 8 * 2048 + tn * 256;
    auto dwu = [&](int head, unsigned slotaddr, int p) { dma16(lds0 + slotaddr + wm * 2048 + p * 1024, vwo, swo, wo0 + (head * 32 + p * 4) * 2048); };
    auto wo_rs = [&](int k) { return (unsigned)(L_R0 + wn * 24576 + k * 8192); };
    auto wo_hs = [&](int k) { return (unsigned)((k >> 1 == 0 ? L_S0 : k >> 1 == 1 ? L_S1 : L_S2) + wn * 16384 + (k & 1) * 8192); };
    bf16x8 sel; int selidx;
    { const int i16 = lane & 15; const short lo = (i16 < 2) ? (short)0x3F80 : (short)0, hi = (i16 == 2 || i16 == 3) ? (short)0x3F80 : (short)0;
      s16x8 t = {lo, lo, lo, lo, hi, hi, hi, hi}; sel = __builtin_bit_cast(bf16x8, t); selidx = (i16 & 1) ? 0xEEEE : 0x4444; }
    const LAS char* kvb = lds + wn * 16384 + lane * 16;
    GAS u64* const kvfl = flags + (FL_KV / 8) + (b * 32 + h) * 32;
    const GAS u64* const kvmine = kvfl + ((lane >> 1) & 7) * 4 + (lane & 1) * 2 + (wm >> 1);
    const LAS char* cntp = lds + L_CNT + wn * 16;
    const unsigned cnta = lds0 + L_CNT + wn * 16;
    f32x16 o[2]; f32x4 lacc[2];
    {
        f32x16 negm[2], S[2];
        bf16x8 kf[2][2], vf[2][2];
        u32x8 P;
        kf[0][0] = BF(kvp[0][0]); kf[0][1] = BF(kvp[0][1]); vf[0][0] = BF(kvp[0][2]); vf[0][1] = BF(kvp[0][3]);
#pragma unroll
        for (int a = 0; a < 2; ++a) {
            f32x16 z;
#pragma unroll
            for (int i = 0; i < 16; ++i) z[i] = 0.f;
            z = MFMA32(kf[0][0], qf[a][0], z); z = MFMA32(kf[0][1], qf[a][1], z);
            float m = z[0];
#pragma unroll
            for (int i = 1; i < 16; ++i) m = fmaxf(m, z[i]);
            m = halfmax(m);
#pragma unroll
            for (int i = 0; i < 16; ++i) { negm[a][i] = -m; o[a][i] = 0.f; }
            lacc[a] = (f32x4){0.f, 0.f, 0.f, 0.f};
        }
        S[0] = MFMA32(kf[0][0], qf[0][0], negm[0]); S[0] = MFMA32(kf[0][1], qf[0][1], S[0]);
        P = (u32x8){0u, 0u, 0u, 0u, 0u, 0u, 0u, 0u};
        bf16x8 vprev1 = vf[0][1];
        unsigned seen;
#define EXPI(X, i) X[i] = ex2(X[i])
#define UNIT(SX, SN, KA0, KA1, QB0, QB1, NEG, DOQK, OPREV, LPREV, VPREV1, OCUR, VCUR0, H0, H1, H2, H3) do { \
            if (DOQK) SN = MFMA32(KA0, QB0, NEG); H0; SBAR(); \
            EXPI(SX, 0); EXPI(SX, 1); EXPI(SX, 2); SBAR(); \
            LPREV = SMF(sel, P, LPREV, selidx); SBAR(); \
            EXPI(SX, 3); P[0] = cvtpk(SX[0], SX[1]); P[1] = cvtpk(SX[2], SX[3]); SBAR(); \
            OPREV = MFMA32(VPREV1, PHI(P), OPREV); H1; SBAR(); \
            EXPI(SX, 4); EXPI(SX, 5); EXPI(SX, 6); EXPI(SX, 7); P[2] = cvtpk(SX[4], SX[5]); P[3] = cvtpk(SX[6], SX[7]); SBAR(); \
            if (DOQK) SN = MFMA32(KA1, QB1, SN); H2; SBAR(); \
            EXPI(SX, 8); EXPI(SX, 9); EXPI(SX, 10); EXPI(SX, 11); P[4] = cvtpk(SX[8], SX[9]); P[5] = cvtpk(SX[10], SX[11]); SBAR(); \
            OCUR = MFMA32(VCUR0, PLO(P), OCUR); H3; SBAR(); \
            EXPI(SX, 12); EXPI(SX, 13); EXPI(SX, 14); EXPI(SX, 15); P[6] = cvtpk(SX[12], SX[13]); P[7] = cvtpk(SX[14], SX[15]); SBAR(); \
        } while (0)
#define TILEG(cur, nxt, LK, LV, LOAD, A2, A3, B0, B1, B2, B3) do { \
            UNIT(S[0], S[1], kf[cur][0], kf[cur][1], qf[1][0], qf[1][1], negm[1], true, o[1], lacc[1], vprev1, o[0], vf[cur][0], LK, LV, A2, A3); \
            UNIT(S[1], S[0], kf[nxt][0], kf[nxt][1], qf[0][0], qf[0][1], negm[0], LOAD, o[0], lacc[0], vf[cur][1], o[1], vf[cur][0], B0, B1, B2, B3); \
            vprev1 = vf[cur][1]; \
        } while (0)
#define TILEX(cur, nxt, rdp, LOAD, A2, A3, B0, B1, B2, B3) TILEG(cur, nxt, \
            if (LOAD) { kf[nxt][0] = ldsfrag(rdp); kf[nxt][1] = ldsfrag((rdp) + 1024); }, if (LOAD) { vf[nxt][0] = ldsfrag((rdp) + 2048); vf[nxt][1] = ldsfrag((rdp) + 3072); }, \
            LOAD, A2, A3, B0, B1, B2, B3)
#define TILE(cur, nxt, rdp, PK) TILEX(cur, nxt, rdp, true, NOP_, NOP_, NOP_, PK, NOP_, NOP_)
#define TILED(cur, nxt, rdp, coff, slot) TILEX(cur, nxt, rdp, true, DKV(coff, slot, 0), DKV(coff, slot, 1), NOP_, NOP_, DKV(coff, slot, 2), DKV(coff, slot, 3))
#define PEEK seen = lds_peek(cntp)
#define SIGNAL() lds_signal(cnta)
#define BALANCE(m) do { if (wn == 0 && (m) == FLIP) __builtin_amdgcn_s_setprio(0); } while (0)
        if (wn == 0) __builtin_amdgcn_s_setprio(2); else __builtin_amdgcn_s_setprio(1);
        auto own = [&](int p) {
            const int lo = wm < 2;
            const int off = lo ? (p < 4 ? L_S0 + ((1 - wm) * 2 + p - 2) * 4096 : L_S1 + (p - 4) * 4096) : (p < 6 ? L_S0 + (p - 2) * 4096 : L_S1 + ((3 - wm) * 2 + p - 6) * 4096);
            return kvb + off; };
        TILEG(0, 1, { kf[1][0] = BF(kvp[1][0]); kf[1][1] = BF(kvp[1][1]); }, { vf[1][0] = BF(kvp[1][2]); vf[1][1] = BF(kvp[1][3]); }, true, NOP_, NOP_, NOP_, NOP_, NOP_, NOP_);
        lds_signal(cnta + 4); lds_await(0, cntp + 4, 4);
        TILE(1, 0, own(2), NOP_);
        TILE(0, 1, own(3), NOP_);
        WAITV(0); if (lane == 0) st_agent(kvfl + mb * 4 + wm, tag);
        TILE(1, 0, own(4), NOP_);
        u64 pollv = ld_agent(kvmine);
        TILE(0, 1, own(5), NOP_);
        if (!__all(pollv == tag)) spin_flags(kvmine, tag);
        SIGNAL();
        lds_await(0, lds + L_CNT + 32, 8);
        unsigned cn = chunk_off(2);
        TILEX(1, 0, own(6), true, DKV(cn, L_S2, 0), DKV(cn, L_S2, 1), NOP_, PEEK, DKV(cn, L_S2, 2), DKV(cn, L_S2, 3));
        lds_await(seen, cntp, 4);
        cn = chunk_off(3);
        TILED(0, 1, own(7), cn, L_S0);
        WAITV(4); SIGNAL(); lds_await(0, cntp, 8);
        TILE(1, 0, kvb + L_S2, NOP_);
        cn = chunk_off(4);
        TILED(0, 1, kvb + L_S2 + 4096, cn, L_S1);
        WAITV(4); SIGNAL();
        TILE(1, 0, kvb + L_S2 + 8192, NOP_);
        TILE(0, 1, kvb + L_S2 + 12288, PEEK);
        lds_await(seen, cntp, 12);
        WAITV(0); SIGNAL();
        TILE(1, 0, kvb + L_S0, PEEK);
        int sc = L_S0, sn = L_S1, sp = L_S2;
        for (int m = 3; m <= 13; ++m) {
            const LAS char* cp = kvb + sc;
            cn = chunk_off(m + 2);
            lds_await(seen, cntp, 4 * (m + 1));
            BALANCE(4 * m);
            TILED(0, 1, cp + 4096, cn, sp);
            TILE(1, 0, cp + 8192, NOP_);
            TILE(0, 1, cp + 12288, NOP_);
            WAITV(0); SIGNAL();
            TILE(1, 0, kvb + sn, PEEK);
            const int t_ = sc; sc = sn; sn = sp; sp = t_;
        }
        lds_await(seen, cntp, 60);
        BALANCE(56);
        TILEX(0, 1, kvb + L_S2 + 4096, true, dwu(wn, wo_rs(0), 0), dwu(wn, wo_rs(0), 1), NOP_, NOP_, NOP_, NOP_);
        BALANCE(57);
        TILEX(1, 0, kvb + L_S2 + 8192, true, dwu(2 + wn, wo_rs(1), 0), dwu(2 + wn, wo_rs(1), 1), NOP_, NOP_, NOP_, NOP_);
        BALANCE(58);
        TILEX(0, 1, kvb + L_S2 + 12288, true, dwu(4 + wn, wo_rs(2), 0), dwu(4 + wn, wo_rs(2), 1), NOP_, NOP_, NOP_, NOP_);
        BALANCE(59);
        TILE(1, 0, kvb + L_S0, NOP_);
        BALANCE(60);
        TILE(0, 1, kvb + L_S0 + 4096, NOP_);
        BALANCE(61);
        TILE(1, 0, kvb + L_S0 + 8192, NOP_);
        BALANCE(62);
        TILE(0, 1, kvb + L_S0 + 12288, NOP_);
        TILEX(1, 0, kvb, false, NOP_, NOP_, NOP_, NOP_, NOP_, NOP_);
        lacc[1] = SMF(sel, P, lacc[1], selidx); o[1] = MFMA32(vprev1, PHI(P), o[1]);
    }
    if (wn == 0) __builtin_amdgcn_s_setprio(0);
    {
        float l[2];
        auto rowsum = [&](int a) { const float t0 = __shfl(lacc[a][0] + lacc[a][1], lane & 15), t1 = __shfl(lacc[a][2] + lacc[a][3], lane & 15); return (lane & 16) ? t1 : t0; };
        bool bad = false;
#pragma unroll
        for (int a = 0; a < 2; ++a) {
            l[a] = rowsum(a);
            float z = l[a] * 0.f, z1 = 0.f;
#pragma unroll
            for (int i = 0; i < 16; i += 2) { z = __builtin_fmaf(o[a][i], 0.f, z); z1 = __builtin_fmaf(o[a][i + 1], 0.f, z1); }
            bad |= !(l[a] < 1.0e36f) || !(z + z1 == 0.f);
        }
        if (__any(bad)) {
            spin_flags(kvfl + (lane & 31), tag);
            const srd_t sk1 = make_srd(ws + WS_KV + (size_t)(b * 32 + h) * 64 * 4096, 64 * 4096);
#define LDKV(t, f) BF(__builtin_amdgcn_raw_buffer_load_b128(sk1, lane * 16 + (f) * 1024, (t) * 4096, 16))
            f32x16 negm[2];
#pragma unroll
            for (int a = 0; a < 2; ++a) {
                float m = -__builtin_inff();
                for (int t = 0; t < 64; ++t) {
                    const bf16x8 k0 = LDKV(t, 0), k1 = LDKV(t, 1);
                    f32x16 z;
#pragma unroll
                    for (int i = 0; i < 16; ++i) z[i] = 0.f;
                    z = MFMA32(k0, qf[a][0], z); z = MFMA32(k1, qf[a][1], z);
#pragma unroll
                    for (int i = 0; i < 16; ++i) m = fmaxf(m, z[i]);
                }
                m = halfmax(m);
#pragma unroll
                for (int i = 0; i < 16; ++i) { negm[a][i] = -m; o[a][i] = 0.f; }
                lacc[a] = (f32x4){0.f, 0.f, 0.f, 0.f};
            }
            for (int t = 0; t < 64; ++t) {
                const bf16x8 k0 = LDKV(t, 0), k1 = LDKV(t, 1), v0 = LDKV(t, 2), v1 = LDKV(t, 3);
#pragma unroll
                for (int a = 0; a < 2; ++a) {
                    f32x16 z = MFMA32(k0, qf[a][0], negm[a]); z = MFMA32(k1, qf[a][1], z);
                    u32x8 P;
#pragma unroll
                    for (int i = 0; i < 8; ++i) P[i] = cvtpk(ex2(z[2 * i]), ex2(z[2 * i + 1]));
                    o[a] = MFMA32(v0, PLO(P), o[a]); o[a] = MFMA32(v1, PHI(P), o[a]); lacc[a] = SMF(sel, P, lacc[a], selidx);
                }
            }
            l[0] = rowsum(0); l[1] = rowsum(1);
        }
        const srd_t sow = make_srd(ws + WS_O + (size_t)(b * 64 + mb * 8 + wm * 2) * 65536, 2 * 65536);
#pragma unroll
        for (int a = 0; a < 2; ++a) {
            const float rl = __builtin_amdgcn_rcpf(l[a]);
            __builtin_amdgcn_raw_buffer_store_b128(pack8(o[a], 0, rl), sow, lane * 16 + a * 65536 + h * 2048, 0, 16);
            __builtin_amdgcn_raw_buffer_store_b128(pack8(o[a], 1, rl), sow, lane * 16 + a * 65536 + h * 2048 + 1024, 0, 16);
        }
    }
    GAS u64* const ofl = flags + (FL_O / 8) + b * 1024;
    WAITV(0); if (lane == 0) st_agent(ofl + (mb * 4 + wm) * 32 + h, tag);

    {
        const int wt = wm;
        unsigned seen;
        SIGNAL(); lds_await(0, cntp, 64);
        for (int i = 0; i < 6; ++i) { dwu(2 * (3 + i) + wn, wo_hs(i), 0); dwu(2 * (3 + i) + wn, wo_hs(i), 1); }
        spin_flags(ofl + (tm * 2 + (wt >> 1)) * 32 + (lane & 15) * 2 + wn, tag);
        const srd_t sor = make_srd(ws + WS_O + (size_t)(b * 64 + tm * 4 + wt) * 65536 + wn * 2048, 65536 - 2048);
        bf16x8 of[8][2], wf[3][4];
#define P3_LO(u) do { of[(u) & 7][0] = BF(__builtin_amdgcn_raw_buffer_load_b128(sor, lane * 16, (u) * 4096, 16)); \
                      of[(u) & 7][1] = BF(__builtin_amdgcn_raw_buffer_load_b128(sor, lane * 16, (u) * 4096 + 1024, 16)); } while (0)
        P3_LO(0); P3_LO(1); P3_LO(2); P3_LO(3); P3_LO(4); P3_LO(5); P3_LO(6); P3_LO(7);
        SIGNAL();
        unsigned wn3[4];
#pragma unroll
        for (int n = 0; n < 4; ++n) wn3[n] = (4 * (g4 >> 1) + q4) * 256 + ((n ^ q4) << 6) + (g4 & 1) * 32 + p4 * 8;
        f32x16 acc[4];
#pragma unroll
        for (int n = 0; n < 4; ++n)
#pragma unroll
            for (int i = 0; i < 16; ++i) acc[n][i] = 0.f;
        auto uslot = [&](int u) { return u < 3 ? wo_rs(u) : u < 9 ? wo_hs(u - 3) : u < 12 ? wo_rs(u - 9) : wo_hs(u - 12); };
#define P3_RW(kk, n) do { const LAS char* w_ = lds + uslot((kk) >> 1) + ((kk) & 1) * 4096 + wn3[n]; wf[(kk) % 3][n] = cat8(ldstr(w_), ldstr(w_ + 2048)); } while (0)
#define P3_KS(kk, M0, M1) do { \
            acc[0] = MFMA32(wf[(kk) % 3][0], of[((kk) >> 1) & 7][(kk) & 1], acc[0]); if ((kk) + 2 < 32) P3_RW((kk) + 2, 0); SBAR(); \
            acc[1] = MFMA32(wf[(kk) % 3][1], of[((kk) >> 1) & 7][(kk) & 1], acc[1]); if ((kk) + 2 < 32) P3_RW((kk) + 2, 1); M0; SBAR(); \
            acc[2] = MFMA32(wf[(kk) % 3][2], of[((kk) >> 1) & 7][(kk) & 1], acc[2]); if ((kk) + 2 < 32) P3_RW((kk) + 2, 2); SBAR(); \
            acc[3] = MFMA32(wf[(kk) % 3][3], of[((kk) >> 1) & 7][(kk) & 1], acc[3]); if ((kk) + 2 < 32) P3_RW((kk) + 2, 3); M1; SBAR(); \
        } while (0)
#define P3_UNIT(u, M0) do { P3_KS(2 * (u), M0, NOP_); P3_KS(2 * (u) + 1, NOP_, if ((u) + 8 < 16) P3_LO((u) + 8)); } while (0)
#define DWU(u, slot) do { dwu(2 * (u) + wn, slot, 0); dwu(2 * (u) + wn, slot, 1); } while (0)
        P3_RW(0, 0); P3_RW(0, 1); P3_RW(0, 2); P3_RW(0, 3); P3_RW(1, 0); P3_RW(1, 1); P3_RW(1, 2); P3_RW(1, 3); SBAR();
        P3_UNIT(0, NOP_);
        P3_UNIT(1, PEEK);
        lds_await(seen, cntp, 68);
        SIGNAL();
        P3_UNIT(2, PEEK);
        lds_await(seen, cntp, 72); DWU(9, wo_rs(0)); DWU(10, wo_rs(1)); DWU(11, wo_rs(2));
        P3_UNIT(3, NOP_);
        P3_UNIT(4, NOP_);
        SIGNAL();
        P3_UNIT(5, PEEK);
        lds_await(seen, cntp, 76); DWU(12, wo_hs(0)); DWU(13, wo_hs(1)); DWU(14, wo_hs(2));
        P3_UNIT(6, NOP_);
        WAITV(14); SIGNAL();
        P3_UNIT(7, PEEK);
        lds_await(seen, cntp, 80);
        SIGNAL();
        P3_UNIT(8, PEEK);
        lds_await(seen, cntp, 84); DWU(15, wo_hs(3));
        P3_UNIT(9, NOP_);
        WAITV(6); SIGNAL();
        P3_UNIT(10, PEEK);
        lds_await(seen, cntp, 88);
        P3_UNIT(11, NOP_);
        P3_UNIT(12, NOP_);
        WAITV(0); SIGNAL();
        P3_UNIT(13, PEEK);
        lds_await(seen, cntp, 92);
        P3_UNIT(14, NOP_);
        P3_UNIT(15, NOP_);
        const int xoff = (wm < 3 ? (wm == 0 ? L_S0 : wm == 1 ? L_S1 : L_S2) : L_R0) + lane * 16;
        if (wn == 0) {
            SIGNAL(); lds_await(0, cntp, 96);
#pragma unroll
            for (int n = 0; n < 4; ++n)
#pragma unroll
                for (int qd = 0; qd < 4; ++qd)
                    *(LAS f32x4*)(lds + xoff + n * 4096 + qd * 1024) = (f32x4){acc[n][4 * qd], acc[n][4 * qd + 1], acc[n][4 * qd + 2], acc[n][4 * qd + 3]};
            SIGNAL();
        } else {
            lds_await(0, lds + L_CNT, 100);
            if (tid == 256 && (xcd & 3) == 0 && j == 0) st_agent(flags + b * 32, tag);
            const srd_t sout = make_srd(out, 2048 * 2048 * 2);
            LAS char* stg = lds + (wm < 3 ? wo_rs(wm) : wo_hs(0));
#pragma unroll
            for (int n = 0; n < 4; ++n) {
                f32x16 v = acc[n];
#pragma unroll
                for (int qd = 0; qd < 4; ++qd) {
                    const f32x4 t = *(const LAS f32x4*)(lds + xoff + n * 4096 + qd * 1024);
                    v[4 * qd] += t[0]; v[4 * qd + 1] += t[1]; v[4 * qd + 2] += t[2]; v[4 * qd + 3] += t[3];
                }
#pragma unroll
                for (int gp = 0; gp < 2; ++gp) {
                    const unsigned a0 = cvtpk(v[8 * gp], v[8 * gp + 1]), a1 = cvtpk(v[8 * gp + 2], v[8 * gp + 3]);
                    const unsigned b0 = cvtpk(v[8 * gp + 4], v[8 * gp + 5]), b1 = cvtpk(v[8 * gp + 6], v[8 * gp + 7]);
                    auto r0 = __builtin_amdgcn_permlane32_swap(a0, b0, false, false);
                    auto r1 = __builtin_amdgcn_permlane32_swap(a1, b1, false, false);
                    const u32x4 w = {r0[0], r1[0], r0[1], r1[1]};
                    *(LAS u32x4*)(stg + r32 * 256 + (((n * 4 + gp * 2 + hh) ^ (r32 & 15)) << 4)) = w;
                }
            }
            const unsigned oo = (unsigned)(tm * 128 + wt * 32 + g4) * 4096u + (unsigned)(b * 1024 + tn * 128) * 2u + (lane & 15) * 16;
#pragma unroll
            for (int i = 0; i < 8; ++i) {
                const int row = 4 * i + g4;
                const u32x4 w = *(const LAS u32x4*)(stg + row * 256 + (((lane & 15) ^ (row & 15)) << 4));
                __builtin_amdgcn_raw_buffer_store_b128(w, sout, oo + i * 4 * 4096, 0, 16);
            }
        }
    }
}

extern "C" void kernel_launch(void* const* d_in, const int* in_sizes, int n_in, void* d_out, int out_size, void* d_ws, size_t ws_size, hipStream_t stream) {
    (void)hipFuncSetAttribute((const void*)mha_fused, hipFuncAttributeMaxDynamicSharedMemorySize, L_END);
    mha_fused<<<256, 512, L_END, stream>>>((const uint16_t*)d_in[0], (const uint16_t*)d_in[1], (const uint16_t*)d_in[2], (const uint16_t*)d_in[3], (const uint16_t*)d_in[4],
                                           (const uint16_t*)d_in[5], (char*)d_ws, (uint16_t*)d_out);
}
```
